```python
import torch

def _softmax(x, axis=-1):
    return torch.softmax(x, dim=axis)
def _gelu(x):
    return torch.nn.functional.gelu(x, approximate="tanh")
def _silu(x):
    return torch.nn.functional.silu(x)
def _sigmoid(x):
    return torch.sigmoid(x)
def _layernorm(x, axis=-1, eps=1e-5):
    m = x.mean(dim=axis, keepdim=True)
    v = x.var(dim=axis, keepdim=True, unbiased=False)
    return (x - m) / torch.sqrt(v + eps)
def _scatter_add(tgt, idx, src, axis=0):
    return tgt.clone().index_add_(axis, idx, src)
def _segment_sum(src, seg, n):
    out = torch.zeros((n,) + src.shape[1:], dtype=src.dtype)
    return out.index_add_(0, seg, src)
def _pad(x, pw):
    flat = tuple(v for pair in reversed(pw) for v in pair)
    return torch.nn.functional.pad(x, flat)

kernel_name = 'sequential_preLN__ssm_conv_s4__mixer_s_mlp__prompt'

def setup_inputs():

    _w = (lambda a: (lambda t: t.to(torch.bfloat16) if t.is_floating_point() else t)(torch.as_tensor(a)))
    def _A_setup():
        B, L, D, N = 1, 2048, 576, 128
        A_re = -(lambda _x: torch.exp(torch.as_tensor(_x)))((lambda *s: torch.randn(*s, dtype=torch.bfloat16) * 3.2)(D, N))
        A_im = torch.pi * (lambda *s: torch.randn(*s, dtype=torch.bfloat16) * 0.05)(D, N)
        B_re = (lambda *s: torch.randn(*s, dtype=torch.bfloat16) * 0.1)(D, N)
        B_im = (lambda *s: torch.randn(*s, dtype=torch.bfloat16) * 0.1)(D, N)
        C_re = (lambda *s: torch.randn(*s, dtype=torch.bfloat16) * 0.1)(D, N)
        C_im = (lambda *s: torch.randn(*s, dtype=torch.bfloat16) * 0.1)(D, N)
        log_dt = (lambda *s: torch.randn(*s, dtype=torch.bfloat16) * 0.05)(D) * 0.5 - 4.0
        Dskip = (lambda *s: torch.randn(*s, dtype=torch.bfloat16) * 0.05)(D)
        return {
            "u": _w((lambda *s: torch.randn(*s, dtype=torch.bfloat16) * 0.05)(B, L, D)),
            "A_re": _w(A_re),
            "A_im": _w(A_im),
            "B_re": _w(B_re),
            "B_im": _w(B_im),
            "C_re": _w(C_re),
            "C_im": _w(C_im),
            "log_dt": _w(log_dt),
            "Dskip": _w(Dskip),
        }
    _A_in = _A_setup()

    def _B_setup():
        B, S, D, F = 1, 2048, 576, 4608
        return {
            "x":  _w((lambda *s: torch.randn(*s, dtype=torch.bfloat16) * 0.05)(B, S, D)),
            "w1": _w((lambda *s: torch.randn(*s, dtype=torch.bfloat16) * 3.2)(D, D) * (D ** -0.5)),
            "w2": _w((lambda *s: torch.randn(*s, dtype=torch.bfloat16) * 3.2)(D, D) * (D ** -0.5)),
            "w3": _w((lambda *s: torch.randn(*s, dtype=torch.bfloat16) * 3.2)(D, F) * (D ** -0.5)),
            "w4": _w((lambda *s: torch.randn(*s, dtype=torch.bfloat16) * 1.6)(F, D) * (F ** -0.5)),
        }
    _B_in = _B_setup()

    return {
        "A_u": _A_in["u"],
        "A_A_re": _A_in["A_re"],
        "A_A_im": _A_in["A_im"],
        "A_B_re": _A_in["B_re"],
        "A_B_im": _A_in["B_im"],
        "A_C_re": _A_in["C_re"],
        "A_C_im": _A_in["C_im"],
        "A_log_dt": _A_in["log_dt"],
        "A_Dskip": _A_in["Dskip"],
        "B_x": _B_in["x"],
        "B_w1": _B_in["w1"],
        "B_w2": _B_in["w2"],
        "B_w3": _B_in["w3"],
        "B_w4": _B_in["w4"],
    }

def reference(A_u, A_A_re, A_A_im, A_B_re, A_B_im, A_C_re, A_C_im, A_log_dt, A_Dskip, B_x, B_w1, B_w2, B_w3, B_w4):
    def _A_ref(u, A_re, A_im, B_re, B_im, C_re, C_im, log_dt, Dskip):
        Bsz, L, D = u.shape
        dt = torch.exp(log_dt)[:, None]
        A = A_re + 1j * A_im
        Bc = B_re + 1j * B_im
        Cc = C_re + 1j * C_im
        logdA = dt * A
        dB = dt * Bc
        l = (lambda x, dt: x.to(dt))(torch.arange(L), A_re.dtype)
        P = torch.exp(logdA[:, :, None] * l[None, None, :])
        K = torch.einsum("dn,dnl,dn->dl", Cc, P, dB).real
        L2 = 2 * L
        Kp = _pad(K, ((0, 0), (0, L)))
        up = (lambda x, *d: x.permute(*d))(u, 0, 2, 1)
        up = _pad(up, ((0, 0), (0, 0), (0, L)))
        Kf = (lambda x, axes=(-2,-1): torch.fft.rfft2(x.float(), dim=axes))(Kp[:, None, :], axes=(-2, -1))
        Uf = (lambda x, axes=(-2,-1): torch.fft.rfft2(x.float(), dim=axes))(up[:, :, None, :], axes=(-2, -1))
        Yf = Uf * Kf[None, :, :, :]
        y = (lambda x, s, axes=(-2,-1): torch.fft.irfft2(x, s=s, dim=axes).to(torch.bfloat16))(Yf, (1, L2), axes=(-2, -1))[:, :, 0, :L]
        y = (lambda x, *d: x.permute(*d))(y, 0, 2, 1) + u * Dskip[None, None, :]
        return y

    def _B_ref(x, w1, w2, w3, w4):
        B, S, D = x.shape
        h = _gelu(_layernorm(x, axis=-1) @ w1)
        c = D // 4
        g0 = _pad(h[:, :, 0*c:1*c], ((0, 0), (1, 0), (0, 0)))[:, :S, :]
        g1 = _pad(h[:, :, 1*c:2*c], ((0, 0), (0, 1), (0, 0)))[:, 1:, :]
        g2 = _pad(h[:, :, 2*c:3*c], ((0, 0), (2, 0), (0, 0)))[:, :S, :]
        g3 = _pad(h[:, :, 3*c:   ], ((0, 0), (0, 2), (0, 0)))[:, 2:, :]
        hs = torch.concatenate([g0, g1, g2, g3], axis=-1) @ w2
        x1 = x + hs
        h2 = _gelu(_layernorm(x1, axis=-1) @ w3) @ w4
        return x1 + h2

    x = A_u
    h = x + _A_ref(_layernorm(x, axis=-1), A_A_re, A_A_im, A_B_re, A_B_im, A_C_re, A_C_im, A_log_dt, A_Dskip)
    h2 = h + _B_ref(_layernorm(h, axis=-1), B_w1, B_w2, B_w3, B_w4)
    return h2
```

```cpp
#include <hip/hip_runtime.h>
#include <hip/hip_bf16.h>
#include <stdint.h>

typedef unsigned long long u64;
typedef unsigned v4u __attribute__((ext_vector_type(4)));
typedef unsigned v2u __attribute__((ext_vector_type(2)));
typedef float f32x4 __attribute__((ext_vector_type(4)));
typedef float f32x2 __attribute__((ext_vector_type(2)));
typedef short s16x8 __attribute__((ext_vector_type(8)));
typedef short s16x4 __attribute__((ext_vector_type(4)));
typedef _Float16 h16x8 __attribute__((ext_vector_type(8)));
typedef _Float16 h16x2 __attribute__((ext_vector_type(2)));
typedef __bf16 bf16x2_t __attribute__((ext_vector_type(2)));
#define GAS __attribute__((address_space(1)))
#define LAS __attribute__((address_space(3)))
#define DI __device__ __forceinline__

constexpr int L = 2048, D = 576, NS = 128, F = 4608;

constexpr size_t WS_W1T = 0;
constexpr size_t WS_W2T = WS_W1T + 36 * 18 * 1024;
constexpr size_t WS_W3T = WS_W2T + 36 * 18 * 1024;
constexpr size_t WS_W4T = WS_W3T + 288 * 18 * 1024;
constexpr size_t WS_CS1 = WS_W4T + 36 * 144 * 1024;
constexpr size_t WS_CS3 = WS_CS1 + 4096;
constexpr size_t WS_ST1 = WS_CS3 + 4608 * 4;
constexpr int KROW = 2080;
constexpr size_t WS_KREV = WS_ST1 + 2048 * 8;
constexpr size_t WS_KSC = WS_KREV + (size_t)576 * KROW * 2;
constexpr size_t WS_HT = WS_KSC + 4096;
constexpr size_t WS_H1 = WS_HT + (size_t)D * L * 2;
constexpr size_t WS_ST2 = WS_H1 + (size_t)D * L * 2;
constexpr size_t WS_X1 = WS_ST2 + 2048 * 8;
constexpr size_t WS_HR = WS_X1 + (size_t)D * L * 2;
constexpr size_t WS_PART = WS_HR + (size_t)D * L * 2;
constexpr size_t WS_END = WS_PART + (size_t)8 * D * L * 2;

DI float bf2f(unsigned short b) { return __builtin_bit_cast(float, (unsigned)b << 16); }
DI float bflo(unsigned u) { return __builtin_bit_cast(float, u << 16); }
DI float bfhi(unsigned u) { return __builtin_bit_cast(float, u & 0xFFFF0000u); }
DI unsigned cvtpk(float lo, float hi) {
  f32x2 v = {lo, hi};
  bf16x2_t b = __builtin_convertvector(v, bf16x2_t);
  return __builtin_bit_cast(unsigned, b);
}
DI float bfr(float x) { return bflo(cvtpk(x, x)); }
DI unsigned cvtpkh(float lo, float hi) {
  f32x2 v = {lo, hi};
  h16x2 b = __builtin_convertvector(v, h16x2);
  return __builtin_bit_cast(unsigned, b);
}
DI f32x4 mfma_bf(v4u a, v4u b, f32x4 c) {
  return __builtin_amdgcn_mfma_f32_16x16x32_bf16(__builtin_bit_cast(s16x8, a), __builtin_bit_cast(s16x8, b), c, 0, 0, 0);
}
typedef __bf16 bf16x8_t __attribute__((ext_vector_type(8)));
DI f32x4 mfma_h(v4u a, v4u b, f32x4 c) {
  return __builtin_amdgcn_mfma_f32_16x16x32_f16(__builtin_bit_cast(h16x8, a), __builtin_bit_cast(h16x8, b), c, 0, 0, 0);
}
DI float dot2bf(unsigned a, unsigned b, float acc) {
  return __builtin_amdgcn_fdot2_f32_bf16(__builtin_bit_cast(bf16x2_t, a), __builtin_bit_cast(bf16x2_t, b), acc, false);
}
DI v2u tr_read(LAS unsigned char* p) {
  typedef short v4i16_t __attribute__((ext_vector_type(4)));
  return __builtin_bit_cast(v2u, __builtin_amdgcn_ds_read_tr16_b64_v4i16((LAS v4i16_t*)p));
}
DI float gelu_f(float x) {
  const float c0 = -2.3022081986f, c1 = -0.10294324f;
  float x2 = x * x;
  float p = __builtin_fmaf(x2, c1, c0);
  float e = __builtin_amdgcn_exp2f(x * p);
  return x * __builtin_amdgcn_rcpf(1.0f + e);
}
DI void ln_scalars(float sum, float sumsq, float& m, float& s) {
  float mean = sum * (1.0f / D);
  float var = __builtin_fmaf(-mean, mean, sumsq * (1.0f / D));
  var = var < 0.f ? 0.f : var;
  m = bfr(mean);
  s = bfr(__builtin_sqrtf(bfr(bfr(var) + 1e-5f)));
}

struct KIdx { unsigned short v[2080]; };
constexpr KIdx make_kidx() {
  KIdx t{};
  for (int y = 0; y < 2080; ++y) {
    if (y >= 2048) { t.v[y] = 767; continue; }
    int j = 2047 - y;
    int v = j;
    if (j >= 256) {
      int sh = j >= 1024 ? 3 : (j >= 512 ? 2 : 1);
      int step = 1 << sh, lo = (j >> sh) << sh, rem = j - lo, half = step >> 1;
      if (rem > half || (rem == half && (((lo >> sh) & 1) != 0))) lo += step;
      v = lo;
    }
    int seg = v >= 1024 ? 3 : (v >= 512 ? 2 : (v >= 256 ? 1 : 0));
    int u = seg == 0 ? v : v - (128 << seg);
    int rb = seg == 0 ? 0 : (seg == 1 ? 16 : (seg == 2 ? 24 : 32));
    int row = rb + ((u & 7) >> seg) + (u >> 7) * (8 >> seg);
    int col = (u >> 3) & 15;
    t.v[y] = (unsigned short)(row * 16 + col);
  }
  return t;
}
__device__ const KIdx kIdxTab = make_kidx();

struct cplx { float r, i; };
DI cplx cmul(cplx a, cplx b) { return cplx{__builtin_fmaf(a.r, b.r, -a.i * b.i), __builtin_fmaf(a.r, b.i, a.i * b.r)}; }
DI cplx cexp_r(float a2, float th, float r) {
  float m = __builtin_amdgcn_exp2f(a2 * r);
  float ang = __builtin_amdgcn_fractf(th * r);
  return cplx{m * __builtin_amdgcn_cosf(ang), m * __builtin_amdgcn_sinf(ang)};
}

constexpr int K1_NPT = 162, K1_NST = 64, K1_NKG = 288;
constexpr int K1_LDS = 66560;

DI void pretile_unit(LAS unsigned char* lds, const unsigned short* __restrict__ w, int ldw, int row0, int col0, unsigned char* __restrict__ dst,
                     int ct0, int ks_stride, int ks0, float* __restrict__ colsum) {
  const int tid = threadIdx.x, lane = tid & 63, wv = tid >> 6;
  float cs = 0.f;
  const unsigned ones = 0x3F803F80u;
  for (int chunk = 0; chunk < 3; ++chunk) {
    v4u a[6];
#pragma unroll
    for (int i = 0; i < 6; ++i) {
      int p = tid + 256 * i, r = p >> 3, c = p & 7;
      a[i] = *(const GAS v4u*)(w + (size_t)(row0 + chunk * 192 + r) * ldw + col0 + c * 8);
    }
    if (chunk) __syncthreads();
#pragma unroll
    for (int i = 0; i < 6; ++i) {
      int p = tid + 256 * i, r = p >> 3, c = p & 7;
      int f = ((r >> 1) & 1) | (((r >> 3) & 1) << 1);
      *(LAS v4u*)(lds + r * 128 + ((((c >> 1) ^ f) << 1 | (c & 1)) << 4)) = a[i];
    }
    __syncthreads();
    const int g = lane >> 4, q = (lane & 15) >> 2, p = lane & 3;
#pragma unroll
    for (int ks = 0; ks < 6; ++ks) {
      v2u lo, hi;
      {
        int r = 32 * ks + 8 * g + q;
        int f = ((r >> 1) & 1) | (((r >> 3) & 1) << 1);
        lo = tr_read(lds + r * 128 + ((wv ^ f) << 5) + p * 8);
        int r2 = r + 4;
        int f2 = ((r2 >> 1) & 1) | (((r2 >> 3) & 1) << 1);
        hi = tr_read(lds + r2 * 128 + ((wv ^ f2) << 5) + p * 8);
      }
      v4u o = {lo.x, lo.y, hi.x, hi.y};
      *(GAS v4u*)(dst + ((size_t)(ct0 + wv) * ks_stride + ks0 + chunk * 6 + ks) * 1024 + lane * 16) = o;
      if (colsum) { cs = dot2bf(o.x, ones, cs); cs = dot2bf(o.y, ones, cs); cs = dot2bf(o.z, ones, cs); cs = dot2bf(o.w, ones, cs); }
    }
  }
  if (colsum) {
    cs += __shfl_xor(cs, 16);
    cs += __shfl_xor(cs, 32);
    if (lane < 16) colsum[wv * 16 + lane] = cs;
  }
}

__global__ __launch_bounds__(256) void k_prep(const unsigned short* __restrict__ x, const unsigned short* __restrict__ Are, const unsigned short* __restrict__ Aim,
                                              const unsigned short* __restrict__ Bre, const unsigned short* __restrict__ Bim, const unsigned short* __restrict__ Cre,
                                              const unsigned short* __restrict__ Cim, const unsigned short* __restrict__ logdt,
                                              const unsigned short* __restrict__ w1, const unsigned short* __restrict__ w2, const unsigned short* __restrict__ w3,
                                              const unsigned short* __restrict__ w4, unsigned char* __restrict__ ws) {
  extern __shared__ __attribute__((aligned(16))) unsigned char smem[];
  LAS unsigned char* lds = (LAS unsigned char*)smem;
  const int tid = threadIdx.x, lane = tid & 63, wv = tid >> 6;
  int b = blockIdx.x;
  if (b < K1_NPT) {
    if (b < 72) pretile_unit(lds, w3, F, 0, b * 64, ws + WS_W3T, b * 4, 18, 0, (float*)(ws + WS_CS3) + b * 64);
    else if (b < 144) { int u = b - 72, cg = u % 9, kc = u / 9; pretile_unit(lds, w4, D, kc * 576, cg * 64, ws + WS_W4T, cg * 4, 144, kc * 18, nullptr); }
    else if (b < 153) { int cg = b - 144; pretile_unit(lds, w1, D, 0, cg * 64, ws + WS_W1T, cg * 4, 18, 0, (float*)(ws + WS_CS1) + cg * 64); }
    else { int cg = b - 153; pretile_unit(lds, w2, D, 0, cg * 64, ws + WS_W2T, cg * 4, 18, 0, nullptr); }
    return;
  }
  b -= K1_NPT;
  if (b < K1_NST) {
    const int tok = b * 32 + wv * 8 + (lane >> 3), sub = lane & 7;
    const GAS v4u* row = (const GAS v4u*)(x + (size_t)tok * D);
    v4u a[9];
#pragma unroll
    for (int i = 0; i < 9; ++i) a[i] = row[sub + 8 * i];
    float s = 0.f, ss = 0.f;
    const unsigned ones = 0x3F803F80u;
#pragma unroll
    for (int i = 0; i < 9; ++i) {
#pragma unroll
      for (int j = 0; j < 4; ++j) { s = dot2bf(a[i][j], ones, s); ss = dot2bf(a[i][j], a[i][j], ss); }
    }
#pragma unroll
    for (int o = 1; o < 8; o <<= 1) { s += __shfl_xor(s, o); ss += __shfl_xor(ss, o); }
    float m, sd;
    ln_scalars(s, ss, m, sd);
    if (sub == 0) *(f32x2*)(ws + WS_ST1 + tok * 8) = f32x2{m, 1.0f / sd};
    return;
  }
  b -= K1_NST;
  {
    const int ch = 2 * b + (wv >> 1), half = wv & 1, n = half * 64 + lane;
    const int gi = ch * NS + n;
    const float dt = bfr(expf(bf2f(logdt[ch])));
    const float zr = dt * bf2f(Are[gi]), zi = dt * bf2f(Aim[gi]);
    const float dBr = dt * bf2f(Bre[gi]), dBi = dt * bf2f(Bim[gi]);
    const float Cr = bf2f(Cre[gi]), Ci = bf2f(Cim[gi]);
    cplx W = cmul(cplx{Cr, Ci}, cplx{dBr, dBi});
    float smax = fmaxf(fabsf(W.r), fabsf(W.i));
#pragma unroll
    for (int o = 1; o < 64; o <<= 1) smax = fmaxf(smax, __shfl_xor(smax, o));
    const float inv = smax > 0.f ? 1.0f / smax : 0.f;
    W.r *= inv; W.i *= inv;
    const float a2 = zr * 1.44269504089f, th = zi * 0.15915494309f;
    const cplx e1 = cexp_r(a2, th, 1.f), e8 = cexp_r(a2, th, 8.f), e128 = cexp_r(a2, th, 128.f);
    const cplx e256 = cexp_r(a2, th, 256.f), e512 = cexp_r(a2, th, 512.f), e1024 = cexp_r(a2, th, 1024.f);
    LAS unsigned char* tab = lds + wv * 16384;
    const int nsw = lane >> 2, nlo = (lane & 3) * 4;
#define PUT(row, val) *(LAS unsigned*)(tab + (row) * 256 + ((nsw ^ ((row) & 15)) << 4) + nlo) = (val)
#define PUTA(row, z) PUT(row, cvtpkh((z).r, -(z).i))
#define PUTB(row, z) PUT(48 + (row), cvtpkh((z).r, (z).i))
    {
      cplx c = {1.f, 0.f};
      PUTB(0, c);
      c = e8;
#pragma unroll
      for (int j = 1; j < 16; ++j) { PUTB(j, c); if (j < 15) c = cmul(c, e8); }
    }
    {
      cplx p = W;
      cplx s0, s1;
#pragma unroll
      for (int c = 0; c < 8; ++c) {
        cplx qv = cmul(p, e128);
        PUTA(c, p);
        PUTA(8 + c, qv);
        if ((c & 1) == 0) {
          cplx a = cmul(p, e256), bb = cmul(qv, e256);
          PUTA(16 + (c >> 1), a);
          PUTA(20 + (c >> 1), bb);
        }
        if (c == 0) s0 = cmul(p, e512);
        if (c == 4) s1 = cmul(p, e512);
        if (c < 7) p = cmul(p, e1);
      }
#pragma unroll
      for (int i = 0; i < 4; ++i) {
        PUTA(24 + 2 * i, s0);
        PUTA(25 + 2 * i, s1);
        if (i < 3) { s0 = cmul(s0, e128); s1 = cmul(s1, e128); }
      }
      cplx t = cmul(W, e1024);
#pragma unroll
      for (int i = 0; i < 9; ++i) { PUTA(32 + i, t); if (i < 8) t = cmul(t, e128); }
    }
#undef PUT
#undef PUTA
#undef PUTB
    __builtin_amdgcn_s_waitcnt(0xc07f);
    __builtin_amdgcn_wave_barrier();
    f32x4 acc[3] = {f32x4{0, 0, 0, 0}, f32x4{0, 0, 0, 0}, f32x4{0, 0, 0, 0}};
    {
      const int r = lane & 15, h = lane >> 4;
#pragma unroll
      for (int ks = 0; ks < 4; ++ks) {
        const int off = ((4 * ks + h) ^ r) << 4;
        v4u bf = *(LAS v4u*)(tab + (48 + r) * 256 + off);
#pragma unroll
        for (int t = 0; t < 3; ++t) {
          v4u af = *(LAS v4u*)(tab + (16 * t + r) * 256 + off);
          acc[t] = mfma_h(af, bf, acc[t]);
        }
      }
    }
    LAS float* kd = (LAS float*)tab;
    {
      const int j = lane & 15, q = lane >> 4;
#pragma unroll
      for (int t = 0; t < 3; ++t)
#pragma unroll
        for (int e = 0; e < 4; ++e) kd[(16 * t + 4 * q + e) * 16 + j] = acc[t][e] * smax;
    }
    LAS float* smx = (LAS float*)(lds + 65536);
    if (lane == 0) smx[wv] = smax;
    __syncthreads();
    const int c2 = wv >> 1, t2 = tid & 127;
    LAS float* k0 = (LAS float*)(lds + (2 * c2) * 16384);
    LAS float* k1 = (LAS float*)(lds + (2 * c2 + 1) * 16384);
    const float sm = fmaxf(smx[2 * c2], smx[2 * c2 + 1]);
    const float ksc = sm * (182.f / 1024.f);
    const float kinv = sm > 0.f ? 1.0f / ksc : 0.f;
    LAS unsigned short* kc = (LAS unsigned short*)(lds + (2 * c2) * 16384 + 4096);
#pragma unroll
    for (int i = 0; i < 6; ++i) {
      int idx = t2 + 128 * i;
      float v = (idx < 656) ? (k0[idx] + k1[idx]) * kinv : 0.f;
      kc[idx] = (unsigned short)(cvtpkh(v, v) & 0xFFFFu);
    }
    if (t2 == 0) ((float*)(ws + WS_KSC))[ch] = ksc;
    __syncthreads();
    GAS v4u* dst = (GAS v4u*)(ws + WS_KREV + (size_t)ch * KROW * 2);
    for (int p = t2; p < KROW / 8; p += 128) {
      v4u id = *(const GAS v4u*)(kIdxTab.v + p * 8);
      v4u o;
#pragma unroll
      for (int j = 0; j < 4; ++j) o[j] = (unsigned)kc[id[j] & 0xFFFFu] | ((unsigned)kc[id[j] >> 16] << 16);
      dst[p] = o;
    }
  }
}

constexpr int CV_KC = 4160;
constexpr int CV_KCH = 4 * CV_KC;
constexpr int CV_U = 4 * CV_KCH;
constexpr int CV_UCH = (256 + 2048) * 2;
constexpr int CV_X = CV_U + 4 * CV_UCH;
constexpr int CV_LDS = CV_X + 4 * 4096;

__global__ __launch_bounds__(256) void k_conv(const unsigned short* __restrict__ x, const unsigned short* __restrict__ dskip, unsigned char* __restrict__ ws) {
  extern __shared__ __attribute__((aligned(16))) unsigned char smem[];
  LAS unsigned char* lds = (LAS unsigned char*)smem;
  const int tid = threadIdx.x, lane = tid & 63, wv = __builtin_amdgcn_readfirstlane(tid >> 6);
  const int g = (blockIdx.x & 7) * 18 + (blockIdx.x >> 3);
  const int c0 = 4 * g;
  v2u xr[8];
  f32x2 st[8];
#pragma unroll
  for (int i = 0; i < 8; ++i) xr[i] = *(const GAS v2u*)(x + (size_t)(8 * tid + i) * D + c0);
#pragma unroll
  for (int i = 0; i < 8; ++i) st[i] = *(const GAS f32x2*)(ws + WS_ST1 + (8 * tid + i) * 8);
  for (int p = tid; p < 4 * 259; p += 256) {
    const int ch = p / 259, z = p % 259;
    const GAS v4u* src = (const GAS v4u*)(ws + WS_KREV + (size_t)(c0 + ch) * KROW * 2);
    v4u a = src[z], b = src[z + 1 < KROW / 8 ? z + 1 : z];
    unsigned d[6] = {a.x, a.y, a.z, a.w, b.x, b.y};
    LAS unsigned char* base = lds + ch * CV_KCH + z * 16;
    *(LAS v4u*)(base) = a;
    *(LAS v4u*)(base + CV_KC) = v4u{__builtin_amdgcn_alignbit(d[1], d[0], 16), __builtin_amdgcn_alignbit(d[2], d[1], 16), __builtin_amdgcn_alignbit(d[3], d[2], 16), __builtin_amdgcn_alignbit(d[4], d[3], 16)};
    *(LAS v4u*)(base + 2 * CV_KC) = v4u{d[1], d[2], d[3], d[4]};
    *(LAS v4u*)(base + 3 * CV_KC) = v4u{__builtin_amdgcn_alignbit(d[2], d[1], 16), __builtin_amdgcn_alignbit(d[3], d[2], 16), __builtin_amdgcn_alignbit(d[4], d[3], 16), __builtin_amdgcn_alignbit(d[5], d[4], 16)};
  }
  {
    if (tid < 128) {
      *(LAS v4u*)(lds + CV_U + (tid >> 5) * CV_UCH + (tid & 31) * 16) = v4u{0, 0, 0, 0};
    }
#pragma unroll
    for (int c = 0; c < 4; ++c) {
      float xv[8], uv[8];
#pragma unroll
      for (int i = 0; i < 8; ++i) {
        unsigned w = (c < 2) ? xr[i].x : xr[i].y;
        xv[i] = (c & 1) ? bfhi(w) : bflo(w);
        uv[i] = bfr(bfr(xv[i] - st[i].x) * st[i].y);
      }
      v4u up = {cvtpkh(uv[0], uv[1]), cvtpkh(uv[2], uv[3]), cvtpkh(uv[4], uv[5]), cvtpkh(uv[6], uv[7])};
      v4u xp = {cvtpk(xv[0], xv[1]), cvtpk(xv[2], xv[3]), cvtpk(xv[4], xv[5]), cvtpk(xv[6], xv[7])};
      *(LAS v4u*)(lds + CV_U + c * CV_UCH + 512 + tid * 16) = up;
      *(LAS v4u*)(lds + CV_X + c * 4096 + tid * 16) = xp;
    }
  }
  __syncthreads();
  f32x4 acc[8];
#pragma unroll
  for (int t = 0; t < 8; ++t) acc[t] = f32x4{0, 0, 0, 0};
  {
    const int b = lane & 15, h = lane >> 4;
    const int cpy = 3 - (b & 3);
    LAS unsigned char* abase = lds + wv * CV_KCH + cpy * CV_KC + (2047 - 16 - b + 8 * h - cpy) * 2;
    LAS unsigned char* bbase = lds + CV_U + wv * CV_UCH + 512 + (16 * (b - 1) + 8 * h) * 2;
#pragma unroll 1
    for (int rho = 0; rho < 8; ++rho) {
      v4u A[8], B[8];
#pragma unroll
      for (int i = 0; i < 8; ++i) {
        LAS unsigned char* pa = abase - 64 * (rho + 8 * i);
        v2u lo = *(LAS v2u*)pa, hi = *(LAS v2u*)(pa + 8);
        A[i] = v4u{lo.x, lo.y, hi.x, hi.y};
        B[i] = *(LAS v4u*)(bbase + 32 * (16 * i - 2 * rho));
      }
#pragma unroll
      for (int i = 0; i < 8; ++i)
#pragma unroll
        for (int m = 0; m + i < 8; ++m) acc[i + m] = mfma_h(A[i], B[m], acc[i + m]);
    }
  }
  {
    const int ch = c0 + wv;
    const float ksc = ((const float*)(ws + WS_KSC))[ch];
    const float dsk = bf2f(dskip[ch]);
    const int a = lane & 15, q = lane >> 4;
    unsigned pk[8][2];
#pragma unroll
    for (int t = 0; t < 8; ++t) {
      const int tok = 256 * t + 16 * a + 4 * q;
      v2u uu = *(LAS v2u*)(lds + CV_U + wv * CV_UCH + 512 + tok * 2);
      v2u xx = *(LAS v2u*)(lds + CV_X + wv * 4096 + tok * 2);
      float hv[4];
#pragma unroll
      for (int e = 0; e < 4; ++e) {
        unsigned uw = e < 2 ? uu.x : uu.y, xw = e < 2 ? xx.x : xx.y;
        h16x2 u2 = __builtin_bit_cast(h16x2, uw);
        float u = (float)u2[e & 1];
        float xv = (e & 1) ? bfhi(xw) : bflo(xw);
        float y = bfr(acc[t][e] * ksc);
        float tt = bfr(y + bfr(u * dsk));
        hv[e] = xv + tt;
      }
      pk[t][0] = cvtpk(hv[0], hv[1]);
      pk[t][1] = cvtpk(hv[2], hv[3]);
    }
    GAS unsigned char* hrow = (GAS unsigned char*)(ws + WS_HT + (size_t)ch * L * 2);
#pragma unroll
    for (int t = 0; t < 8; t += 2) {
      auto s0 = __builtin_amdgcn_permlane16_swap(pk[t][0], pk[t + 1][0], false, false);
      auto s1 = __builtin_amdgcn_permlane16_swap(pk[t][1], pk[t + 1][1], false, false);
      v4u o = {s0[0], s1[0], s0[1], s1[1]};
      const int tok = 256 * (t + (q & 1)) + 16 * a + 8 * (q >> 1);
      *(GAS v4u*)(hrow + tok * 2) = o;
    }
  }
}

constexpr int G1_A = 0;
constexpr int G1_ST = 576 * 64;
constexpr int G1_LDS = G1_ST + 12 * 32 * 8;

__global__ __launch_bounds__(768, 3) void k_gemm1(unsigned char* __restrict__ ws) {
  extern __shared__ __attribute__((aligned(16))) unsigned char smem[];
  LAS unsigned char* lds = (LAS unsigned char*)smem;
  const int tid = threadIdx.x, lane = tid & 63, wv = __builtin_amdgcn_readfirstlane(tid >> 6);
  const int sl = blockIdx.x % 3, tt = blockIdx.x / 3;
  const int ct = 12 * sl + wv;
  v4u bw[18];
  {
    const GAS v4u* wp = (const GAS v4u*)(ws + WS_W1T) + (size_t)ct * 18 * 64 + lane;
#pragma unroll
    for (int ks = 0; ks < 18; ++ks) bw[ks] = wp[ks * 64];
  }
  {
    v4u a[3];
#pragma unroll
    for (int i = 0; i < 3; ++i) {
      int p = tid + 768 * i, c = p >> 2, pc = p & 3;
      a[i] = *(const GAS v4u*)(ws + WS_HT + ((size_t)c * L + tt * 32 + pc * 8) * 2);
    }
#pragma unroll
    for (int i = 0; i < 3; ++i) {
      int p = tid + 768 * i, c = p >> 2, pc = p & 3;
      *(LAS v4u*)(lds + G1_A + c * 64 + ((((pc >> 1) ^ ((c >> 3) & 1)) << 1 | (pc & 1)) << 4)) = a[i];
    }
  }
  __syncthreads();
  f32x4 acc[2] = {f32x4{0, 0, 0, 0}, f32x4{0, 0, 0, 0}};
  float ps[2] = {0.f, 0.f}, pq[2] = {0.f, 0.f};
  {
    const int gq = lane >> 4, q = (lane & 15) >> 2, p = lane & 3;
    const unsigned ones = 0x3F803F80u;
#pragma unroll
    for (int ks = 0; ks < 18; ++ks) {
#pragma unroll
      for (int r = 0; r < 2; ++r) {
        const int c = 32 * ks + 8 * gq + q;
        LAS unsigned char* pa = lds + G1_A + c * 64 + ((r ^ (gq & 1)) << 5) + p * 8;
        v2u lo = tr_read(pa), hi = tr_read(pa + 4 * 64);
        v4u af = {lo.x, lo.y, hi.x, hi.y};
        acc[r] = mfma_bf(bw[ks], af, acc[r]);
        if ((2 * ks + r) % 12 == wv) {
          ps[r] = dot2bf(af.x, ones, ps[r]); ps[r] = dot2bf(af.y, ones, ps[r]); ps[r] = dot2bf(af.z, ones, ps[r]); ps[r] = dot2bf(af.w, ones, ps[r]);
          pq[r] = dot2bf(af.x, af.x, pq[r]); pq[r] = dot2bf(af.y, af.y, pq[r]); pq[r] = dot2bf(af.z, af.z, pq[r]); pq[r] = dot2bf(af.w, af.w, pq[r]);
        }
      }
    }
#pragma unroll
    for (int r = 0; r < 2; ++r) {
      ps[r] += __shfl_xor(ps[r], 16); ps[r] += __shfl_xor(ps[r], 32);
      pq[r] += __shfl_xor(pq[r], 16); pq[r] += __shfl_xor(pq[r], 32);
      if (lane < 16) *(LAS f32x2*)(lds + G1_ST + (wv * 32 + r * 16 + lane) * 8) = f32x2{ps[r], pq[r]};
    }
  }
  __syncthreads();
  {
    const int tok = lane & 15, q = lane >> 4;
    const f32x4 cs = *(const GAS f32x4*)(ws + WS_CS1 + (ct * 16 + 4 * q) * 4);
    unsigned pk[2][2];
#pragma unroll
    for (int r = 0; r < 2; ++r) {
      float s = 0.f, ss = 0.f;
#pragma unroll
      for (int w = 0; w < 12; ++w) { f32x2 v = *(LAS f32x2*)(lds + G1_ST + (w * 32 + r * 16 + tok) * 8); s += v.x; ss += v.y; }
      float m1, s1;
      ln_scalars(s, ss, m1, s1);
      const float mean = s * (1.0f / D);
      float var = __builtin_fmaf(-mean, mean, ss * (1.0f / D));
      var = var < 0.f ? 0.f : var;
      const float r1 = 1.0f / s1;
      const float m2 = bfr((mean - m1) * r1);
      const float s2 = bfr(__builtin_sqrtf(bfr(bfr(var * r1 * r1) + 1e-5f)));
      const float R = r1 / s2, me = __builtin_fmaf(m2, s1, m1);
      float v[4];
#pragma unroll
      for (int e = 0; e < 4; ++e) v[e] = gelu_f(bfr((acc[r][e] - me * cs[e]) * R));
      pk[r][0] = cvtpk(v[0], v[1]);
      pk[r][1] = cvtpk(v[2], v[3]);
      if (sl == 0 && wv == 0 && q == 0) *(f32x2*)(ws + WS_ST2 + (tt * 32 + r * 16 + tok) * 8) = f32x2{m1, r1};
    }
    auto s0 = __builtin_amdgcn_permlane16_swap(pk[0][0], pk[1][0], false, false);
    auto s1 = __builtin_amdgcn_permlane16_swap(pk[0][1], pk[1][1], false, false);
    v4u o = {s0[0], s1[0], s0[1], s1[1]};
    *(GAS v4u*)(ws + WS_H1 + ((size_t)(tt * 32 + (q & 1) * 16 + tok) * D + ct * 16 + 8 * (q >> 1)) * 2) = o;
  }
}

constexpr int ROWB = 1152;
constexpr int G2_A = 0;
constexpr int G2_H = 32 * ROWB;
constexpr int G2_LDS = G2_H + 12 * 1024;

__global__ __launch_bounds__(768, 3) void k_gemm2(unsigned char* __restrict__ ws) {
  extern __shared__ __attribute__((aligned(16))) unsigned char smem[];
  LAS unsigned char* lds = (LAS unsigned char*)smem;
  const int tid = threadIdx.x, lane = tid & 63, wv = __builtin_amdgcn_readfirstlane(tid >> 6);
  const int sl = blockIdx.x % 3, tt = blockIdx.x / 3;
  const int ct = 12 * sl + wv;
  v4u bw[18];
  {
    const GAS v4u* wp = (const GAS v4u*)(ws + WS_W2T) + (size_t)ct * 18 * 64 + lane;
#pragma unroll
    for (int ks = 0; ks < 18; ++ks) bw[ks] = wp[ks * 64];
  }
  v4u hpiece = *(const GAS v4u*)(ws + WS_HT + ((size_t)(ct * 16 + (lane >> 2)) * L + tt * 32 + (lane & 3) * 8) * 2);
  {
    v4u a[3];
#pragma unroll
    for (int i = 0; i < 3; ++i) {
      int p = tid + 768 * i, r = p / 72, c = p % 72;
      int grp = c / 18;
      int sh = grp == 0 ? -1 : (grp == 1 ? 1 : (grp == 2 ? -2 : 2));
      int t = tt * 32 + r + sh;
      a[i] = (t >= 0 && t < L) ? *(const GAS v4u*)(ws + WS_H1 + ((size_t)t * D + c * 8) * 2) : v4u{0, 0, 0, 0};
    }
#pragma unroll
    for (int i = 0; i < 3; ++i) {
      int p = tid + 768 * i, r = p / 72, c = p % 72;
      *(LAS v4u*)(lds + G2_A + r * ROWB + ((c ^ ((r >> 1) & 7)) << 4)) = a[i];
    }
    {
      const int c = lane >> 2, pc = lane & 3;
      *(LAS v4u*)(lds + G2_H + wv * 1024 + c * 64 + ((((pc >> 1) ^ ((c >> 3) & 1)) << 1 | (pc & 1)) << 4)) = hpiece;
    }
  }
  __syncthreads();
  f32x4 acc[2] = {f32x4{0, 0, 0, 0}, f32x4{0, 0, 0, 0}};
  const int tok = lane & 15, q = lane >> 4;
  {
    const int sw = (tok >> 1) & 7;
#pragma unroll
    for (int ks = 0; ks < 18; ++ks) {
#pragma unroll
      for (int r = 0; r < 2; ++r) {
        v4u af = *(LAS v4u*)(lds + G2_A + (r * 16 + tok) * ROWB + (((4 * ks + q) ^ sw) << 4));
        acc[r] = mfma_bf(bw[ks], af, acc[r]);
      }
    }
  }
  {
    unsigned pk[2][2], hk[2][2];
#pragma unroll
    for (int r = 0; r < 2; ++r) {
      const int c = 4 * q + ((lane & 15) >> 2);
      v2u hh = tr_read(lds + G2_H + wv * 1024 + c * 64 + ((r ^ ((c >> 3) & 1)) << 5) + (lane & 3) * 8);
      const f32x2 st = *(const GAS f32x2*)(ws + WS_ST2 + (tt * 32 + r * 16 + tok) * 8);
      float v[4];
#pragma unroll
      for (int e = 0; e < 4; ++e) {
        unsigned hw = e < 2 ? hh.x : hh.y;
        float h = (e & 1) ? bfhi(hw) : bflo(hw);
        float z = bfr(bfr(h - st.x) * st.y);
        v[e] = z + bfr(acc[r][e]);
      }
      pk[r][0] = cvtpk(v[0], v[1]);
      pk[r][1] = cvtpk(v[2], v[3]);
      hk[r][0] = hh.x;
      hk[r][1] = hh.y;
    }
    auto s0 = __builtin_amdgcn_permlane16_swap(pk[0][0], pk[1][0], false, false);
    auto s1 = __builtin_amdgcn_permlane16_swap(pk[0][1], pk[1][1], false, false);
    auto h0 = __builtin_amdgcn_permlane16_swap(hk[0][0], hk[1][0], false, false);
    auto h1 = __builtin_amdgcn_permlane16_swap(hk[0][1], hk[1][1], false, false);
    const size_t off = ((size_t)(tt * 32 + (q & 1) * 16 + tok) * D + ct * 16 + 8 * (q >> 1)) * 2;
    *(GAS v4u*)(ws + WS_X1 + off) = v4u{s0[0], s1[0], s0[1], s1[1]};
    *(GAS v4u*)(ws + WS_HR + off) = v4u{h0[0], h1[0], h0[1], h1[1]};
  }
}

constexpr int ML_A = 0, ML_H = 64 * ROWB, ML_ST = 2 * 64 * ROWB;
constexpr int ML_LDS = ML_ST + 64 * 8;
constexpr int DEPTH = 3;

__global__ __launch_bounds__(768, 3) void k_mlp(unsigned char* __restrict__ ws) {
  extern __shared__ __attribute__((aligned(16))) unsigned char smem[];
  LAS unsigned char* lds = (LAS unsigned char*)smem;
  const int tid = threadIdx.x, lane = tid & 63, wv = __builtin_amdgcn_readfirstlane(tid >> 6);
  const int s = blockIdx.x & 7, tt = blockIdx.x >> 3;
  const GAS v4u* p3 = (const GAS v4u*)(ws + WS_W3T) + (size_t)((36 * s + 3 * wv) * 18) * 64 + lane;
  const GAS v4u* p4 = (const GAS v4u*)(ws + WS_W4T) + (size_t)((3 * wv) * 144 + 18 * s) * 64 + lane;
  v4u bw[DEPTH][3];
#pragma unroll
  for (int d = 0; d < DEPTH; ++d)
#pragma unroll
    for (int c = 0; c < 3; ++c) bw[d][c] = p3[(c * 18 + d) * 64];
  __builtin_amdgcn_sched_barrier(0);
  {
    const GAS v4u* xa = (const GAS v4u*)(ws + WS_X1) + (size_t)tt * 64 * 72;
    v4u a[6];
#pragma unroll
    for (int i = 0; i < 6; ++i) a[i] = xa[tid + 768 * i];
#pragma unroll
    for (int i = 0; i < 6; ++i) {
      int p = tid + 768 * i, r = p / 72, c = p % 72;
      *(LAS v4u*)(lds + ML_A + r * ROWB + ((c ^ ((r >> 1) & 7)) << 4)) = a[i];
    }
  }
  __syncthreads();
  if (wv < 8) {
    const int r = wv * 8 + (lane >> 3), sub = lane & 7;
    float sm = 0.f, ss = 0.f;
    const unsigned ones = 0x3F803F80u;
#pragma unroll
    for (int i = 0; i < 9; ++i) {
      v4u a = *(LAS v4u*)(lds + ML_A + r * ROWB + (sub + 8 * i) * 16);
#pragma unroll
      for (int j = 0; j < 4; ++j) { sm = dot2bf(a[j], ones, sm); ss = dot2bf(a[j], a[j], ss); }
    }
#pragma unroll
    for (int o = 1; o < 8; o <<= 1) { sm += __shfl_xor(sm, o); ss += __shfl_xor(ss, o); }
    float m, sd;
    ln_scalars(sm, ss, m, sd);
    if (sub == 0) *(LAS f32x2*)(lds + ML_ST + r * 8) = f32x2{m, 1.0f / sd};
  }
  const int tok = lane & 15, q = lane >> 4;
  const int sw = (tok >> 1) & 7;
  LAS unsigned char* abase = lds + ML_A + tok * ROWB;
  f32x4 acc[4][3];
#pragma unroll
  for (int r = 0; r < 4; ++r)
#pragma unroll
    for (int c = 0; c < 3; ++c) acc[r][c] = f32x4{0, 0, 0, 0};
#pragma unroll
  for (int ks = 0; ks < 18; ++ks) {
    v4u af[4];
#pragma unroll
    for (int r = 0; r < 4; ++r) af[r] = *(LAS v4u*)(abase + r * 16 * ROWB + (((4 * ks + q) ^ sw) << 4));
    v4u cur[3];
#pragma unroll
    for (int c = 0; c < 3; ++c) {
      cur[c] = bw[ks % DEPTH][c];
      int nk = ks + DEPTH;
      if (nk < 18) bw[ks % DEPTH][c] = p3[(c * 18 + nk) * 64];
      else bw[ks % DEPTH][c] = p4[(c * 144 + nk - 18) * 64];
    }
#pragma unroll
    for (int c = 0; c < 3; ++c)
#pragma unroll
      for (int r = 0; r < 4; ++r) acc[r][c] = mfma_bf(cur[c], af[r], acc[r][c]);
    __builtin_amdgcn_sched_barrier(0);
  }
  __syncthreads();
  LAS unsigned char* H = lds + ML_H;
  {
    f32x2 st[4];
#pragma unroll
    for (int r = 0; r < 4; ++r) st[r] = *(LAS f32x2*)(lds + ML_ST + (r * 16 + tok) * 8);
#pragma unroll
    for (int c = 0; c < 3; ++c) {
      const f32x4 cs = *(const GAS f32x4*)(ws + WS_CS3 + ((36 * s + 3 * wv + c) * 16 + 4 * q) * 4);
#pragma unroll
      for (int r = 0; r < 4; ++r) {
        float v[4];
#pragma unroll
        for (int e = 0; e < 4; ++e) v[e] = gelu_f((acc[r][c][e] - st[r].x * cs[e]) * st[r].y);
        v2u o = {cvtpk(v[0], v[1]), cvtpk(v[2], v[3])};
        const int chunk = 2 * (3 * wv + c) + (q >> 1);
        *(LAS v2u*)(H + (r * 16 + tok) * ROWB + ((chunk ^ sw) << 4) + (q & 1) * 8) = o;
        acc[r][c] = f32x4{0, 0, 0, 0};
      }
    }
  }
  __syncthreads();
  LAS unsigned char* hbase = H + tok * ROWB;
#pragma unroll
  for (int k2 = 0; k2 < 18; ++k2) {
    const int ks = 18 + k2;
    v4u af[4];
#pragma unroll
    for (int r = 0; r < 4; ++r) af[r] = *(LAS v4u*)(hbase + r * 16 * ROWB + (((4 * k2 + q) ^ sw) << 4));
    v4u cur[3];
#pragma unroll
    for (int c = 0; c < 3; ++c) {
      cur[c] = bw[ks % DEPTH][c];
      int nk = k2 + DEPTH;
      if (nk < 18) bw[ks % DEPTH][c] = p4[(c * 144 + nk) * 64];
    }
#pragma unroll
    for (int c = 0; c < 3; ++c)
#pragma unroll
      for (int r = 0; r < 4; ++r) acc[r][c] = mfma_bf(cur[c], af[r], acc[r][c]);
    __builtin_amdgcn_sched_barrier(0);
  }
  {
    GAS v4u* po = (GAS v4u*)(ws + WS_PART) + (size_t)((s * 32 + tt) * 72) * 64 + lane;
#pragma unroll
    for (int c = 0; c < 3; ++c)
#pragma unroll
      for (int rp = 0; rp < 2; ++rp) {
        unsigned x0 = cvtpk(acc[2 * rp][c][0], acc[2 * rp][c][1]), x1 = cvtpk(acc[2 * rp][c][2], acc[2 * rp][c][3]);
        unsigned y0 = cvtpk(acc[2 * rp + 1][c][0], acc[2 * rp + 1][c][1]), y1 = cvtpk(acc[2 * rp + 1][c][2], acc[2 * rp + 1][c][3]);
        auto s0 = __builtin_amdgcn_permlane16_swap(x0, y0, false, false);
        auto s1 = __builtin_amdgcn_permlane16_swap(x1, y1, false, false);
        po[(rp * 36 + 3 * wv + c) * 64] = v4u{s0[0], s1[0], s0[1], s1[1]};
      }
  }
}

__global__ __launch_bounds__(256) void k_reduce(const unsigned char* __restrict__ ws, unsigned short* __restrict__ out) {
  const int lane = threadIdx.x & 63, unit = blockIdx.x * 4 + (threadIdx.x >> 6);
  const int tt = unit / 72, u = unit % 72, rp = u / 36, oc = u % 36;
  const int q = lane >> 4, tok = lane & 15;
  const size_t off = ((size_t)(tt * 64 + rp * 32 + (q & 1) * 16 + tok) * D + oc * 16 + 8 * (q >> 1)) * 2;
  v4u p[8];
#pragma unroll
  for (int s = 0; s < 8; ++s) p[s] = *((const GAS v4u*)(ws + WS_PART) + (size_t)((s * 32 + tt) * 72 + u) * 64 + lane);
  const v4u xv = *(const GAS v4u*)(ws + WS_X1 + off), hv = *(const GAS v4u*)(ws + WS_HR + off);
  v4u o;
#pragma unroll
  for (int j = 0; j < 4; ++j) {
    float lo = 0.f, hi = 0.f;
#pragma unroll
    for (int s = 0; s < 8; ++s) { lo += bflo(p[s][j]); hi += bfhi(p[s][j]); }
    lo = bflo(hv[j]) + bfr(bflo(xv[j]) + bfr(lo));
    hi = bfhi(hv[j]) + bfr(bfhi(xv[j]) + bfr(hi));
    o[j] = cvtpk(lo, hi);
  }
  *(GAS v4u*)((GAS unsigned char*)out + off) = o;
}

extern "C" void kernel_launch(void* const* d_in, const int* in_sizes, int n_in, void* d_out, int out_size, void* d_ws, size_t ws_size,
                              hipStream_t stream) {
  const unsigned short* x = (const unsigned short*)d_in[0];
  unsigned char* ws = (unsigned char*)d_ws;
  (void)hipFuncSetAttribute((const void*)k_prep, hipFuncAttributeMaxDynamicSharedMemorySize, K1_LDS);
  (void)hipFuncSetAttribute((const void*)k_conv, hipFuncAttributeMaxDynamicSharedMemorySize, CV_LDS);
  (void)hipFuncSetAttribute((const void*)k_gemm1, hipFuncAttributeMaxDynamicSharedMemorySize, G1_LDS);
  (void)hipFuncSetAttribute((const void*)k_gemm2, hipFuncAttributeMaxDynamicSharedMemorySize, G2_LDS);
  (void)hipFuncSetAttribute((const void*)k_mlp, hipFuncAttributeMaxDynamicSharedMemorySize, ML_LDS);
  k_prep<<<K1_NPT + K1_NST + K1_NKG, 256, K1_LDS, stream>>>(
      x, (const unsigned short*)d_in[1], (const unsigned short*)d_in[2], (const unsigned short*)d_in[3], (const unsigned short*)d_in[4],
      (const unsigned short*)d_in[5], (const unsigned short*)d_in[6], (const unsigned short*)d_in[7], (const unsigned short*)d_in[10],
      (const unsigned short*)d_in[11], (const unsigned short*)d_in[12], (const unsigned short*)d_in[13], ws);
  k_conv<<<144, 256, CV_LDS, stream>>>(x, (const unsigned short*)d_in[8], ws);
  k_gemm1<<<192, 768, G1_LDS, stream>>>(ws);
  k_gemm2<<<192, 768, G2_LDS, stream>>>(ws);
  k_mlp<<<256, 768, ML_LDS, stream>>>(ws);
  k_reduce<<<32 * 72 / 4, 256, 0, stream>>>(ws, (unsigned short*)d_out);
}
```

```cpp
#include <hip/hip_runtime.h>
#include <hip/hip_bf16.h>
#include <stdint.h>

typedef unsigned long long u64;
typedef unsigned v4u __attribute__((ext_vector_type(4)));
typedef unsigned v2u __attribute__((ext_vector_type(2)));
typedef float f32x4 __attribute__((ext_vector_type(4)));
typedef float f32x2 __attribute__((ext_vector_type(2)));
typedef short s16x8 __attribute__((ext_vector_type(8)));
typedef _Float16 h16x8 __attribute__((ext_vector_type(8)));
typedef _Float16 h16x2 __attribute__((ext_vector_type(2)));
typedef __bf16 bf16x2_t __attribute__((ext_vector_type(2)));
#define GAS __attribute__((address_space(1)))
#define LAS __attribute__((address_space(3)))
#define DI __device__ __forceinline__
#define RLX __ATOMIC_RELAXED, __HIP_MEMORY_SCOPE_AGENT
typedef GAS u64 gu64;
typedef __amdgpu_buffer_rsrc_t rsrc_t;

#define NLAUNCH 1

constexpr int L = 2048, D = 576, NS = 128, F = 4608;
constexpr int NT = 768;

constexpr int WS_CTRL = 0;
constexpr int WS_GR = 4096;
constexpr int WS_FPT = WS_GR + 2048 * 16;
constexpr int WS_F2 = WS_FPT + 4 * 2048;
constexpr int WS_F3 = WS_F2 + 8 * 2048;
constexpr int WS_F4 = WS_F3 + 64 * 32;
constexpr int WS_F5 = WS_F4 + 64 * 32;
constexpr int WS_W1T = 131072;
constexpr int WS_W2T = WS_W1T + 36 * 18 * 1024;
constexpr int WS_W3T = WS_W2T + 36 * 18 * 1024;
constexpr int WS_W4T = WS_W3T + 288 * 18 * 1024;
constexpr int WS_CS1 = WS_W4T + 36 * 144 * 1024;
constexpr int WS_CS3 = WS_CS1 + 4096;
constexpr int WS_HT = WS_CS3 + 4608 * 4;
constexpr int WS_H1 = WS_HT + D * L * 2;
constexpr int WS_ST2 = WS_H1 + D * L * 2;
constexpr int WS_X1 = WS_ST2 + 2048 * 8;
constexpr int WS_HR = WS_X1 + D * L * 2;
constexpr int WS_PART = WS_HR + D * L * 2;
constexpr int WS_END = WS_PART + 8 * D * L * 2;
static_assert(WS_F5 + 32 * 64 <= WS_W1T, "control block");

#define STAMP(i)
#define STAMPW(w, i)

DI float bf2f(unsigned short b) { return __builtin_bit_cast(float, (unsigned)b << 16); }
DI float asf(unsigned u) { return __builtin_bit_cast(float, u); }
DI unsigned asu(float f) { return __builtin_bit_cast(unsigned, f); }
DI float bflo(unsigned u) { return __builtin_bit_cast(float, u << 16); }
DI float bfhi(unsigned u) { return __builtin_bit_cast(float, u & 0xFFFF0000u); }
DI unsigned cvtpk(float lo, float hi) {
  f32x2 v = {lo, hi};
  bf16x2_t b = __builtin_convertvector(v, bf16x2_t);
  return __builtin_bit_cast(unsigned, b);
}
DI float bfr(float x) { return bflo(cvtpk(x, x)); }
DI unsigned cvtpkh(float lo, float hi) {
  f32x2 v = {lo, hi};
  h16x2 b = __builtin_convertvector(v, h16x2);
  return __builtin_bit_cast(unsigned, b);
}
DI f32x4 mfma_bf(v4u a, v4u b, f32x4 c) {
  return __builtin_amdgcn_mfma_f32_16x16x32_bf16(__builtin_bit_cast(s16x8, a), __builtin_bit_cast(s16x8, b), c, 0, 0, 0);
}
DI f32x4 mfma_h(v4u a, v4u b, f32x4 c) {
  return __builtin_amdgcn_mfma_f32_16x16x32_f16(__builtin_bit_cast(h16x8, a), __builtin_bit_cast(h16x8, b), c, 0, 0, 0);
}
DI float dot2bf(unsigned a, unsigned b, float acc) {
  return __builtin_amdgcn_fdot2_f32_bf16(__builtin_bit_cast(bf16x2_t, a), __builtin_bit_cast(bf16x2_t, b), acc, false);
}
DI v2u tr_read(LAS unsigned char* p) {
  typedef short v4i16_t __attribute__((ext_vector_type(4)));
  return __builtin_bit_cast(v2u, __builtin_amdgcn_ds_read_tr16_b64_v4i16((LAS v4i16_t*)p));
}
DI float gelu_f(float x) {
  const float c0 = -2.3022081986f, c1 = -0.10294324f;
  float x2 = x * x;
  float p = __builtin_fmaf(x2, c1, c0);
  float e = __builtin_amdgcn_exp2f(x * p);
  return x * __builtin_amdgcn_rcpf(1.0f + e);
}
DI void ln_scalars(float sum, float sumsq, float& m, float& s) {
  float mean = sum * (1.0f / D);
  float var = __builtin_fmaf(-mean, mean, sumsq * (1.0f / D));
  var = var < 0.f ? 0.f : var;
  m = bfr(mean);
  s = bfr(__builtin_sqrtf(bfr(bfr(var) + 1e-5f)));
}
constexpr int SC1 = 16;
DI v4u ld16(rsrc_t rs, int voff, int soff = 0) { return __builtin_amdgcn_raw_buffer_load_b128(rs, voff, soff, SC1); }
DI v2u ld8(rsrc_t rs, int voff, int soff = 0) { return __builtin_amdgcn_raw_buffer_load_b64(rs, voff, soff, SC1); }
DI void st16(rsrc_t rs, v4u v, int voff, int soff = 0) { __builtin_amdgcn_raw_buffer_store_b128(v, rs, voff, soff, SC1); }
DI void st8(rsrc_t rs, v2u v, int voff, int soff = 0) { __builtin_amdgcn_raw_buffer_store_b64(v, rs, voff, soff, SC1); }
DI void st4(rsrc_t rs, unsigned v, int voff, int soff = 0) { __builtin_amdgcn_raw_buffer_store_b32(v, rs, voff, soff, SC1); }
#define DRAIN() asm volatile("s_waitcnt vmcnt(0)" ::: "memory")
#define ORDER() __builtin_amdgcn_fence(__ATOMIC_ACQUIRE, "wavefront")
constexpr unsigned SPIN_CAP = 1u << 15;

DI void wait_flags(unsigned char* ws, int off, int n, u64 tag, int lane) {
  gu64* p = (gu64*)(ws + off) + (lane < n ? lane : 0);
  for (unsigned spins = 0; spins < SPIN_CAP; ++spins) {
    if (__all(__hip_atomic_load(p, RLX) == tag)) break;
  }
  ORDER();
}

struct KIdx { unsigned short v[2080]; };
constexpr KIdx make_kidx() {
  KIdx t{};
  for (int y = 0; y < 2080; ++y) {
    if (y >= 2048) { t.v[y] = 767; continue; }
    int j = 2047 - y;
    int v = j;
    if (j >= 256) {
      int sh = j >= 1024 ? 3 : (j >= 512 ? 2 : 1);
      int step = 1 << sh, lo = (j >> sh) << sh, rem = j - lo, half = step >> 1;
      if (rem > half || (rem == half && (((lo >> sh) & 1) != 0))) lo += step;
      v = lo;
    }
    int seg = v >= 1024 ? 3 : (v >= 512 ? 2 : (v >= 256 ? 1 : 0));
    int u = seg == 0 ? v : v - (128 << seg);
    int rb = seg == 0 ? 0 : (seg == 1 ? 16 : (seg == 2 ? 24 : 32));
    int row = rb + ((u & 7) >> seg) + (u >> 7) * (8 >> seg);
    int col = (u >> 3) & 15;
    t.v[y] = (unsigned short)(row * 16 + col);
  }
  return t;
}
__device__ const KIdx kIdxTab = make_kidx();

struct cplx { float r, i; };
DI cplx cmul(cplx a, cplx b) { return cplx{__builtin_fmaf(a.r, b.r, -a.i * b.i), __builtin_fmaf(a.r, b.i, a.i * b.r)}; }
DI cplx cexp_r(float a2, float th, float r) {
  float m = __builtin_amdgcn_exp2f(a2 * r);
  float ang = __builtin_amdgcn_fractf(th * r);
  return cplx{m * __builtin_amdgcn_cosf(ang), m * __builtin_amdgcn_sinf(ang)};
}

constexpr int CV_TAB = 57 * 256;
constexpr int CV_CH = 2 * CV_TAB;
constexpr int CV_KC = 4160;
constexpr int CV_XCH = 4 * CV_KC;
constexpr int CV_KCT = 4 * CV_KC + 8192;
constexpr int CV_U = 4 * CV_CH;
constexpr int CV_UCH = (256 + 2048) * 2;
constexpr int CV_X = CV_U + 4 * CV_UCH;
constexpr int CV_MISC = CV_X + 4 * 4096;
static_assert(CV_KCT + 1536 <= CV_CH, "conv channel region");
constexpr int ROWB = 1152;
constexpr int G_A = 0;
constexpr int G_ST = 36864;
constexpr int G_H = G_ST + 3072;
constexpr int G_O = G_H + 12 * 1024;
constexpr int G_OROW = 400;
constexpr int ML_A = 0, ML_H = 64 * ROWB, ML_ST = 2 * 64 * ROWB;
constexpr int LDS_BYTES = 152 * 1024;
static_assert(CV_MISC + 128 <= LDS_BYTES && ML_ST + 512 <= LDS_BYTES && G_O + 2 * 32 * G_OROW <= LDS_BYTES, "LDS");

DI void lds_arrive_wait(LAS unsigned* ctr, unsigned target, int lane) {
  asm volatile("s_waitcnt lgkmcnt(0)" ::: "memory");
  if (lane == 0) __hip_atomic_fetch_add(ctr, 1u, __ATOMIC_RELAXED, __HIP_MEMORY_SCOPE_WORKGROUP);
  while (__hip_atomic_load(ctr, __ATOMIC_RELAXED, __HIP_MEMORY_SCOPE_WORKGROUP) < target) __builtin_amdgcn_s_sleep(1);
  asm volatile("" ::: "memory");
}

DI void pretile_unit(LAS unsigned char* lds, rsrc_t rs, unsigned char* ws, int unit, u64 tag, const unsigned short* __restrict__ w1, const unsigned short* __restrict__ w2,
                     const unsigned short* __restrict__ w3, const unsigned short* __restrict__ w4) {
  const int tid = threadIdx.x, lane = tid & 63, wv = __builtin_amdgcn_readfirstlane(tid >> 6);
  const unsigned short* w;
  int ldw, dst, ksn, cs = -1;
  if (unit < 9) { w = w1 + unit * 64; ldw = D; dst = WS_W1T + unit * 4 * 18 * 1024; ksn = 18; cs = WS_CS1 + unit * 256; }
  else if (unit < 18) { w = w2 + (unit - 9) * 64; ldw = D; dst = WS_W2T + (unit - 9) * 4 * 18 * 1024; ksn = 18; }
  else if (unit < 90) { w = w3 + (unit - 18) * 64; ldw = F; dst = WS_W3T + (unit - 18) * 4 * 18 * 1024; ksn = 18; cs = WS_CS3 + (unit - 18) * 256; }
  else { int u = unit - 90, kc = u / 9, cg = u % 9; w = w4 + (size_t)kc * 576 * D + cg * 64; ldw = D; dst = WS_W4T + (cg * 4 * 144 + kc * 18) * 1024; ksn = 144; }
  v4u a[6];
#pragma unroll
  for (int i = 0; i < 6; ++i) {
    int p = tid + NT * i, r = p >> 3, c = p & 7;
    a[i] = *(const GAS v4u*)(w + (size_t)r * ldw + c * 8);
  }
  __syncthreads();
#pragma unroll
  for (int i = 0; i < 6; ++i) {
    int p = tid + NT * i, r = p >> 3, c = p & 7;
    int f = ((r >> 1) & 1) | (((r >> 3) & 1) << 1);
    *(LAS v4u*)(lds + r * 128 + ((((c >> 1) ^ f) << 1 | (c & 1)) << 4)) = a[i];
  }
  __syncthreads();
  const int ct = wv & 3, k0 = 6 * (wv >> 2);
  const int g = lane >> 4, q = (lane & 15) >> 2, p = lane & 3;
  const int f = ((q >> 1) & 1) | ((g & 1) << 1);
  float csum = 0.f;
  const unsigned ones = 0x3F803F80u;
#pragma unroll
  for (int ks = 0; ks < 6; ++ks) {
    LAS unsigned char* pa = lds + (32 * (k0 + ks) + 8 * g + q) * 128 + ((ct ^ f) << 5) + p * 8;
    v2u lo = tr_read(pa), hi = tr_read(pa + 4 * 128);
    v4u o = {lo.x, lo.y, hi.x, hi.y};
    st16(rs, o, lane * 16, dst + (ct * ksn + k0 + ks) * 1024);
    csum = dot2bf(o.x, ones, csum); csum = dot2bf(o.y, ones, csum); csum = dot2bf(o.z, ones, csum); csum = dot2bf(o.w, ones, csum);
  }
  if (cs >= 0) {
    csum += __shfl_xor(csum, 16);
    csum += __shfl_xor(csum, 32);
    LAS float* pc = (LAS float*)(lds + 73728);
    if (lane < 16) pc[wv * 16 + lane] = csum;
    __syncthreads();
    if (tid < 64) st4(rs, __builtin_bit_cast(unsigned, pc[tid] + pc[64 + tid] + pc[128 + tid]), cs + tid * 4);
  }
  DRAIN();
  __syncthreads();
  if (tid < 4) __hip_atomic_store((gu64*)(ws + WS_FPT) + tid * 256 + unit, tag, RLX);
}

__global__ __launch_bounds__(NT, 3) void k_all(const unsigned short* __restrict__ x, const unsigned short* __restrict__ Are, const unsigned short* __restrict__ Aim,
                                               const unsigned short* __restrict__ Bre, const unsigned short* __restrict__ Bim, const unsigned short* __restrict__ Cre,
                                               const unsigned short* __restrict__ Cim, const unsigned short* __restrict__ logdt, const unsigned short* __restrict__ dskip,
                                               const unsigned short* __restrict__ w1, const unsigned short* __restrict__ w2, const unsigned short* __restrict__ w3,
                                               const unsigned short* __restrict__ w4, unsigned char* __restrict__ ws, unsigned short* __restrict__ out, int plo, int phi) {
  extern __shared__ __attribute__((aligned(16))) unsigned char smem[];
  LAS unsigned char* lds = (LAS unsigned char*)smem;
  const int tid = threadIdx.x, lane = tid & 63, wv = __builtin_amdgcn_readfirstlane(tid >> 6);
  const int b = blockIdx.x, xcd = b & 7, jj = b >> 3;
  const rsrc_t rs = __builtin_amdgcn_make_buffer_rsrc((void*)ws, 0, WS_END, 0x00020000);
  STAMP(0);
  u64 tag;
  {
    u64 e = __hip_atomic_load((gu64*)(ws + WS_CTRL), RLX);
    unsigned lo = __builtin_amdgcn_readfirstlane((unsigned)e), hi = __builtin_amdgcn_readfirstlane((unsigned)(e >> 32));
    tag = (((u64)hi << 32) | lo) + 1;
  }
  const bool is_conv = jj < 18, is_gemm = jj < 24;

  const int c0 = 4 * (18 * xcd + jj);
  v2u xr[8];
  if (plo <= 1) {
    if (tid < 4) ((LAS unsigned*)(lds + CV_MISC))[tid] = 0;
    __syncthreads();
  }
  if (plo <= 0) {
    if (wv == 0) {
      const int tok = b * 8 + (lane >> 3), sub = lane & 7;
      const GAS v4u* row = (const GAS v4u*)(x + (size_t)tok * D);
      v4u a[9];
#pragma unroll
      for (int i = 0; i < 9; ++i) a[i] = row[sub + 8 * i];
      const int gc = is_conv ? c0 : 0, gs = is_conv ? D : 0;
#pragma unroll
      for (int i = 0; i < 8; ++i) xr[i] = *(const GAS v2u*)(x + (size_t)(8 * tid + i) * gs + gc);
      __builtin_amdgcn_sched_barrier(0);
      float s = 0.f, ss = 0.f;
      const unsigned ones = 0x3F803F80u;
#pragma unroll
      for (int i = 0; i < 9; ++i) {
#pragma unroll
        for (int j = 0; j < 4; ++j) { s = dot2bf(a[i][j], ones, s); ss = dot2bf(a[i][j], a[i][j], ss); }
      }
#pragma unroll
      for (int o = 1; o < 8; o <<= 1) { s += __shfl_xor(s, o); ss += __shfl_xor(ss, o); }
      float m, sd;
      ln_scalars(s, ss, m, sd);
      if (sub < 2) {
        unsigned bits = __builtin_bit_cast(unsigned, sub == 0 ? m : sd) >> 16;
        __hip_atomic_store((gu64*)(ws + WS_GR) + tok * 2 + sub, (tag << 16) | bits, RLX);
      }
      STAMP(1);
    } else if (wv < 4 && is_conv) {
#pragma unroll
      for (int i = 0; i < 8; ++i) xr[i] = *(const GAS v2u*)(x + (size_t)(8 * tid + i) * D + c0);
    }
    if (!is_conv) {
      const int idx = (jj - 18) * 8 + xcd;
      pretile_unit(lds, rs, ws, idx, tag, w1, w2, w3, w4);
      if (idx >= 48 && idx < 98) pretile_unit(lds, rs, ws, 112 + idx - 48, tag, w1, w2, w3, w4);
      STAMP(2);
    }
  }
  if (plo <= 1 && phi > 1 && is_conv) {
    LAS unsigned* cnt = (LAS unsigned*)(lds + CV_MISC);
    LAS float* smx = (LAS float*)(lds + CV_MISC + 32);
    LAS float* kscs = (LAS float*)(lds + CV_MISC + 64);
    if (wv < 4) {
      if (tid < 128) *(LAS v4u*)(lds + CV_U + (tid >> 5) * CV_UCH + (tid & 31) * 16) = v4u{0, 0, 0, 0};
      v4u gr[8];
      const unsigned tg = (unsigned)tag, tgh = (unsigned)(tag >> 32) & 0xFFFFu;
      for (unsigned spins = 0; spins < SPIN_CAP; ++spins) {
        bool ok = true;
        asm volatile("" ::: "memory");
#pragma unroll
        for (int i = 0; i < 8; ++i) gr[i] = ld16(rs, WS_GR + (8 * tid + i) * 16);
#pragma unroll
        for (int i = 0; i < 8; ++i) {
          ok &= (__builtin_amdgcn_alignbit(gr[i].y, gr[i].x, 16) == tg) && ((gr[i].y >> 16) == tgh);
          ok &= (__builtin_amdgcn_alignbit(gr[i].w, gr[i].z, 16) == tg) && ((gr[i].w >> 16) == tgh);
        }
        if (__all(ok)) break;
      }
      STAMP(3);
#pragma unroll
      for (int c = 0; c < 4; ++c) {
        float xv[8], uv[8];
#pragma unroll
        for (int i = 0; i < 8; ++i) {
          unsigned w = (c < 2) ? xr[i].x : xr[i].y;
          xv[i] = (c & 1) ? bfhi(w) : bflo(w);
          const float m = bflo(gr[i].x), r = __builtin_amdgcn_rcpf(bflo(gr[i].z));
          uv[i] = bfr(bfr(xv[i] - m) * r);
        }
        v4u up = {cvtpkh(uv[0], uv[1]), cvtpkh(uv[2], uv[3]), cvtpkh(uv[4], uv[5]), cvtpkh(uv[6], uv[7])};
        v4u xp = {cvtpk(xv[0], xv[1]), cvtpk(xv[2], xv[3]), cvtpk(xv[4], xv[5]), cvtpk(xv[6], xv[7])};
        *(LAS v4u*)(lds + CV_U + c * CV_UCH + 512 + tid * 16) = up;
        *(LAS v4u*)(lds + CV_X + c * 4096 + tid * 16) = xp;
      }
      STAMP(4);
    } else {
      const int cl = (wv - 4) >> 1, half = wv & 1, ch = c0 + cl, n = half * 64 + lane;
      const int gi = ch * NS + n;
      const float dt = bfr(expf(bf2f(logdt[ch])));
      const float zr = dt * bf2f(Are[gi]), zi = dt * bf2f(Aim[gi]);
      const float dBr = dt * bf2f(Bre[gi]), dBi = dt * bf2f(Bim[gi]);
      const float Cr = bf2f(Cre[gi]), Ci = bf2f(Cim[gi]);
      cplx W = cmul(cplx{Cr, Ci}, cplx{dBr, dBi});
      float smax = fmaxf(fabsf(W.r), fabsf(W.i));
#pragma unroll
      for (int o = 1; o < 64; o <<= 1) smax = fmaxf(smax, __shfl_xor(smax, o));
      const float inv = smax > 0.f ? 1.0f / smax : 0.f;
      W.r *= inv; W.i *= inv;
      const float a2 = zr * 1.44269504089f, th = zi * 0.15915494309f;
      const cplx e1 = cexp_r(a2, th, 1.f), e8 = cexp_r(a2, th, 8.f), e128 = cexp_r(a2, th, 128.f);
      const cplx e256 = cexp_r(a2, th, 256.f), e512 = cexp_r(a2, th, 512.f), e1024 = cexp_r(a2, th, 1024.f);
      STAMPW(4, 5);
      LAS unsigned char* chan = lds + cl * CV_CH;
      LAS unsigned char* tab = chan + half * CV_TAB;
      const int nsw = lane >> 2, nlo = (lane & 3) * 4;
#define PUT(row, val) *(LAS unsigned*)(tab + (row) * 256 + ((nsw ^ ((row) & 15)) << 4) + nlo) = (val)
#define PUTA(row, z) PUT(row, cvtpkh((z).r, -(z).i))
#define PUTB(row, z) PUT(41 + (row), cvtpkh((z).r, (z).i))
      {
        cplx c = {1.f, 0.f};
        PUTB(0, c);
        c = e8;
#pragma unroll
        for (int j = 1; j < 16; ++j) { PUTB(j, c); if (j < 15) c = cmul(c, e8); }
      }
      {
        cplx p = W;
        cplx s0, s1;
#pragma unroll
        for (int c = 0; c < 8; ++c) {
          cplx qv = cmul(p, e128);
          PUTA(c, p);
          PUTA(8 + c, qv);
          if ((c & 1) == 0) {
            cplx a = cmul(p, e256), bb = cmul(qv, e256);
            PUTA(16 + (c >> 1), a);
            PUTA(20 + (c >> 1), bb);
          }
          if (c == 0) s0 = cmul(p, e512);
          if (c == 4) s1 = cmul(p, e512);
          if (c < 7) p = cmul(p, e1);
        }
#pragma unroll
        for (int i = 0; i < 4; ++i) {
          PUTA(24 + 2 * i, s0);
          PUTA(25 + 2 * i, s1);
          if (i < 3) { s0 = cmul(s0, e128); s1 = cmul(s1, e128); }
        }
        cplx t = cmul(W, e1024);
#pragma unroll
        for (int i = 0; i < 9; ++i) { PUTA(32 + i, t); if (i < 8) t = cmul(t, e128); }
      }
#undef PUT
#undef PUTA
#undef PUTB
      STAMPW(4, 6);
      f32x4 acc[3] = {f32x4{0, 0, 0, 0}, f32x4{0, 0, 0, 0}, f32x4{0, 0, 0, 0}};
      {
        const int r = lane & 15, h = lane >> 4;
#pragma unroll
        for (int ks = 0; ks < 4; ++ks) {
          v4u bf = *(LAS v4u*)(tab + (41 + r) * 256 + (((4 * ks + h) ^ ((41 + r) & 15)) << 4));
#pragma unroll
          for (int t = 0; t < 3; ++t) {
            v4u af = *(LAS v4u*)(tab + (16 * t + r) * 256 + (((4 * ks + h) ^ r) << 4));
            acc[t] = mfma_h(af, bf, acc[t]);
          }
        }
      }
      LAS float* kd = (LAS float*)tab;
      {
        const int j = lane & 15, q = lane >> 4;
#pragma unroll
        for (int t = 0; t < 3; ++t)
#pragma unroll
          for (int e = 0; e < 4; ++e) kd[(16 * t + 4 * q + e) * 16 + j] = acc[t][e] * smax;
      }
      if (lane == 0) smx[wv - 4] = smax;
      lds_arrive_wait(cnt + cl, 2, lane);
      const int t2 = half * 64 + lane;
      LAS float* k0 = (LAS float*)chan;
      LAS float* k1 = (LAS float*)(chan + CV_TAB);
      const float sm = fmaxf(smx[2 * cl], smx[2 * cl + 1]);
      const float ksc = sm * (182.f / 1024.f);
      const float kinv = sm > 0.f ? 1.0f / ksc : 0.f;
      LAS unsigned short* kc = (LAS unsigned short*)(chan + CV_KCT);
#pragma unroll
      for (int i = 0; i < 6; ++i) {
        int idx = t2 + 128 * i;
        float v = (idx < 656) ? (k0[idx] + k1[idx]) * kinv : 0.f;
        kc[idx] = (unsigned short)(cvtpkh(v, v) & 0xFFFFu);
      }
      if (t2 == 0) kscs[cl] = ksc;
      lds_arrive_wait(cnt + cl, 4, lane);
      for (int p = t2; p < 260; p += 128) {
        v4u id = *(const GAS v4u*)(kIdxTab.v + p * 8);
        v4u o;
#pragma unroll
        for (int j = 0; j < 4; ++j) o[j] = (unsigned)kc[id[j] & 0xFFFFu] | ((unsigned)kc[id[j] >> 16] << 16);
        *(LAS v4u*)(chan + p * 16) = o;
      }
      lds_arrive_wait(cnt + cl, 6, lane);
      for (int z = t2; z < 259; z += 128) {
        v4u a = *(LAS v4u*)(chan + z * 16);
        v2u bb = *(LAS v2u*)(chan + z * 16 + 16);
        unsigned d[6] = {a.x, a.y, a.z, a.w, bb.x, bb.y};
        LAS unsigned char* base = chan + z * 16;
        *(LAS v4u*)(base + CV_KC) = v4u{__builtin_amdgcn_alignbit(d[1], d[0], 16), __builtin_amdgcn_alignbit(d[2], d[1], 16), __builtin_amdgcn_alignbit(d[3], d[2], 16), __builtin_amdgcn_alignbit(d[4], d[3], 16)};
        *(LAS v4u*)(base + 2 * CV_KC) = v4u{d[1], d[2], d[3], d[4]};
        *(LAS v4u*)(base + 3 * CV_KC) = v4u{__builtin_amdgcn_alignbit(d[2], d[1], 16), __builtin_amdgcn_alignbit(d[3], d[2], 16), __builtin_amdgcn_alignbit(d[4], d[3], 16), __builtin_amdgcn_alignbit(d[5], d[4], 16)};
      }
      STAMPW(4, 7);
    }
    __syncthreads();
    STAMP(8);
    const int cl = wv & 3, kh = (wv >> 2) & 1;
    f32x4 acc[8];
    if (wv < 8) {
#pragma unroll
      for (int t = 0; t < 8; ++t) acc[t] = f32x4{0, 0, 0, 0};
      const int bb = lane & 15, h = lane >> 4;
      const int cpy = 3 - (bb & 3);
      LAS unsigned char* abase = lds + cl * CV_CH + cpy * CV_KC + (2047 - 16 - bb + 8 * h - cpy) * 2 - 64 * 4 * kh;
      LAS unsigned char* bbase = lds + CV_U + cl * CV_UCH + 512 + (16 * (bb - 1) + 8 * h) * 2 - 64 * 4 * kh;
#pragma unroll 2
      for (int rho = 0; rho < 4; ++rho) {
        v4u A[8], B[8];
#pragma unroll
        for (int i = 0; i < 8; ++i) {
          B[i] = *(LAS v4u*)(bbase + 32 * (16 * i - 2 * rho));
          LAS unsigned char* pa = abase - 64 * (rho + 8 * i);
          v2u lo = *(LAS v2u*)pa, hi = *(LAS v2u*)(pa + 8);
          A[i] = v4u{lo.x, lo.y, hi.x, hi.y};
        }
#pragma unroll
        for (int i = 0; i < 8; ++i)
#pragma unroll
          for (int m = 0; m + i < 8; ++m) acc[i + m] = mfma_h(A[i], B[m], acc[i + m]);
      }
      LAS unsigned char* xo = lds + cl * CV_CH + CV_XCH + kh * 4096 + lane * 16;
#pragma unroll
      for (int t = 0; t < 4; ++t) {
        f32x4 lo = acc[t], hi = acc[4 + t];
        asm volatile("" : "+v"(lo), "+v"(hi));
        *(LAS f32x4*)(xo + t * 1024) = kh ? lo : hi;
        acc[t] = kh ? hi : lo;
      }
    }
    STAMP(9);
    __syncthreads();
    if (wv < 8) {
      const int ch = c0 + cl;
      const float ksc = kscs[cl];
      const float dsk = bf2f(dskip[ch]);
      const int a = lane & 15, q = lane >> 4;
      LAS unsigned char* xi = lds + cl * CV_CH + CV_XCH + (kh ^ 1) * 4096 + lane * 16;
      unsigned pk[4][2];
#pragma unroll
      for (int t = 0; t < 4; ++t) {
        const f32x4 o = *(LAS f32x4*)(xi + t * 1024);
        const f32x4 y4 = acc[t] + o;
        const int tok = 256 * (4 * kh + t) + 16 * a + 4 * q;
        v2u uu = *(LAS v2u*)(lds + CV_U + cl * CV_UCH + 512 + tok * 2);
        v2u xx = *(LAS v2u*)(lds + CV_X + cl * 4096 + tok * 2);
        float hv[4];
#pragma unroll
        for (int e = 0; e < 4; ++e) {
          unsigned uw = e < 2 ? uu.x : uu.y, xw = e < 2 ? xx.x : xx.y;
          h16x2 u2 = __builtin_bit_cast(h16x2, uw);
          float u = (float)u2[e & 1];
          float xv = (e & 1) ? bfhi(xw) : bflo(xw);
          float y = bfr(y4[e] * ksc);
          float tt = bfr(y + bfr(u * dsk));
          hv[e] = xv + tt;
        }
        pk[t][0] = cvtpk(hv[0], hv[1]);
        pk[t][1] = cvtpk(hv[2], hv[3]);
      }
#pragma unroll
      for (int t = 0; t < 4; t += 2) {
        auto s0 = __builtin_amdgcn_permlane16_swap(pk[t][0], pk[t + 1][0], false, false);
        auto s1 = __builtin_amdgcn_permlane16_swap(pk[t][1], pk[t + 1][1], false, false);
        const int tok = 256 * (4 * kh + t + (q & 1)) + 16 * a + 8 * (q >> 1);
        st16(rs, v4u{s0[0], s1[0], s0[1], s1[1]}, WS_HT + (ch * L + tok) * 2);
      }
      DRAIN();
    }
    __syncthreads();
    if (tid < 8) __hip_atomic_store((gu64*)(ws + WS_F2) + tid * 256 + (18 * xcd + jj), tag, RLX);
    STAMP(10);
  }

  const int tt = 8 * xcd + jj / 3, sl = jj % 3;
  if (plo <= 2 && phi > 2 && is_gemm) {
    const int ct = 12 * sl + wv;
    if (wv == 0) wait_flags(ws, WS_FPT + (b & 3) * 2048 + 3 * sl * 8, 3, tag, lane);
    __syncthreads();
    v4u bw[18];
    if (wv != 0) {
#pragma unroll
      for (int ks = 0; ks < 18; ++ks) bw[ks] = ld16(rs, lane * 16, WS_W1T + (ct * 18 + ks) * 1024);
    }
    const f32x4 cs = __builtin_bit_cast(f32x4, ld16(rs, WS_CS1 + (ct * 16 + 4 * (lane >> 4)) * 4));
    if (wv == 0) {
      gu64* p = (gu64*)(ws + WS_F2 + (b & 7) * 2048);
      for (unsigned spins = 0; spins < SPIN_CAP; ++spins) {
        u64 a0 = __hip_atomic_load(p + lane, RLX), a1 = __hip_atomic_load(p + 64 + lane, RLX), a2 = __hip_atomic_load(p + 128 + (lane & 15), RLX);
        if (__all(a0 == tag && a1 == tag && a2 == tag)) break;
      }
      ORDER();
#pragma unroll
      for (int ks = 0; ks < 18; ++ks) bw[ks] = ld16(rs, lane * 16, WS_W1T + (ct * 18 + ks) * 1024);
    }
    __syncthreads();
    STAMP(11);
    {
      v4u a[3];
#pragma unroll
      for (int i = 0; i < 3; ++i) {
        int p = tid + NT * i, c = p >> 2, pc = p & 3;
        a[i] = ld16(rs, WS_HT + (c * L + tt * 32 + pc * 8) * 2);
      }
#pragma unroll
      for (int i = 0; i < 3; ++i) {
        int p = tid + NT * i, c = p >> 2, pc = p & 3;
        *(LAS v4u*)(lds + G_A + c * 64 + ((((pc >> 1) ^ ((c >> 3) & 1)) << 1 | (pc & 1)) << 4)) = a[i];
      }
    }
    __syncthreads();
    STAMP(12);
    f32x4 acc[2] = {f32x4{0, 0, 0, 0}, f32x4{0, 0, 0, 0}};
    {
      float ps[2] = {0.f, 0.f}, pq[2] = {0.f, 0.f};
      const int gq = lane >> 4, q = (lane & 15) >> 2, p = lane & 3;
      const unsigned ones = 0x3F803F80u;
      LAS unsigned char* pa0 = lds + G_A + (8 * gq + q) * 64 + p * 8;
#pragma unroll
      for (int ks = 0; ks < 18; ++ks) {
#pragma unroll
        for (int r = 0; r < 2; ++r) {
          LAS unsigned char* pa = pa0 + ks * 2048 + ((r ^ (gq & 1)) << 5);
          v2u lo = tr_read(pa), hi = tr_read(pa + 4 * 64);
          v4u af = {lo.x, lo.y, hi.x, hi.y};
          acc[r] = mfma_bf(bw[ks], af, acc[r]);
          if ((2 * ks + r) % 12 == wv) {
            ps[r] = dot2bf(af.x, ones, ps[r]); ps[r] = dot2bf(af.y, ones, ps[r]); ps[r] = dot2bf(af.z, ones, ps[r]); ps[r] = dot2bf(af.w, ones, ps[r]);
            pq[r] = dot2bf(af.x, af.x, pq[r]); pq[r] = dot2bf(af.y, af.y, pq[r]); pq[r] = dot2bf(af.z, af.z, pq[r]); pq[r] = dot2bf(af.w, af.w, pq[r]);
          }
        }
      }
#pragma unroll
      for (int r = 0; r < 2; ++r) {
        ps[r] += __shfl_xor(ps[r], 16); ps[r] += __shfl_xor(ps[r], 32);
        pq[r] += __shfl_xor(pq[r], 16); pq[r] += __shfl_xor(pq[r], 32);
        if (lane < 16) *(LAS f32x2*)(lds + G_ST + (wv * 32 + r * 16 + lane) * 8) = f32x2{ps[r], pq[r]};
      }
    }
    __syncthreads();
    STAMP(13);
    {
      const int tok = lane & 15, q = lane >> 4;
#pragma unroll
      for (int r = 0; r < 2; ++r) {
        float s = 0.f, ss = 0.f;
#pragma unroll
        for (int w = 0; w < 12; ++w) { f32x2 v = *(LAS f32x2*)(lds + G_ST + (w * 32 + r * 16 + tok) * 8); s += v.x; ss += v.y; }
        float m1, s1;
        ln_scalars(s, ss, m1, s1);
        const float mean = s * (1.0f / D);
        float var = __builtin_fmaf(-mean, mean, ss * (1.0f / D));
        var = var < 0.f ? 0.f : var;
        const float r1 = 1.0f / s1;
        const float m2 = bfr((mean - m1) * r1);
        const float s2 = bfr(__builtin_sqrtf(bfr(bfr(var * r1 * r1) + 1e-5f)));
        const float R = r1 / s2, me = __builtin_fmaf(m2, s1, m1);
        float v[4];
#pragma unroll
        for (int e = 0; e < 4; ++e) v[e] = gelu_f(bfr((acc[r][e] - me * cs[e]) * R));
        *(LAS v2u*)(lds + G_O + (r * 16 + tok) * G_OROW + wv * 32 + q * 8) = v2u{cvtpk(v[0], v[1]), cvtpk(v[2], v[3])};
        if (sl == 0 && wv == 0 && q == 0) st8(rs, v2u{__builtin_bit_cast(unsigned, m1), __builtin_bit_cast(unsigned, r1)}, WS_ST2 + (tt * 32 + r * 16 + tok) * 8);
      }
    }
    __syncthreads();
    {
      const int r = tid / 24, c = tid % 24;
      st16(rs, *(LAS v4u*)(lds + G_O + r * G_OROW + c * 16), WS_H1 + ((tt * 32 + r) * D + sl * 192 + c * 8) * 2);
    }
    DRAIN();
    __syncthreads();
    if (tid == 0) __hip_atomic_store((gu64*)(ws + WS_F3) + tt * 4 + sl, tag, RLX);
    STAMP(14);
  }

  if (plo <= 3 && phi > 3 && is_gemm) {
    const int ct = 12 * sl + wv;
    if (wv == 0) wait_flags(ws, WS_FPT + (b & 3) * 2048 + (9 + 3 * sl) * 8, 3, tag, lane);
    __syncthreads();
    v4u bw[18];
    if (wv != 0) {
#pragma unroll
      for (int ks = 0; ks < 18; ++ks) bw[ks] = ld16(rs, lane * 16, WS_W2T + (ct * 18 + ks) * 1024);
    }
    v4u hpiece = ld16(rs, WS_HT + ((ct * 16 + (lane >> 2)) * L + tt * 32 + (lane & 3) * 8) * 2);
    if (wv == 0) {
      int t = tt - 1 + (lane < 9 ? lane / 3 : 1);
      t = t < 0 ? 0 : (t > 63 ? 63 : t);
      gu64* p = (gu64*)(ws + WS_F3) + t * 4 + (lane < 9 ? lane % 3 : 0);
      for (unsigned spins = 0; spins < SPIN_CAP; ++spins) {
        if (__all(__hip_atomic_load(p, RLX) == tag)) break;
      }
      ORDER();
#pragma unroll
      for (int ks = 0; ks < 18; ++ks) bw[ks] = ld16(rs, lane * 16, WS_W2T + (ct * 18 + ks) * 1024);
    }
    __syncthreads();
    STAMP(15);
    const int tok = lane & 15, q = lane >> 4;
    v2u st[2];
    {
      v4u a[3];
#pragma unroll
      for (int i = 0; i < 3; ++i) {
        int p = tid + NT * i, r = p / 72, c = p % 72;
        int grp = c / 18;
        int sh = grp == 0 ? -1 : (grp == 1 ? 1 : (grp == 2 ? -2 : 2));
        int t = tt * 32 + r + sh;
        a[i] = ld16(rs, (t >= 0 && t < L) ? WS_H1 + (t * D + c * 8) * 2 : WS_END);
      }
#pragma unroll
      for (int r = 0; r < 2; ++r) st[r] = ld8(rs, WS_ST2 + (tt * 32 + r * 16 + tok) * 8);
#pragma unroll
      for (int i = 0; i < 3; ++i) {
        int p = tid + NT * i, r = p / 72, c = p % 72;
        *(LAS v4u*)(lds + G_A + r * ROWB + ((c ^ ((r >> 1) & 7)) << 4)) = a[i];
      }
      const int c = lane >> 2, pc = lane & 3;
      *(LAS v4u*)(lds + G_H + wv * 1024 + c * 64 + ((((pc >> 1) ^ ((c >> 3) & 1)) << 1 | (pc & 1)) << 4)) = hpiece;
    }
    __syncthreads();
    STAMP(16);
    f32x4 acc[2] = {f32x4{0, 0, 0, 0}, f32x4{0, 0, 0, 0}};
    {
      const int sw = (tok >> 1) & 7;
#pragma unroll
      for (int ks = 0; ks < 18; ++ks) {
#pragma unroll
        for (int r = 0; r < 2; ++r) {
          v4u af = *(LAS v4u*)(lds + G_A + (r * 16 + tok) * ROWB + (((4 * ks + q) ^ sw) << 4));
          acc[r] = mfma_bf(bw[ks], af, acc[r]);
        }
      }
    }
    STAMP(17);
#pragma unroll
    for (int r = 0; r < 2; ++r) {
      const int c = 4 * q + ((lane & 15) >> 2);
      v2u hh = tr_read(lds + G_H + wv * 1024 + c * 64 + ((r ^ ((c >> 3) & 1)) << 5) + (lane & 3) * 8);
      const float m1 = asf(st[r].x), r1 = asf(st[r].y);
      float v[4];
#pragma unroll
      for (int e = 0; e < 4; ++e) {
        unsigned hw = e < 2 ? hh.x : hh.y;
        float h = (e & 1) ? bfhi(hw) : bflo(hw);
        float z = bfr(bfr(h - m1) * r1);
        v[e] = z + bfr(acc[r][e]);
      }
      *(LAS v2u*)(lds + G_O + (r * 16 + tok) * G_OROW + wv * 32 + q * 8) = v2u{cvtpk(v[0], v[1]), cvtpk(v[2], v[3])};
      *(LAS v2u*)(lds + G_O + 32 * G_OROW + (r * 16 + tok) * G_OROW + wv * 32 + q * 8) = hh;
    }
    __syncthreads();
    {
      const int r = tid / 24, c = tid % 24;
      const int off = ((tt * 32 + r) * D + sl * 192 + c * 8) * 2;
      st16(rs, *(LAS v4u*)(lds + G_O + r * G_OROW + c * 16), WS_X1 + off);
      st16(rs, *(LAS v4u*)(lds + G_O + 32 * G_OROW + r * G_OROW + c * 16), WS_HR + off);
    }
    DRAIN();
    __syncthreads();
    if (tid == 0) __hip_atomic_store((gu64*)(ws + WS_F4) + tt * 4 + sl, tag, RLX);
    STAMP(18);
  }

  const int grp = 2 * (jj >> 1) + (xcd >> 2), s = 2 * (xcd & 3) + (jj & 1);
  if (plo <= 4 && phi > 4) {
    constexpr int DEPTH = 3;
    if (wv == 0) {
      gu64* p = (gu64*)(ws + WS_FPT + (b & 3) * 2048) + (lane < 9 ? 18 + 9 * s + lane : (lane < 18 ? 90 + 9 * s + lane - 9 : 18 + 9 * s));
      for (unsigned spins = 0; spins < SPIN_CAP; ++spins) {
        if (__all(__hip_atomic_load(p, RLX) == tag)) break;
      }
      ORDER();
    }
    __syncthreads();
    const int o3 = WS_W3T + (36 * s + 3 * wv) * 18 * 1024, o4 = WS_W4T + (3 * wv * 144 + 18 * s) * 1024;
    v4u bw[DEPTH][3];
    if (wv != 0) {
#pragma unroll
      for (int d = 0; d < DEPTH; ++d)
#pragma unroll
        for (int c = 0; c < 3; ++c) bw[d][c] = ld16(rs, lane * 16, o3 + (c * 18 + d) * 1024);
    }
    if (wv == 0) {
      gu64* p = (gu64*)(ws + WS_F4) + (2 * grp + (lane < 6 ? lane / 3 : 0)) * 4 + (lane < 6 ? lane % 3 : 0);
      for (unsigned spins = 0; spins < SPIN_CAP; ++spins) {
        if (__all(__hip_atomic_load(p, RLX) == tag)) break;
      }
      ORDER();
#pragma unroll
      for (int d = 0; d < DEPTH; ++d)
#pragma unroll
        for (int c = 0; c < 3; ++c) bw[d][c] = ld16(rs, lane * 16, o3 + (c * 18 + d) * 1024);
    }
    __syncthreads();
    STAMP(19);
    {
      v4u a[6];
#pragma unroll
      for (int i = 0; i < 6; ++i) a[i] = ld16(rs, WS_X1 + grp * 64 * ROWB + (tid + NT * i) * 16);
#pragma unroll
      for (int i = 0; i < 6; ++i) {
        int p = tid + NT * i, r = p / 72, c = p % 72;
        *(LAS v4u*)(lds + ML_A + r * ROWB + ((c ^ ((r >> 1) & 7)) << 4)) = a[i];
      }
    }
    __syncthreads();
    STAMP(20);
    if (wv >= 4) {
      const int r = (wv - 4) * 8 + (lane >> 3), sub = lane & 7;
      float sm = 0.f, ss = 0.f;
      const unsigned ones = 0x3F803F80u;
#pragma unroll
      for (int i = 0; i < 9; ++i) {
        v4u a = *(LAS v4u*)(lds + ML_A + r * ROWB + (sub + 8 * i) * 16);
#pragma unroll
        for (int j = 0; j < 4; ++j) { sm = dot2bf(a[j], ones, sm); ss = dot2bf(a[j], a[j], ss); }
      }
#pragma unroll
      for (int o = 1; o < 8; o <<= 1) { sm += __shfl_xor(sm, o); ss += __shfl_xor(ss, o); }
      float m, sd;
      ln_scalars(sm, ss, m, sd);
      if (sub == 0) *(LAS f32x2*)(lds + ML_ST + r * 8) = f32x2{m, 1.0f / sd};
    }
    const int tok = lane & 15, q = lane >> 4;
    const int sw = (tok >> 1) & 7;
    LAS unsigned char* abase = lds + ML_A + tok * ROWB;
    f32x4 acc[4][3];
#pragma unroll
    for (int r = 0; r < 4; ++r)
#pragma unroll
      for (int c = 0; c < 3; ++c) acc[r][c] = f32x4{0, 0, 0, 0};
#pragma unroll
    for (int ks = 0; ks < 18; ++ks) {
      v4u af[4];
#pragma unroll
      for (int r = 0; r < 4; ++r) af[r] = *(LAS v4u*)(abase + r * 16 * ROWB + (((4 * ks + q) ^ sw) << 4));
      v4u cur[3];
#pragma unroll
      for (int c = 0; c < 3; ++c) {
        cur[c] = bw[ks % DEPTH][c];
        int nk = ks + DEPTH;
        if (nk < 18) bw[ks % DEPTH][c] = ld16(rs, lane * 16, o3 + (c * 18 + nk) * 1024);
        else bw[ks % DEPTH][c] = ld16(rs, lane * 16, o4 + (c * 144 + nk - 18) * 1024);
      }
#pragma unroll
      for (int c = 0; c < 3; ++c)
#pragma unroll
        for (int r = 0; r < 4; ++r) acc[r][c] = mfma_bf(cur[c], af[r], acc[r][c]);
      __builtin_amdgcn_sched_barrier(0);
    }
    STAMP(21);
    __syncthreads();
    STAMP(22);
    LAS unsigned char* H = lds + ML_H;
    {
      f32x2 st[4];
#pragma unroll
      for (int r = 0; r < 4; ++r) st[r] = *(LAS f32x2*)(lds + ML_ST + (r * 16 + tok) * 8);
#pragma unroll
      for (int c = 0; c < 3; ++c) {
        const f32x4 cs = __builtin_bit_cast(f32x4, ld16(rs, WS_CS3 + ((36 * s + 3 * wv + c) * 16 + 4 * q) * 4));
#pragma unroll
        for (int r = 0; r < 4; ++r) {
          float v[4];
#pragma unroll
          for (int e = 0; e < 4; ++e) v[e] = gelu_f((acc[r][c][e] - st[r].x * cs[e]) * st[r].y);
          v2u o = {cvtpk(v[0], v[1]), cvtpk(v[2], v[3])};
          const int chunk = 2 * (3 * wv + c) + (q >> 1);
          *(LAS v2u*)(H + (r * 16 + tok) * ROWB + ((chunk ^ sw) << 4) + (q & 1) * 8) = o;
          acc[r][c] = f32x4{0, 0, 0, 0};
        }
      }
    }
    __syncthreads();
    STAMP(23);
    LAS unsigned char* hbase = H + tok * ROWB;
#pragma unroll
    for (int k2 = 0; k2 < 18; ++k2) {
      const int ks = 18 + k2;
      v4u af[4];
#pragma unroll
      for (int r = 0; r < 4; ++r) af[r] = *(LAS v4u*)(hbase + r * 16 * ROWB + (((4 * k2 + q) ^ sw) << 4));
      v4u cur[3];
#pragma unroll
      for (int c = 0; c < 3; ++c) {
        cur[c] = bw[ks % DEPTH][c];
        int nk = k2 + DEPTH;
        if (nk < 18) bw[ks % DEPTH][c] = ld16(rs, lane * 16, o4 + (c * 144 + nk) * 1024);
      }
#pragma unroll
      for (int c = 0; c < 3; ++c)
#pragma unroll
        for (int r = 0; r < 4; ++r) acc[r][c] = mfma_bf(cur[c], af[r], acc[r][c]);
      __builtin_amdgcn_sched_barrier(0);
    }
    STAMP(24);
    {
      const int po = WS_PART + (s * 32 + grp) * 72 * 1024;
#pragma unroll
      for (int c = 0; c < 3; ++c)
#pragma unroll
        for (int rp = 0; rp < 2; ++rp) {
          unsigned x0 = cvtpk(acc[2 * rp][c][0], acc[2 * rp][c][1]), x1 = cvtpk(acc[2 * rp][c][2], acc[2 * rp][c][3]);
          unsigned y0 = cvtpk(acc[2 * rp + 1][c][0], acc[2 * rp + 1][c][1]), y1 = cvtpk(acc[2 * rp + 1][c][2], acc[2 * rp + 1][c][3]);
          auto s0 = __builtin_amdgcn_permlane16_swap(x0, y0, false, false);
          auto s1 = __builtin_amdgcn_permlane16_swap(x1, y1, false, false);
          st16(rs, v4u{s0[0], s1[0], s0[1], s1[1]}, lane * 16, po + (rp * 36 + 3 * wv + c) * 1024);
        }
    }
    DRAIN();
    __syncthreads();
    if (tid == 0) __hip_atomic_store((gu64*)(ws + WS_F5) + grp * 8 + s, tag, RLX);
    STAMP(25);
  }

  if (plo <= 5 && phi > 5) {
    if (wv == 0) wait_flags(ws, WS_F5 + grp * 64, 8, tag, lane);
    __syncthreads();
    STAMP(26);
    if (wv < 9) {
      const int rp = s & 1, oc = 9 * (s >> 1) + wv, u = rp * 36 + oc;
      const int q = lane >> 4, tok = lane & 15;
      const int off = ((grp * 64 + rp * 32 + (q & 1) * 16 + tok) * D + oc * 16 + 8 * (q >> 1)) * 2;
      v4u p[8];
#pragma unroll
      for (int k = 0; k < 8; ++k) p[k] = ld16(rs, lane * 16, WS_PART + ((k * 32 + grp) * 72 + u) * 1024);
      const v4u xv = ld16(rs, WS_X1 + off), hv = ld16(rs, WS_HR + off);
      v4u o;
#pragma unroll
      for (int j = 0; j < 4; ++j) {
        float lo = 0.f, hi = 0.f;
#pragma unroll
        for (int k = 0; k < 8; ++k) { lo += bflo(p[k][j]); hi += bfhi(p[k][j]); }
        lo = bflo(hv[j]) + bfr(bflo(xv[j]) + bfr(lo));
        hi = bfhi(hv[j]) + bfr(bfhi(xv[j]) + bfr(hi));
        o[j] = cvtpk(lo, hi);
      }
      *(GAS v4u*)((GAS unsigned char*)out + off) = o;
    }
    if (b == 0 && tid == 0) __hip_atomic_store((gu64*)(ws + WS_CTRL), tag, RLX);
    STAMP(27);
  }
}

extern "C" void kernel_launch(void* const* d_in, const int* in_sizes, int n_in, void* d_out, int out_size, void* d_ws, size_t ws_size,
                              hipStream_t stream) {
  (void)hipFuncSetAttribute((const void*)k_all, hipFuncAttributeMaxDynamicSharedMemorySize, LDS_BYTES);
  const unsigned short* const* in = (const unsigned short* const*)d_in;
  k_all<<<256, NT, LDS_BYTES, stream>>>(in[0], in[1], in[2], in[3], in[4], in[5], in[6], in[7], in[8], in[10], in[11], in[12], in[13], (unsigned char*)d_ws,
                                        (unsigned short*)d_out, 0, 6);
}
```

```cpp
#include <hip/hip_runtime.h>
#include <hip/hip_bf16.h>
#include <stdint.h>

typedef unsigned long long u64;
typedef unsigned v4u __attribute__((ext_vector_type(4)));
typedef unsigned v2u __attribute__((ext_vector_type(2)));
typedef float f32x4 __attribute__((ext_vector_type(4)));
typedef float f32x2 __attribute__((ext_vector_type(2)));
typedef short s16x8 __attribute__((ext_vector_type(8)));
typedef _Float16 h16x8 __attribute__((ext_vector_type(8)));
typedef _Float16 h16x2 __attribute__((ext_vector_type(2)));
typedef __bf16 bf16x2_t __attribute__((ext_vector_type(2)));
#define GAS __attribute__((address_space(1)))
#define LAS __attribute__((address_space(3)))
#define DI __device__ __forceinline__
#define RLX __ATOMIC_RELAXED, __HIP_MEMORY_SCOPE_AGENT
typedef GAS u64 gu64;
typedef __amdgpu_buffer_rsrc_t rsrc_t;

#define NLAUNCH 1

constexpr int L = 2048, D = 576, NS = 128, F = 4608;
constexpr int NT = 768;

constexpr int WS_CTRL = 0;
constexpr int WS_F1 = 4096;
constexpr int WS_FPT = WS_F1 + 8 * 2048;
constexpr int WS_F2 = WS_FPT + 4 * 2048;
constexpr int WS_F3 = WS_F2 + 8 * 2048;
constexpr int WS_F4 = WS_F3 + 32 * 64;
constexpr int WS_F5 = WS_F4 + 32 * 64;
constexpr int WS_W1T = 131072;
constexpr int WS_W2T = WS_W1T + 36 * 18 * 1024;
constexpr int WS_W3T = WS_W2T + 36 * 18 * 1024;
constexpr int WS_W4T = WS_W3T + 288 * 18 * 1024;
constexpr int WS_CS1 = WS_W4T + 36 * 144 * 1024;
constexpr int WS_CS3 = WS_CS1 + 4096;
constexpr int WS_HT = WS_CS3 + 4608 * 4;
constexpr int WS_H1 = WS_HT + D * L * 2;
constexpr int WS_ST2 = WS_H1 + D * L * 2;
constexpr int WS_X1 = WS_ST2 + 2048 * 8;
constexpr int WS_HR = WS_X1 + D * L * 2;
constexpr int WS_PART = WS_HR + D * L * 2;
constexpr int WS_UX = WS_PART + 8 * D * L * 2;
constexpr int WS_END = WS_UX + 256 * 144 * 128;
static_assert(WS_F5 + 32 * 64 <= WS_W1T, "control block");

#define STAMP(i)
#define STAMPW(w, i)
#define STAMPC(i)

DI float bf2f(unsigned short b) { return __builtin_bit_cast(float, (unsigned)b << 16); }
DI float asf(unsigned u) { return __builtin_bit_cast(float, u); }
DI unsigned asu(float f) { return __builtin_bit_cast(unsigned, f); }
DI float bflo(unsigned u) { return __builtin_bit_cast(float, u << 16); }
DI float bfhi(unsigned u) { return __builtin_bit_cast(float, u & 0xFFFF0000u); }
DI unsigned cvtpk(float lo, float hi) {
  f32x2 v = {lo, hi};
  bf16x2_t b = __builtin_convertvector(v, bf16x2_t);
  return __builtin_bit_cast(unsigned, b);
}
DI float bfr(float x) { return bflo(cvtpk(x, x)); }
DI unsigned cvtpkh(float lo, float hi) {
  f32x2 v = {lo, hi};
  h16x2 b = __builtin_convertvector(v, h16x2);
  return __builtin_bit_cast(unsigned, b);
}
DI f32x4 mfma_bf(v4u a, v4u b, f32x4 c) {
  return __builtin_amdgcn_mfma_f32_16x16x32_bf16(__builtin_bit_cast(s16x8, a), __builtin_bit_cast(s16x8, b), c, 0, 0, 0);
}
DI f32x4 mfma_h(v4u a, v4u b, f32x4 c) {
  return __builtin_amdgcn_mfma_f32_16x16x32_f16(__builtin_bit_cast(h16x8, a), __builtin_bit_cast(h16x8, b), c, 0, 0, 0);
}
DI float dot2bf(unsigned a, unsigned b, float acc) {
  return __builtin_amdgcn_fdot2_f32_bf16(__builtin_bit_cast(bf16x2_t, a), __builtin_bit_cast(bf16x2_t, b), acc, false);
}
DI v2u tr_read(LAS unsigned char* p) {
  typedef short v4i16_t __attribute__((ext_vector_type(4)));
  return __builtin_bit_cast(v2u, __builtin_amdgcn_ds_read_tr16_b64_v4i16((LAS v4i16_t*)p));
}
DI float gelu_f(float x) {
  const float c0 = -2.3022081986f, c1 = -0.10294324f;
  float x2 = x * x;
  float p = __builtin_fmaf(x2, c1, c0);
  float e = __builtin_amdgcn_exp2f(x * p);
  return x * __builtin_amdgcn_rcpf(1.0f + e);
}
DI void ln_scalars(float sum, float sumsq, float& m, float& s) {
  float mean = sum * (1.0f / D);
  float var = __builtin_fmaf(-mean, mean, sumsq * (1.0f / D));
  var = var < 0.f ? 0.f : var;
  m = bfr(mean);
  s = bfr(__builtin_amdgcn_sqrtf(bfr(bfr(var) + 1e-5f)));
}
constexpr int SC1 = 16;
DI v4u ld16(rsrc_t rs, int voff, int soff = 0) { return __builtin_amdgcn_raw_buffer_load_b128(rs, voff, soff, SC1); }
#define WAUX 16
DI v4u ldw(rsrc_t rs, int voff, int soff = 0) { return __builtin_amdgcn_raw_buffer_load_b128(rs, voff, soff, WAUX); }
DI v2u ld8(rsrc_t rs, int voff, int soff = 0) { return __builtin_amdgcn_raw_buffer_load_b64(rs, voff, soff, SC1); }
DI void st16(rsrc_t rs, v4u v, int voff, int soff = 0) { __builtin_amdgcn_raw_buffer_store_b128(v, rs, voff, soff, SC1); }
DI void st8(rsrc_t rs, v2u v, int voff, int soff = 0) { __builtin_amdgcn_raw_buffer_store_b64(v, rs, voff, soff, SC1); }
DI void st4(rsrc_t rs, unsigned v, int voff, int soff = 0) { __builtin_amdgcn_raw_buffer_store_b32(v, rs, voff, soff, SC1); }
#define DRAIN() asm volatile("s_waitcnt vmcnt(0)" ::: "memory")
#define ORDER() __builtin_amdgcn_fence(__ATOMIC_ACQUIRE, "wavefront")
constexpr unsigned SPIN_CAP = 1u << 15;

DI void wait_flags(unsigned char* ws, int off, int n, u64 tag, int lane) {
  gu64* p = (gu64*)(ws + off) + (lane < n ? lane : 0);
  for (unsigned spins = 0; spins < SPIN_CAP; ++spins) {
    if (__all(__hip_atomic_load(p, RLX) == tag)) break;
  }
  ORDER();
}

struct KIdx { unsigned short v[2080]; };
constexpr KIdx make_kidx() {
  KIdx t{};
  for (int y = 0; y < 2080; ++y) {
    if (y >= 2048) { t.v[y] = 767; continue; }
    int j = 2047 - y;
    int v = j;
    if (j >= 256) {
      int sh = j >= 1024 ? 3 : (j >= 512 ? 2 : 1);
      int step = 1 << sh, lo = (j >> sh) << sh, rem = j - lo, half = step >> 1;
      if (rem > half || (rem == half && (((lo >> sh) & 1) != 0))) lo += step;
      v = lo;
    }
    int seg = v >= 1024 ? 3 : (v >= 512 ? 2 : (v >= 256 ? 1 : 0));
    int u = seg == 0 ? v : v - (128 << seg);
    int rb = seg == 0 ? 0 : (seg == 1 ? 16 : (seg == 2 ? 24 : 32));
    int row = rb + ((u & 7) >> seg) + (u >> 7) * (8 >> seg);
    int col = (u >> 3) & 15;
    t.v[y] = (unsigned short)(row * 16 + col);
  }
  return t;
}
__device__ const KIdx kIdxTab = make_kidx();

struct cplx { float r, i; };
DI cplx cmul(cplx a, cplx b) { return cplx{__builtin_fmaf(a.r, b.r, -a.i * b.i), __builtin_fmaf(a.r, b.i, a.i * b.r)}; }
DI cplx cexp_r(float a2, float th, float r) {
  float m = __builtin_amdgcn_exp2f(a2 * r);
  float ang = __builtin_amdgcn_fractf(th * r);
  return cplx{m * __builtin_amdgcn_cosf(ang), m * __builtin_amdgcn_sinf(ang)};
}

constexpr int CV_TAB = 57 * 256;
constexpr int CV_CH = 2 * CV_TAB;
constexpr int CV_KC = 4160;
constexpr int CV_XCH = 4 * CV_KC;
constexpr int CV_KCT = 4 * CV_KC + 8192;
constexpr int CV_U = 4 * CV_CH;
constexpr int CV_UCH = (256 + 2048) * 2;
constexpr int CV_X = CV_U + 4 * CV_UCH;
constexpr int CV_MISC = CV_X + 4 * 4096;
static_assert(CV_KCT + 1536 <= CV_CH, "conv channel region");
constexpr int PR_ROW = 1168;
constexpr int PR_U = CV_U, PR_X = CV_U + 8 * PR_ROW;
constexpr int ROWB = 1152;
constexpr int G_A = 0;
constexpr int G_ST = 73728;
constexpr int G_X = G_ST + 4096;
constexpr int G_H = G_X + 12 * 2048;
constexpr int G_O = G_H + 12 * 1024;
constexpr int G_OROW = 208;
constexpr int ML_A = 0, ML_H = 64 * ROWB, ML_ST = 2 * 64 * ROWB;
constexpr int LDS_BYTES = 152 * 1024;
static_assert(CV_MISC + 128 <= LDS_BYTES && ML_ST + 528 <= LDS_BYTES && G_O + 2 * 64 * G_OROW <= LDS_BYTES && PR_X + 8 * PR_ROW <= CV_MISC, "LDS");

DI void lds_arrive_wait(LAS unsigned* ctr, unsigned target, int lane) {
  asm volatile("s_waitcnt lgkmcnt(0)" ::: "memory");
  if (lane == 0) __hip_atomic_fetch_add(ctr, 1u, __ATOMIC_RELAXED, __HIP_MEMORY_SCOPE_WORKGROUP);
  while (__hip_atomic_load(ctr, __ATOMIC_RELAXED, __HIP_MEMORY_SCOPE_WORKGROUP) < target) __builtin_amdgcn_s_sleep(1);
  asm volatile("" ::: "memory");
}

DI void lds_arrive(LAS unsigned* ctr, int lane) {
  asm volatile("s_waitcnt lgkmcnt(0)" ::: "memory");
  if (lane == 0) __hip_atomic_fetch_add(ctr, 1u, __ATOMIC_RELAXED, __HIP_MEMORY_SCOPE_WORKGROUP);
  asm volatile("" ::: "memory");
}
DI void lds_wait(LAS unsigned* ctr, unsigned target) {
  while (__hip_atomic_load(ctr, __ATOMIC_RELAXED, __HIP_MEMORY_SCOPE_WORKGROUP) < target) __builtin_amdgcn_s_sleep(1);
  asm volatile("" ::: "memory");
}
DI void pretile_unit(LAS unsigned char* lds, rsrc_t rs, unsigned char* ws, int unit, u64 tag, const unsigned short* __restrict__ w1, const unsigned short* __restrict__ w2,
                     const unsigned short* __restrict__ w3, const unsigned short* __restrict__ w4) {
  const int tid = threadIdx.x, lane = tid & 63, wv = __builtin_amdgcn_readfirstlane(tid >> 6);
  const unsigned short* w;
  int ldw, dst, ksn, cs = -1;
  if (unit < 9) { w = w1 + unit * 64; ldw = D; dst = WS_W1T + unit * 4 * 18 * 1024; ksn = 18; cs = WS_CS1 + unit * 256; }
  else if (unit < 18) { w = w2 + (unit - 9) * 64; ldw = D; dst = WS_W2T + (unit - 9) * 4 * 18 * 1024; ksn = 18; }
  else if (unit < 90) { w = w3 + (unit - 18) * 64; ldw = F; dst = WS_W3T + (unit - 18) * 4 * 18 * 1024; ksn = 18; cs = WS_CS3 + (unit - 18) * 256; }
  else { int u = unit - 90, kc = u / 9, cg = u % 9; w = w4 + (size_t)kc * 576 * D + cg * 64; ldw = D; dst = WS_W4T + (cg * 4 * 144 + kc * 18) * 1024; ksn = 144; }
  v4u a[6];
#pragma unroll
  for (int i = 0; i < 6; ++i) {
    int p = tid + NT * i, r = p >> 3, c = p & 7;
    a[i] = *(const GAS v4u*)(w + (size_t)r * ldw + c * 8);
  }
  __syncthreads();
#pragma unroll
  for (int i = 0; i < 6; ++i) {
    int p = tid + NT * i, r = p >> 3, c = p & 7;
    int f = ((r >> 1) & 1) | (((r >> 3) & 1) << 1);
    *(LAS v4u*)(lds + r * 128 + ((((c >> 1) ^ f) << 1 | (c & 1)) << 4)) = a[i];
  }
  __syncthreads();
  const int ct = wv & 3, k0 = 6 * (wv >> 2);
  const int g = lane >> 4, q = (lane & 15) >> 2, p = lane & 3;
  const int f = ((q >> 1) & 1) | ((g & 1) << 1);
  float csum = 0.f;
  const unsigned ones = 0x3F803F80u;
#pragma unroll
  for (int ks = 0; ks < 6; ++ks) {
    LAS unsigned char* pa = lds + (32 * (k0 + ks) + 8 * g + q) * 128 + ((ct ^ f) << 5) + p * 8;
    v2u lo = tr_read(pa), hi = tr_read(pa + 4 * 128);
    v4u o = {lo.x, lo.y, hi.x, hi.y};
    st16(rs, o, lane * 16, dst + (ct * ksn + k0 + ks) * 1024);
    csum = dot2bf(o.x, ones, csum); csum = dot2bf(o.y, ones, csum); csum = dot2bf(o.z, ones, csum); csum = dot2bf(o.w, ones, csum);
  }
  if (cs >= 0) {
    csum += __shfl_xor(csum, 16);
    csum += __shfl_xor(csum, 32);
    LAS float* pc = (LAS float*)(lds + 73728);
    if (lane < 16) pc[wv * 16 + lane] = csum;
    __syncthreads();
    if (tid < 64) st4(rs, __builtin_bit_cast(unsigned, pc[tid] + pc[64 + tid] + pc[128 + tid]), cs + tid * 4);
  }
  DRAIN();
  __syncthreads();
  if (tid < 4) __hip_atomic_store((gu64*)(ws + WS_FPT) + tid * 256 + unit, tag, RLX);
}

__global__ __launch_bounds__(NT, 3) void k_all(const unsigned short* __restrict__ x, const unsigned short* __restrict__ Are, const unsigned short* __restrict__ Aim,
                                               const unsigned short* __restrict__ Bre, const unsigned short* __restrict__ Bim, const unsigned short* __restrict__ Cre,
                                               const unsigned short* __restrict__ Cim, const unsigned short* __restrict__ logdt, const unsigned short* __restrict__ dskip,
                                               const unsigned short* __restrict__ w1, const unsigned short* __restrict__ w2, const unsigned short* __restrict__ w3,
                                               const unsigned short* __restrict__ w4, unsigned char* __restrict__ ws, unsigned short* __restrict__ out) {
  extern __shared__ __attribute__((aligned(16))) unsigned char smem[];
  LAS unsigned char* lds = (LAS unsigned char*)smem;
  const int tid = threadIdx.x, lane = tid & 63, wv = __builtin_amdgcn_readfirstlane(tid >> 6);
  const int b = blockIdx.x, xcd = b & 7, jj = b >> 3;
  const rsrc_t rs = __builtin_amdgcn_make_buffer_rsrc((void*)ws, 0, WS_END, 0x00020000);
  STAMP(0);
  STAMPC(30);
  const bool is_conv = jj < 18, is_gemm = jj < 24;
  const int c0 = 4 * (18 * xcd + jj);
  if (tid < 8) ((LAS unsigned*)(lds + CV_MISC))[tid] = 0;
  asm volatile("" ::"s"(x), "s"(Are), "s"(Aim), "s"(Bre), "s"(Bim), "s"(Cre), "s"(Cim), "s"(logdt), "s"(ws));
  __syncthreads();
  STAMP(42);
  const u64 epoch = __hip_atomic_load((gu64*)(ws + WS_CTRL), RLX);
#define GET_TAG() ((((u64)(unsigned)__builtin_amdgcn_readfirstlane((unsigned)(epoch >> 32)) << 32) | (unsigned)__builtin_amdgcn_readfirstlane((unsigned)epoch)) + 1)
  u64 tag;
  LAS unsigned* cnt = (LAS unsigned*)(lds + CV_MISC);
  LAS float* smx = (LAS float*)(lds + CV_MISC + 32);
  LAS float* kscs = (LAS float*)(lds + CV_MISC + 64);

  if (wv < 4) {
    if (wv < 3) {
      const int t8 = lane >> 3, sub = lane & 7;
      const GAS v4u* row = (const GAS v4u*)(x + (size_t)(b * 8 + t8) * D) + 24 * wv + sub;
      v4u a[3];
#pragma unroll
      for (int i = 0; i < 3; ++i) a[i] = row[8 * i];
      __builtin_amdgcn_sched_barrier(0);
      tag = GET_TAG();
      float s = 0.f, ss = 0.f;
      const unsigned ones = 0x3F803F80u;
#pragma unroll
      for (int i = 0; i < 3; ++i) {
#pragma unroll
        for (int j = 0; j < 4; ++j) { s = dot2bf(a[i][j], ones, s); ss = dot2bf(a[i][j], a[i][j], ss); }
      }
#pragma unroll
      for (int o = 1; o < 8; o <<= 1) { s += __shfl_xor(s, o); ss += __shfl_xor(ss, o); }
      STAMP(38);
      LAS f32x2* part = (LAS f32x2*)(lds + CV_MISC + 128);
      if (sub == 0) part[wv * 8 + t8] = f32x2{s, ss};
#pragma unroll
      for (int i = 0; i < 3; ++i) *(LAS v4u*)(lds + PR_X + t8 * PR_ROW + (24 * wv + 8 * i + sub) * 16) = a[i];
      lds_arrive_wait(cnt + 4, 3, lane);
      STAMP(39);
      {
        const f32x2 p0 = part[t8], p1 = part[8 + t8], p2 = part[16 + t8];
        float m, sd;
        ln_scalars(p0.x + p1.x + p2.x, p0.y + p1.y + p2.y, m, sd);
        const float r = __builtin_amdgcn_rcpf(sd);
#pragma unroll
        for (int i = 0; i < 3; ++i) {
          v4u up;
#pragma unroll
          for (int j = 0; j < 4; ++j) {
            const unsigned d = cvtpk(bflo(a[i][j]) - m, bfhi(a[i][j]) - m);
            const unsigned u2 = cvtpk(bflo(d) * r, bfhi(d) * r);
            up[j] = cvtpkh(bflo(u2), bfhi(u2));
          }
          *(LAS v4u*)(lds + PR_U + t8 * PR_ROW + (24 * wv + 8 * i + sub) * 16) = up;
        }
      }
      {
        const int g = lane >> 4, q = (lane & 15) >> 2, p = lane & 3;
        LAS unsigned char* pa = lds + ((p & 1) ? PR_X : PR_U) + q * PR_ROW + (192 * wv + 8 * g + 4 * (p >> 1)) * 2;
#pragma unroll
        for (int k = 0; k < 6; ++k) {
          v2u lo = tr_read(pa + k * 64), hi = tr_read(pa + k * 64 + 4 * PR_ROW);
          st16(rs, v4u{lo.x, lo.y, hi.x, hi.y}, lane * 16, WS_UX + b * 18432 + (6 * wv + k) * 1024);
        }
      }
      STAMP(40);
      DRAIN();
      STAMP(41);
      lds_arrive_wait(cnt + 4, 6, lane);
      if (tid < 8) __hip_atomic_store((gu64*)(ws + WS_F1) + tid * 256 + b, tag, RLX);
      STAMP(1);
    } else {
      tag = GET_TAG();
    }
    if (is_conv) {
      gu64* p = (gu64*)(ws + WS_F1 + (b & 7) * 2048) + 64 * wv + lane;
      for (unsigned spins = 0; spins < SPIN_CAP; ++spins) {
        if (__all(__hip_atomic_load(p, RLX) == tag)) break;
      }
      ORDER();
      STAMP(3);
      v4u a[8];
#pragma unroll
      for (int i = 0; i < 8; ++i) a[i] = ld16(rs, (lane >> 3) * 18432 + (lane & 7) * 16, WS_UX + (64 * wv + 8 * i) * 18432 + (c0 >> 2) * 128);
      if (wv == 3) lds_arrive_wait(cnt + 4, 6, 64);
      const int pc = lane & 7;
      LAS unsigned char* dst = lds + (pc < 4 ? CV_U + pc * CV_UCH + 512 : CV_X + (pc - 4) * 4096) + (64 * wv + (lane >> 3)) * 16;
#pragma unroll
      for (int i = 0; i < 8; ++i) *(LAS v4u*)(dst + i * 128) = a[i];
      if (tid < 128) *(LAS v4u*)(lds + CV_U + (tid >> 5) * CV_UCH + (tid & 31) * 16) = v4u{0, 0, 0, 0};
      STAMP(4);
    }
  } else if (is_conv) {
    const int cl = (wv - 4) >> 1, half = wv & 1, ch = c0 + cl, n = half * 64 + lane;
    const int gi = ch * NS + n;
    const int t2 = half * 64 + lane;
    const unsigned short rld = logdt[ch], rar = Are[gi], rai = Aim[gi], rbr = Bre[gi], rbi = Bim[gi], rcr = Cre[gi], rci = Cim[gi];
    v4u kid[3];
#pragma unroll
    for (int i = 0; i < 3; ++i) kid[i] = *(const GAS v4u*)(kIdxTab.v + (t2 + 128 * i < 260 ? t2 + 128 * i : 259) * 8);
    __builtin_amdgcn_sched_barrier(0);
    tag = GET_TAG();
    {
      const float dt = bfr(expf(bf2f(rld)));
      const float zr = dt * bf2f(rar), zi = dt * bf2f(rai);
      const float dBr = dt * bf2f(rbr), dBi = dt * bf2f(rbi);
      cplx W = cmul(cplx{bf2f(rcr), bf2f(rci)}, cplx{dBr, dBi});
      float smax = fmaxf(fabsf(W.r), fabsf(W.i));
#pragma unroll
      for (int o = 1; o < 64; o <<= 1) smax = fmaxf(smax, __shfl_xor(smax, o));
      const float inv = smax > 0.f ? 1.0f / smax : 0.f;
      W.r *= inv; W.i *= inv;
      const float a2 = zr * 1.44269504089f, th = zi * 0.15915494309f;
      const cplx e1 = cexp_r(a2, th, 1.f), e8 = cexp_r(a2, th, 8.f), e128 = cexp_r(a2, th, 128.f);
      const cplx e256 = cexp_r(a2, th, 256.f), e512 = cexp_r(a2, th, 512.f), e1024 = cexp_r(a2, th, 1024.f);
      STAMPW(4, 5);
      LAS unsigned char* chan = lds + cl * CV_CH;
      LAS unsigned char* tab = chan + half * CV_TAB;
      const int nsw = lane >> 2, nlo = (lane & 3) * 4;
#define PUT(row, val) *(LAS unsigned*)(tab + (row) * 256 + ((nsw ^ ((row) & 15)) << 4) + nlo) = (val)
#define PUTA(row, z) PUT(row, cvtpkh((z).r, -(z).i))
#define PUTB(row, z) PUT(41 + (row), cvtpkh((z).r, (z).i))
      {
        cplx c[16];
        c[0] = cplx{1.f, 0.f}; c[1] = e8; c[2] = cmul(e8, e8); c[3] = cmul(c[2], e8); c[4] = cmul(c[2], c[2]);
        c[5] = cmul(c[4], c[1]); c[6] = cmul(c[4], c[2]); c[7] = cmul(c[4], c[3]); c[8] = cmul(c[4], c[4]);
#pragma unroll
        for (int j = 9; j < 16; ++j) c[j] = cmul(c[8], c[j - 8]);
#pragma unroll
        for (int j = 0; j < 16; ++j) PUTB(j, c[j]);
      }
      {
        const cplx e2 = cmul(e1, e1), e4 = cmul(e2, e2);
        cplx p[8];
        p[0] = W; p[1] = cmul(W, e1); p[2] = cmul(W, e2); p[3] = cmul(p[1], e2);
#pragma unroll
        for (int c = 4; c < 8; ++c) p[c] = cmul(p[c - 4], e4);
#pragma unroll
        for (int c = 0; c < 8; ++c) {
          const cplx qv = cmul(p[c], e128);
          PUTA(c, p[c]);
          PUTA(8 + c, qv);
          if ((c & 1) == 0) {
            PUTA(16 + (c >> 1), cmul(p[c], e256));
            PUTA(20 + (c >> 1), cmul(qv, e256));
          }
        }
        const cplx e384 = cmul(e256, e128);
        const cplx s0 = cmul(p[0], e512), s1 = cmul(p[4], e512);
        PUTA(24, s0); PUTA(25, s1);
        PUTA(26, cmul(s0, e128)); PUTA(27, cmul(s1, e128));
        PUTA(28, cmul(s0, e256)); PUTA(29, cmul(s1, e256));
        PUTA(30, cmul(s0, e384)); PUTA(31, cmul(s1, e384));
        const cplx t0 = cmul(W, e1024), t4 = cmul(t0, e512);
        PUTA(32, t0); PUTA(33, cmul(t0, e128)); PUTA(34, cmul(t0, e256)); PUTA(35, cmul(t0, e384));
        PUTA(36, t4); PUTA(37, cmul(t4, e128)); PUTA(38, cmul(t4, e256)); PUTA(39, cmul(t4, e384));
        PUTA(40, cmul(t0, e1024));
      }
#undef PUT
#undef PUTA
#undef PUTB
      STAMPW(4, 6);
      f32x4 acc[3] = {f32x4{0, 0, 0, 0}, f32x4{0, 0, 0, 0}, f32x4{0, 0, 0, 0}};
      {
        const int r = lane & 15, h = lane >> 4;
#pragma unroll
        for (int ks = 0; ks < 4; ++ks) {
          v4u bf = *(LAS v4u*)(tab + (41 + r) * 256 + (((4 * ks + h) ^ ((41 + r) & 15)) << 4));
#pragma unroll
          for (int t = 0; t < 3; ++t) {
            v4u af = *(LAS v4u*)(tab + (16 * t + r) * 256 + (((4 * ks + h) ^ r) << 4));
            acc[t] = mfma_h(af, bf, acc[t]);
          }
        }
      }
      LAS float* kd = (LAS float*)tab;
      {
        const int j = lane & 15, q = lane >> 4;
#pragma unroll
        for (int t = 0; t < 3; ++t)
#pragma unroll
          for (int e = 0; e < 4; ++e) kd[(16 * t + 4 * q + e) * 16 + j] = acc[t][e] * smax;
      }
      if (lane == 0) smx[wv - 4] = smax;
      lds_arrive_wait(cnt + cl, 2, lane);
      LAS float* k0 = (LAS float*)chan;
      LAS float* k1 = (LAS float*)(chan + CV_TAB);
      const float sm = fmaxf(smx[2 * cl], smx[2 * cl + 1]);
      const float ksc = sm * (182.f / 1024.f);
      const float kinv = sm > 0.f ? 1.0f / ksc : 0.f;
      LAS unsigned short* kc = (LAS unsigned short*)(chan + CV_KCT);
#pragma unroll
      for (int i = 0; i < 6; ++i) {
        int idx = t2 + 128 * i;
        float v = (idx < 656) ? (k0[idx] + k1[idx]) * kinv : 0.f;
        kc[idx] = (unsigned short)(cvtpkh(v, v) & 0xFFFFu);
      }
      if (t2 == 0) kscs[cl] = ksc;
      lds_arrive_wait(cnt + cl, 4, lane);
#pragma unroll
      for (int i = 0; i < 3; ++i) {
        const int p = t2 + 128 * i;
        v4u o;
#pragma unroll
        for (int j = 0; j < 4; ++j) o[j] = (unsigned)kc[kid[i][j] & 0xFFFFu] | ((unsigned)kc[kid[i][j] >> 16] << 16);
        if (p < 260) *(LAS v4u*)(chan + p * 16) = o;
      }
      lds_arrive_wait(cnt + cl, 6, lane);
#pragma unroll
      for (int i = 0; i < 3; ++i) {
        const int z = t2 + 128 * i;
        if (z < 259) {
          v4u a = *(LAS v4u*)(chan + z * 16);
          v2u bb = *(LAS v2u*)(chan + z * 16 + 16);
          unsigned d[6] = {a.x, a.y, a.z, a.w, bb.x, bb.y};
          LAS unsigned char* base = chan + z * 16;
          *(LAS v4u*)(base + CV_KC) = v4u{__builtin_amdgcn_alignbit(d[1], d[0], 16), __builtin_amdgcn_alignbit(d[2], d[1], 16), __builtin_amdgcn_alignbit(d[3], d[2], 16), __builtin_amdgcn_alignbit(d[4], d[3], 16)};
          *(LAS v4u*)(base + 2 * CV_KC) = v4u{d[1], d[2], d[3], d[4]};
          *(LAS v4u*)(base + 3 * CV_KC) = v4u{__builtin_amdgcn_alignbit(d[2], d[1], 16), __builtin_amdgcn_alignbit(d[3], d[2], 16), __builtin_amdgcn_alignbit(d[4], d[3], 16), __builtin_amdgcn_alignbit(d[5], d[4], 16)};
        }
      }
      STAMPW(4, 7);
    }
  } else {
    tag = GET_TAG();
  }
  if (!is_conv) {
    const int idx = (jj - 18) * 8 + xcd;
#define PT_DELAY 4
    for (int i = 0; i < PT_DELAY; ++i) __builtin_amdgcn_s_sleep(32);
    pretile_unit(lds, rs, ws, idx, tag, w1, w2, w3, w4);
    if (idx >= 48 && idx < 98) pretile_unit(lds, rs, ws, 112 + idx - 48, tag, w1, w2, w3, w4);
    STAMP(2);
  }
  if (is_conv) {
    const float dsk = bf2f(dskip[c0 + (wv & 3)]);
    __syncthreads();
    STAMP(8);
    STAMPC(31);
    const int cl = wv & 3, kh = (wv >> 2) & 1;
    f32x4 acc[8];
    if (wv < 8) {
#pragma unroll
      for (int t = 0; t < 8; ++t) acc[t] = f32x4{0, 0, 0, 0};
      const int bb = lane & 15, h = lane >> 4;
      const int cpy = 3 - (bb & 3);
      LAS unsigned char* abase = lds + cl * CV_CH + cpy * CV_KC + (2047 - 16 - bb + 8 * h - cpy) * 2 - 64 * 4 * kh;
      LAS unsigned char* bbase = lds + CV_U + cl * CV_UCH + 512 + (16 * (bb - 1) + 8 * h) * 2 - 64 * 4 * kh;
      LAS unsigned char* abase2 = abase + 8;
      asm volatile("" : "+v"(abase2));
      v4u A[8], B[8];
#define LDA(i, rho) do { v2u lo_ = *(LAS v2u*)(abase - 64 * ((rho) + 8 * (i))), hi_ = *(LAS v2u*)(abase2 - 64 * ((rho) + 8 * (i))); A[i] = v4u{lo_.x, lo_.y, hi_.x, hi_.y}; } while (0)
#define LDB(m, rho) B[m] = *(LAS v4u*)(bbase + 32 * (16 * (m) - 2 * (rho)))
#pragma unroll
      for (int i = 0; i < 8; ++i) { LDB(i, 0); LDA(i, 0); }
#pragma unroll
      for (int rho = 0; rho < 4; ++rho) {
#pragma unroll
        for (int i = 0; i < 8; ++i) {
#pragma unroll
          for (int m = 7 - i; m >= 0; --m) acc[i + m] = mfma_h(A[i], B[m], acc[i + m]);
          if (rho < 3) { LDA(i, rho + 1); LDB(7 - i, rho + 1); }
          __builtin_amdgcn_sched_barrier(0);
        }
      }
#undef LDA
#undef LDB
      LAS unsigned char* xo = lds + cl * CV_CH + CV_XCH + kh * 4096 + lane * 16;
#pragma unroll
      for (int t = 0; t < 4; ++t) {
        f32x4 lo = acc[t], hi = acc[4 + t];
        asm volatile("" : "+v"(lo), "+v"(hi));
        *(LAS f32x4*)(xo + t * 1024) = kh ? lo : hi;
        acc[t] = kh ? hi : lo;
      }
    }
    STAMP(9);
    __syncthreads();
    STAMP(32);
    if (wv < 8) {
      const int ch = c0 + cl;
      const float ksc = kscs[cl];
      const int a = lane & 15, q = lane >> 4;
      LAS unsigned char* xi = lds + cl * CV_CH + CV_XCH + (kh ^ 1) * 4096 + lane * 16;
      unsigned pk[4][2];
#pragma unroll
      for (int t = 0; t < 4; ++t) {
        const f32x4 o = *(LAS f32x4*)(xi + t * 1024);
        const f32x4 y4 = acc[t] + o;
        const int tok = 256 * (4 * kh + t) + 16 * a + 4 * q;
        v2u uu = *(LAS v2u*)(lds + CV_U + cl * CV_UCH + 512 + tok * 2);
        v2u xx = *(LAS v2u*)(lds + CV_X + cl * 4096 + tok * 2);
#pragma unroll
        for (int e = 0; e < 2; ++e) {
          const unsigned uw = e ? uu.y : uu.x, xw = e ? xx.y : xx.x;
          const h16x2 u2 = __builtin_bit_cast(h16x2, uw);
          const unsigned y = cvtpk(y4[2 * e] * ksc, y4[2 * e + 1] * ksc);
          const unsigned ud = cvtpk((float)u2[0] * dsk, (float)u2[1] * dsk);
          const unsigned tt = cvtpk(bflo(y) + bflo(ud), bfhi(y) + bfhi(ud));
          pk[t][e] = cvtpk(bflo(xw) + bflo(tt), bfhi(xw) + bfhi(tt));
        }
      }
#pragma unroll
      for (int t = 0; t < 4; t += 2) {
        auto s0 = __builtin_amdgcn_permlane16_swap(pk[t][0], pk[t + 1][0], false, false);
        auto s1 = __builtin_amdgcn_permlane16_swap(pk[t][1], pk[t + 1][1], false, false);
        const int tok = 256 * (4 * kh + t + (q & 1)) + 16 * a + 8 * (q >> 1);
        st16(rs, v4u{s0[0], s1[0], s0[1], s1[1]}, WS_HT + (ch * L + tok) * 2);
      }
      STAMP(33);
      DRAIN();
      STAMP(34);
    }
    __syncthreads();
    if (tid < 8) __hip_atomic_store((gu64*)(ws + WS_F2) + tid * 256 + (18 * xcd + jj), tag, RLX);
    STAMP(10);
  }

  const int tt = 4 * xcd + jj / 6, sl = jj % 6;
  if (is_gemm) {
    const int cl = wv % 6, kh = wv / 6, ct = 6 * sl + cl;
    const int tok = lane & 15, q = lane >> 4;
    if (wv == 0) {
      const int u0 = (96 * sl) >> 6;
      gu64* p = (gu64*)(ws + WS_FPT + (b & 3) * 2048) + (lane & 1) + u0 + ((lane & 2) ? 9 : 0);
      for (unsigned spins = 0; spins < SPIN_CAP; ++spins) {
        if (__all(__hip_atomic_load(p, RLX) == tag)) break;
      }
      ORDER();
    }
    __syncthreads();
    v4u bw[9];
    if (wv != 0) {
#pragma unroll
      for (int ks = 0; ks < 9; ++ks) bw[ks] = ld16(rs, lane * 16, WS_W1T + (ct * 18 + 9 * kh + ks) * 1024);
    }
    const f32x4 cs = __builtin_bit_cast(f32x4, ld16(rs, WS_CS1 + (ct * 16 + 4 * q) * 4));
    if (wv == 0) {
      gu64* p = (gu64*)(ws + WS_F2 + (b & 7) * 2048);
      for (unsigned spins = 0; spins < SPIN_CAP; ++spins) {
        u64 a0 = __hip_atomic_load(p + lane, RLX), a1 = __hip_atomic_load(p + 64 + lane, RLX), a2 = __hip_atomic_load(p + 128 + (lane & 15), RLX);
        if (__all(a0 == tag && a1 == tag && a2 == tag)) break;
      }
      ORDER();
#pragma unroll
      for (int ks = 0; ks < 9; ++ks) bw[ks] = ld16(rs, lane * 16, WS_W1T + (ct * 18 + 9 * kh + ks) * 1024);
    }
    __syncthreads();
    STAMP(11);
    {
      v4u a[6];
#pragma unroll
      for (int i = 0; i < 6; ++i) {
        const int p = tid + NT * i, c = p >> 3, pc = p & 7;
        a[i] = ld16(rs, WS_HT + (c * L + tt * 64 + pc * 8) * 2);
      }
#pragma unroll
      for (int i = 0; i < 6; ++i) {
        const int p = tid + NT * i, c = p >> 3, pc = p & 7;
        const int f = ((c >> 1) & 1) | (((c >> 3) & 1) << 1);
        *(LAS v4u*)(lds + G_A + c * 128 + ((((pc >> 1) ^ f) << 1 | (pc & 1)) << 4)) = a[i];
      }
      if (tid == 0) *(LAS unsigned*)(lds + G_ST + 3584) = 0;
    }
    __syncthreads();
    v4u bw2[9];
#pragma unroll
    for (int ks = 0; ks < 9; ++ks) bw2[ks] = ld16(rs, lane * 16, WS_W2T + (ct * 18 + 9 * kh + ks) * 1024);
    const v4u hpiece = ld16(rs, WS_HT + ((ct * 16 + (lane >> 2)) * L + tt * 64 + kh * 32 + (lane & 3) * 8) * 2);
    __builtin_amdgcn_sched_barrier(0);
    STAMP(12);
    f32x4 acc[4];
#pragma unroll
    for (int r = 0; r < 4; ++r) acc[r] = f32x4{0, 0, 0, 0};
    {
      const int pq = (lane & 15) >> 2, pp = lane & 3;
      const int f = ((pq >> 1) & 1) | ((q & 1) << 1);
      const unsigned ones = 0x3F803F80u;
      LAS unsigned char* pa0 = lds + G_A + (8 * q + pq) * 128 + pp * 8;
      LAS unsigned char* par[4];
#pragma unroll
      for (int r = 0; r < 4; ++r) par[r] = pa0 + ((r ^ f) << 5);
      {
        float ps = 0.f, pv = 0.f;
        LAS unsigned char* ps0 = pa0 + 6 * (wv >> 2) * 4096 + (((wv & 3) ^ f) << 5);
        v2u lo[6], hi[6];
#pragma unroll
        for (int i = 0; i < 6; ++i) { lo[i] = tr_read(ps0 + i * 4096); hi[i] = tr_read(ps0 + i * 4096 + 4 * 128); }
#pragma unroll
        for (int i = 0; i < 6; ++i) {
          ps = dot2bf(lo[i].x, ones, ps); ps = dot2bf(lo[i].y, ones, ps); ps = dot2bf(hi[i].x, ones, ps); ps = dot2bf(hi[i].y, ones, ps);
          pv = dot2bf(lo[i].x, lo[i].x, pv); pv = dot2bf(lo[i].y, lo[i].y, pv); pv = dot2bf(hi[i].x, hi[i].x, pv); pv = dot2bf(hi[i].y, hi[i].y, pv);
        }
        ps += __shfl_xor(ps, 16); ps += __shfl_xor(ps, 32);
        pv += __shfl_xor(pv, 16); pv += __shfl_xor(pv, 32);
        if (lane < 16) *(LAS f32x2*)(lds + G_ST + (((wv & 3) * 16 + lane) * 3 + (wv >> 2)) * 8) = f32x2{ps, pv};
        lds_arrive((LAS unsigned*)(lds + G_ST + 3584), lane);
      }
      v4u af[2][4];
#pragma unroll
      for (int r = 0; r < 4; ++r) { v2u lo = tr_read(par[r] + 9 * kh * 4096), hi = tr_read(par[r] + 9 * kh * 4096 + 512); af[0][r] = v4u{lo.x, lo.y, hi.x, hi.y}; }
#pragma unroll
      for (int ks = 0; ks < 9; ++ks) {
        if (ks < 8) {
#pragma unroll
          for (int r = 0; r < 4; ++r) {
            v2u lo = tr_read(par[r] + (9 * kh + ks + 1) * 4096), hi = tr_read(par[r] + (9 * kh + ks + 1) * 4096 + 512);
            af[(ks + 1) & 1][r] = v4u{lo.x, lo.y, hi.x, hi.y};
          }
        }
#pragma unroll
        for (int r = 0; r < 4; ++r) acc[r] = mfma_bf(bw[ks], af[ks & 1][r], acc[r]);
        __builtin_amdgcn_sched_barrier(0);
      }
    }
    STAMP(35);
    {
      LAS unsigned char* xo = lds + G_X + wv * 2048 + lane * 16;
#pragma unroll
      for (int r = 0; r < 2; ++r) {
        f32x4 lo = acc[r], hi = acc[2 + r];
        asm volatile("" : "+v"(lo), "+v"(hi));
        *(LAS f32x4*)(xo + r * 1024) = kh ? lo : hi;
        acc[r] = kh ? hi : lo;
      }
    }
    if (wv == 11) {
      lds_wait((LAS unsigned*)(lds + G_ST + 3584), 12);
      float s_ = 0.f, ss = 0.f;
#pragma unroll
      for (int w = 0; w < 3; ++w) { f32x2 v = *(LAS f32x2*)(lds + G_ST + (lane * 3 + w) * 8); s_ += v.x; ss += v.y; }
      float m1, s1;
      ln_scalars(s_, ss, m1, s1);
      const float mean = s_ * (1.0f / D);
      float var = __builtin_fmaf(-mean, mean, ss * (1.0f / D));
      var = var < 0.f ? 0.f : var;
      const float r1 = __builtin_amdgcn_rcpf(s1);
      const float m2 = bfr((mean - m1) * r1);
      const float s2 = bfr(__builtin_amdgcn_sqrtf(bfr(bfr(var * r1 * r1) + 1e-5f)));
      *(LAS f32x4*)(lds + G_ST + 2048 + lane * 16) = f32x4{__builtin_fmaf(m2, s1, m1), r1 * __builtin_amdgcn_rcpf(s2), m1, r1};
    }
    __syncthreads();
    {
      LAS unsigned char* xi = lds + G_X + (kh ? wv - 6 : wv + 6) * 2048 + lane * 16;
#pragma unroll
      for (int r = 0; r < 2; ++r) acc[r] += *(LAS f32x4*)(xi + r * 1024);
    }
    STAMP(13);
    f32x2 st1[2];
#pragma unroll
    for (int r = 0; r < 2; ++r) {
      const int row = (2 * kh + r) * 16 + tok;
      const f32x4 sc = *(LAS f32x4*)(lds + G_ST + 2048 + row * 16);
      st1[r] = f32x2{sc[2], sc[3]};
      float v[4];
#pragma unroll
      for (int e = 0; e < 4; ++e) v[e] = (acc[r][e] - sc[0] * cs[e]) * sc[1];
      const unsigned p0 = cvtpk(v[0], v[1]), p1 = cvtpk(v[2], v[3]);
      *(LAS v2u*)(lds + G_O + row * G_OROW + cl * 32 + q * 8) = v2u{cvtpk(gelu_f(bflo(p0)), gelu_f(bfhi(p0))), cvtpk(gelu_f(bflo(p1)), gelu_f(bfhi(p1)))};
    }
    __syncthreads();
    const int orow = tid / 12, ocol = tid % 12;
    st16(rs, *(LAS v4u*)(lds + G_O + orow * G_OROW + ocol * 16), WS_H1 + ((tt * 64 + orow) * D + sl * 96 + ocol * 8) * 2);
    STAMP(36);
    DRAIN();
    STAMP(37);
    __syncthreads();
    if (tid == 0) __hip_atomic_store((gu64*)(ws + WS_F3) + tt * 8 + sl, tag, RLX);
    STAMP(14);

    {
      const int c = lane >> 2, pc = lane & 3;
      *(LAS v4u*)(lds + G_H + wv * 1024 + c * 64 + ((((pc >> 1) ^ ((c >> 3) & 1)) << 1 | (pc & 1)) << 4)) = hpiece;
    }
    if (wv == 0) {
      int t = tt - 1 + (lane < 18 ? lane / 6 : 1);
      t = t < 0 ? 0 : (t > 31 ? 31 : t);
      gu64* p = (gu64*)(ws + WS_F3) + t * 8 + (lane < 18 ? lane % 6 : 0);
      for (unsigned spins = 0; spins < SPIN_CAP; ++spins) {
        if (__all(__hip_atomic_load(p, RLX) == tag)) break;
      }
      ORDER();
    }
    __syncthreads();
    STAMP(15);
    {
      v4u a[6];
#pragma unroll
      for (int i = 0; i < 6; ++i) {
        const int p = tid + NT * i, r = p / 72, c = p % 72;
        const int cg = c / 18;
        const int sh = cg == 0 ? -1 : (cg == 1 ? 1 : (cg == 2 ? -2 : 2));
        const int t = tt * 64 + r + sh;
        a[i] = ld16(rs, (t >= 0 && t < L) ? WS_H1 + (t * D + c * 8) * 2 : WS_END);
      }
#pragma unroll
      for (int i = 0; i < 6; ++i) {
        const int p = tid + NT * i, r = p / 72, c = p % 72;
        *(LAS v4u*)(lds + G_A + r * ROWB + ((c ^ ((r >> 1) & 7)) << 4)) = a[i];
      }
    }
    __syncthreads();
    STAMP(16);
#pragma unroll
    for (int r = 0; r < 4; ++r) acc[r] = f32x4{0, 0, 0, 0};
    {
      const int sw = (tok >> 1) & 7;
      LAS unsigned char* pa = lds + G_A + tok * ROWB;
      v4u af[2][4];
#pragma unroll
      for (int r = 0; r < 4; ++r) af[0][r] = *(LAS v4u*)(pa + r * 16 * ROWB + (((4 * 9 * kh + q) ^ sw) << 4));
#pragma unroll
      for (int ks = 0; ks < 9; ++ks) {
        if (ks < 8) {
#pragma unroll
          for (int r = 0; r < 4; ++r) af[(ks + 1) & 1][r] = *(LAS v4u*)(pa + r * 16 * ROWB + (((4 * (9 * kh + ks + 1) + q) ^ sw) << 4));
        }
#pragma unroll
        for (int r = 0; r < 4; ++r) acc[r] = mfma_bf(bw2[ks], af[ks & 1][r], acc[r]);
        __builtin_amdgcn_sched_barrier(0);
      }
    }
    {
      LAS unsigned char* xo = lds + G_X + wv * 2048 + lane * 16;
#pragma unroll
      for (int r = 0; r < 2; ++r) {
        f32x4 lo = acc[r], hi = acc[2 + r];
        asm volatile("" : "+v"(lo), "+v"(hi));
        *(LAS f32x4*)(xo + r * 1024) = kh ? lo : hi;
        acc[r] = kh ? hi : lo;
      }
    }
    __syncthreads();
    STAMP(17);
    {
      LAS unsigned char* xi = lds + G_X + (kh ? wv - 6 : wv + 6) * 2048 + lane * 16;
#pragma unroll
      for (int r = 0; r < 2; ++r) {
        acc[r] += *(LAS f32x4*)(xi + r * 1024);
        const int c = 4 * q + ((lane & 15) >> 2);
        const v2u hh = tr_read(lds + G_H + wv * 1024 + c * 64 + ((r ^ ((c >> 3) & 1)) << 5) + (lane & 3) * 8);
        const float m1 = st1[r][0], r1 = st1[r][1];
        unsigned o[2];
#pragma unroll
        for (int e = 0; e < 2; ++e) {
          const unsigned hw = e ? hh.y : hh.x;
          const unsigned d = cvtpk(bflo(hw) - m1, bfhi(hw) - m1);
          const unsigned z = cvtpk(bflo(d) * r1, bfhi(d) * r1);
          const unsigned hs = cvtpk(acc[r][2 * e], acc[r][2 * e + 1]);
          o[e] = cvtpk(bflo(z) + bflo(hs), bfhi(z) + bfhi(hs));
        }
        const int row = (2 * kh + r) * 16 + tok;
        *(LAS v2u*)(lds + G_O + row * G_OROW + cl * 32 + q * 8) = v2u{o[0], o[1]};
        *(LAS v2u*)(lds + G_O + 64 * G_OROW + row * G_OROW + cl * 32 + q * 8) = hh;
      }
    }
    __syncthreads();
    {
      const int off = ((tt * 64 + orow) * D + sl * 96 + ocol * 8) * 2;
      st16(rs, *(LAS v4u*)(lds + G_O + orow * G_OROW + ocol * 16), WS_X1 + off);
      st16(rs, *(LAS v4u*)(lds + G_O + 64 * G_OROW + orow * G_OROW + ocol * 16), WS_HR + off);
    }
    DRAIN();
    __syncthreads();
    if (tid == 0) __hip_atomic_store((gu64*)(ws + WS_F4) + tt * 8 + sl, tag, RLX);
    STAMP(18);
  }

  if (!is_gemm) {
    const int sb = 2 * (xcd & 3), part = jj - 24;
    if (wv == 0) {
      gu64* p = (gu64*)(ws + WS_FPT + (b & 3) * 2048) + (lane < 18 ? 18 + 9 * sb + lane : (lane < 36 ? 90 + 9 * sb + lane - 18 : 18 + 9 * sb));
      for (unsigned spins = 0; spins < SPIN_CAP; ++spins) {
        if (__all(__hip_atomic_load(p, RLX) == tag)) break;
        __builtin_amdgcn_s_sleep(8);
      }
      ORDER();
    }
    __syncthreads();
    unsigned sink = 0;
    const int n3 = 72 * 18 / 8, n4 = 36 * 36 / 8;
#pragma unroll 7
    for (int f = wv; f < n3; f += 12) { v4u v = ldw(rs, lane * 16, WS_W3T + (36 * sb * 18 + part * n3 + f) * 1024); sink ^= v.x; }
#pragma unroll 7
    for (int f = wv; f < n4; f += 12) { const int g = part * n4 + f; v4u v = ldw(rs, lane * 16, WS_W4T + ((g / 36) * 144 + 18 * sb + g % 36) * 1024); sink ^= v.x; }
    asm volatile("" ::"v"(sink));
    STAMP(2);
  }
  const int grp = 2 * (jj >> 1) + (xcd >> 2), s = 2 * (xcd & 3) + (jj & 1);
  {
    constexpr int DEPTH = 3;
    if (wv == 0) {
      gu64* p = (gu64*)(ws + WS_FPT + (b & 3) * 2048) + (lane < 9 ? 18 + 9 * s + lane : (lane < 18 ? 90 + 9 * s + lane - 9 : 18 + 9 * s));
      for (unsigned spins = 0; spins < SPIN_CAP; ++spins) {
        if (__all(__hip_atomic_load(p, RLX) == tag)) break;
      }
      ORDER();
    }
    __syncthreads();
    const int o3 = WS_W3T + (36 * s + 3 * wv) * 18 * 1024, o4 = WS_W4T + (3 * wv * 144 + 18 * s) * 1024;
    v4u bw[DEPTH][3];
    if (wv != 0) {
#pragma unroll
      for (int d = 0; d < DEPTH; ++d)
#pragma unroll
        for (int c = 0; c < 3; ++c) bw[d][c] = ldw(rs, lane * 16, o3 + (c * 18 + d) * 1024);
    }
    if (wv == 0) {
      gu64* p = (gu64*)(ws + WS_F4) + grp * 8 + (lane < 6 ? lane : 0);
      for (unsigned spins = 0; spins < SPIN_CAP; ++spins) {
        if (__all(__hip_atomic_load(p, RLX) == tag)) break;
      }
      ORDER();
#pragma unroll
      for (int d = 0; d < DEPTH; ++d)
#pragma unroll
        for (int c = 0; c < 3; ++c) bw[d][c] = ldw(rs, lane * 16, o3 + (c * 18 + d) * 1024);
    }
    __syncthreads();
    STAMP(19);
    f32x4 cs3[3];
#pragma unroll
    for (int c = 0; c < 3; ++c) cs3[c] = __builtin_bit_cast(f32x4, ld16(rs, WS_CS3 + ((36 * s + 3 * wv + c) * 16 + 4 * (lane >> 4)) * 4));
    if (tid < 4) ((LAS unsigned*)(lds + ML_ST + 512))[tid] = 0;
    {
      v4u a[6];
#pragma unroll
      for (int i = 0; i < 6; ++i) a[i] = ld16(rs, WS_X1 + grp * 64 * ROWB + (tid + NT * i) * 16);
#pragma unroll
      for (int i = 0; i < 6; ++i) {
        int p = tid + NT * i, r = p / 72, c = p % 72;
        *(LAS v4u*)(lds + ML_A + r * ROWB + ((c ^ ((r >> 1) & 7)) << 4)) = a[i];
      }
    }
    __syncthreads();
    STAMP(20);
    STAMPC(28);
    LAS unsigned* mcnt = (LAS unsigned*)(lds + ML_ST + 512);
    if (wv >= 8) {
#pragma unroll
      for (int ps = 0; ps < 2; ++ps) {
        const int r = (wv - 8) * 16 + ps * 8 + (lane >> 3), sub = lane & 7;
        float sm = 0.f, ss = 0.f;
        const unsigned ones = 0x3F803F80u;
#pragma unroll
        for (int i = 0; i < 9; ++i) {
          v4u a = *(LAS v4u*)(lds + ML_A + r * ROWB + (sub + 8 * i) * 16);
#pragma unroll
          for (int j = 0; j < 4; ++j) { sm = dot2bf(a[j], ones, sm); ss = dot2bf(a[j], a[j], ss); }
        }
#pragma unroll
        for (int o = 1; o < 8; o <<= 1) { sm += __shfl_xor(sm, o); ss += __shfl_xor(ss, o); }
        float m, sd;
        ln_scalars(sm, ss, m, sd);
        if (sub == 0) *(LAS f32x2*)(lds + ML_ST + r * 8) = f32x2{m, __builtin_amdgcn_rcpf(sd)};
      }
      lds_arrive(mcnt, lane);
    }
    const int tok = lane & 15, q = lane >> 4;
    const int sw = (tok >> 1) & 7;
    LAS unsigned char* abase = lds + ML_A + tok * ROWB;
    f32x4 acc[4][3];
#pragma unroll
    for (int r = 0; r < 4; ++r)
#pragma unroll
      for (int c = 0; c < 3; ++c) acc[r][c] = f32x4{0, 0, 0, 0};
    __builtin_amdgcn_s_setprio(2);
    v4u af[2][4];
#pragma unroll
    for (int r = 0; r < 4; ++r) af[0][r] = *(LAS v4u*)(abase + r * 16 * ROWB + ((q ^ sw) << 4));
#pragma unroll
    for (int ks = 0; ks < 18; ++ks) {
      if (ks < 17) {
#pragma unroll
        for (int r = 0; r < 4; ++r) af[(ks + 1) & 1][r] = *(LAS v4u*)(abase + r * 16 * ROWB + (((4 * (ks + 1) + q) ^ sw) << 4));
      }
      v4u cur[3];
#pragma unroll
      for (int c = 0; c < 3; ++c) {
        cur[c] = bw[ks % DEPTH][c];
        int nk = ks + DEPTH;
        if (nk < 18) bw[ks % DEPTH][c] = ldw(rs, lane * 16, o3 + (c * 18 + nk) * 1024);
        else bw[ks % DEPTH][c] = ldw(rs, lane * 16, o4 + (c * 144 + nk - 18) * 1024);
      }
#pragma unroll
      for (int c = 0; c < 3; ++c)
#pragma unroll
        for (int r = 0; r < 4; ++r) acc[r][c] = mfma_bf(cur[c], af[ks & 1][r], acc[r][c]);
      __builtin_amdgcn_sched_barrier(0);
    }
    __builtin_amdgcn_s_setprio(0);
    STAMP(21);
    lds_wait(mcnt, 4);
    STAMP(22);
    LAS unsigned char* H = lds + ML_H;
    {
      f32x2 st[4];
#pragma unroll
      for (int r = 0; r < 4; ++r) st[r] = *(LAS f32x2*)(lds + ML_ST + (r * 16 + tok) * 8);
#pragma unroll
      for (int c = 0; c < 3; ++c) {
#pragma unroll
        for (int r = 0; r < 4; ++r) {
          float v[4];
#pragma unroll
          for (int e = 0; e < 4; ++e) v[e] = gelu_f((acc[r][c][e] - st[r].x * cs3[c][e]) * st[r].y);
          v2u o = {cvtpk(v[0], v[1]), cvtpk(v[2], v[3])};
          const int chunk = 2 * (3 * wv + c) + (q >> 1);
          *(LAS v2u*)(H + (r * 16 + tok) * ROWB + ((chunk ^ sw) << 4) + (q & 1) * 8) = o;
          acc[r][c] = f32x4{0, 0, 0, 0};
        }
      }
    }
    lds_arrive(mcnt + 1 + (wv >> 2), lane);
    STAMP(23);
    LAS unsigned char* hbase = H + tok * ROWB;
#pragma unroll
    for (int k2 = 0; k2 < 18; ++k2) {
      const int ks = 18 + k2;
      if (k2 % 6 == 0) {
        lds_wait(mcnt + 1 + k2 / 6, 4);
#pragma unroll
        for (int r = 0; r < 4; ++r) af[k2 & 1][r] = *(LAS v4u*)(hbase + r * 16 * ROWB + (((4 * k2 + q) ^ sw) << 4));
      }
      if (k2 % 6 != 5) {
#pragma unroll
        for (int r = 0; r < 4; ++r) af[(k2 + 1) & 1][r] = *(LAS v4u*)(hbase + r * 16 * ROWB + (((4 * (k2 + 1) + q) ^ sw) << 4));
      }
      v4u cur[3];
#pragma unroll
      for (int c = 0; c < 3; ++c) {
        cur[c] = bw[ks % DEPTH][c];
        int nk = k2 + DEPTH;
        if (nk < 18) bw[ks % DEPTH][c] = ldw(rs, lane * 16, o4 + (c * 144 + nk) * 1024);
      }
#pragma unroll
      for (int c = 0; c < 3; ++c)
#pragma unroll
        for (int r = 0; r < 4; ++r) acc[r][c] = mfma_bf(cur[c], af[k2 & 1][r], acc[r][c]);
      __builtin_amdgcn_sched_barrier(0);
    }
    STAMP(24);
    {
      const int po = WS_PART + (s * 32 + grp) * 72 * 1024;
#pragma unroll
      for (int c = 0; c < 3; ++c)
#pragma unroll
        for (int rp = 0; rp < 2; ++rp) {
          unsigned x0 = cvtpk(acc[2 * rp][c][0], acc[2 * rp][c][1]), x1 = cvtpk(acc[2 * rp][c][2], acc[2 * rp][c][3]);
          unsigned y0 = cvtpk(acc[2 * rp + 1][c][0], acc[2 * rp + 1][c][1]), y1 = cvtpk(acc[2 * rp + 1][c][2], acc[2 * rp + 1][c][3]);
          auto s0 = __builtin_amdgcn_permlane16_swap(x0, y0, false, false);
          auto s1 = __builtin_amdgcn_permlane16_swap(x1, y1, false, false);
          st16(rs, v4u{s0[0], s1[0], s0[1], s1[1]}, lane * 16, po + (rp * 36 + 3 * wv + c) * 1024);
        }
    }
    DRAIN();
    __syncthreads();
    if (tid == 0) __hip_atomic_store((gu64*)(ws + WS_F5) + grp * 8 + s, tag, RLX);
    STAMP(25);
    STAMPC(29);
  }

  {
    if (wv == 0) wait_flags(ws, WS_F5 + grp * 64, 8, tag, lane);
    __syncthreads();
    STAMP(26);
    if (wv < 9) {
      const int rp = s & 1, oc = 9 * (s >> 1) + wv, u = rp * 36 + oc;
      const int q = lane >> 4, tok = lane & 15;
      const int off = ((grp * 64 + rp * 32 + (q & 1) * 16 + tok) * D + oc * 16 + 8 * (q >> 1)) * 2;
      v4u p[8];
#pragma unroll
      for (int k = 0; k < 8; ++k) p[k] = ld16(rs, lane * 16, WS_PART + ((k * 32 + grp) * 72 + u) * 1024);
      const v4u xv = ld16(rs, WS_X1 + off), hv = ld16(rs, WS_HR + off);
      v4u o;
#pragma unroll
      for (int j = 0; j < 4; ++j) {
        float lo = 0.f, hi = 0.f;
#pragma unroll
        for (int k = 0; k < 8; ++k) { lo += bflo(p[k][j]); hi += bfhi(p[k][j]); }
        lo = bflo(hv[j]) + bfr(bflo(xv[j]) + bfr(lo));
        hi = bfhi(hv[j]) + bfr(bfhi(xv[j]) + bfr(hi));
        o[j] = cvtpk(lo, hi);
      }
      *(GAS v4u*)((GAS unsigned char*)out + off) = o;
    }
    if (b == 0 && tid == 0) __hip_atomic_store((gu64*)(ws + WS_CTRL), tag, RLX);
    STAMP(27);
  }
}

extern "C" void kernel_launch(void* const* d_in, const int* in_sizes, int n_in, void* d_out, int out_size, void* d_ws, size_t ws_size,
                              hipStream_t stream) {
  (void)hipFuncSetAttribute((const void*)k_all, hipFuncAttributeMaxDynamicSharedMemorySize, LDS_BYTES);
  const unsigned short* const* in = (const unsigned short* const*)d_in;
  k_all<<<256, NT, LDS_BYTES, stream>>>(in[0], in[1], in[2], in[3], in[4], in[5], in[6], in[7], in[8], in[10], in[11], in[12], in[13], (unsigned char*)d_ws,
                                        (unsigned short*)d_out);
}
```

```cpp
#define TLBW 2097152
#include <hip/hip_runtime.h>
#include <hip/hip_bf16.h>
#include <stdint.h>

typedef unsigned long long u64;
typedef unsigned v4u __attribute__((ext_vector_type(4)));
typedef unsigned v2u __attribute__((ext_vector_type(2)));
typedef float f32x4 __attribute__((ext_vector_type(4)));
typedef float f32x2 __attribute__((ext_vector_type(2)));
typedef short s16x8 __attribute__((ext_vector_type(8)));
typedef _Float16 h16x8 __attribute__((ext_vector_type(8)));
typedef _Float16 h16x2 __attribute__((ext_vector_type(2)));
typedef __bf16 bf16x2_t __attribute__((ext_vector_type(2)));
#define GAS __attribute__((address_space(1)))
#define LAS __attribute__((address_space(3)))
#define DI __device__ __forceinline__
#define RLX __ATOMIC_RELAXED, __HIP_MEMORY_SCOPE_AGENT
typedef GAS u64 gu64;
typedef __amdgpu_buffer_rsrc_t rsrc_t;

#define NLAUNCH 1

constexpr int L = 2048, D = 576, NS = 128, F = 4608;
constexpr int NT = 768;

constexpr int WS_CTRL = 0;
constexpr int WS_F1 = 4096;
constexpr int WS_FPT = WS_F1 + 8 * 2048;
constexpr int WS_F2 = WS_FPT + 4 * 2048;
constexpr int WS_F3 = WS_F2 + 8 * 2048;
constexpr int WS_F4 = WS_F3 + 32 * 64;
constexpr int WS_F5 = WS_F4 + 32 * 64;
constexpr int WS_W1T = 131072;
constexpr int WS_W2T = WS_W1T + 36 * 18 * 1024;
constexpr int WS_W3T = WS_W2T + 36 * 18 * 1024;
constexpr int WS_W4T = WS_W3T + 288 * 18 * 1024;
constexpr int WS_CS1 = WS_W4T + 36 * 144 * 1024;
constexpr int WS_CS3 = WS_CS1 + 4096;
constexpr int WS_HT = WS_CS3 + 4608 * 4;
constexpr int WS_H1 = WS_HT + D * L * 2;
constexpr int WS_ST2 = WS_H1 + D * L * 2;
constexpr int WS_X1 = WS_ST2 + 2048 * 8;
constexpr int WS_HR = WS_X1 + D * L * 2;
constexpr int WS_PART = WS_HR + D * L * 2;
constexpr int WS_UX = WS_PART + 8 * D * L * 2;
constexpr int WS_END = WS_UX + 256 * 144 * 128;
static_assert(WS_F5 + 32 * 64 <= WS_W1T, "control block");

#define STAMP(i)
#define STAMPW(w, i)
#define STAMPC(i)

DI float bf2f(unsigned short b) { return __builtin_bit_cast(float, (unsigned)b << 16); }
DI float asf(unsigned u) { return __builtin_bit_cast(float, u); }
DI unsigned asu(float f) { return __builtin_bit_cast(unsigned, f); }
DI float bflo(unsigned u) { return __builtin_bit_cast(float, u << 16); }
DI float bfhi(unsigned u) { return __builtin_bit_cast(float, u & 0xFFFF0000u); }
DI unsigned cvtpk(float lo, float hi) {
  f32x2 v = {lo, hi};
  bf16x2_t b = __builtin_convertvector(v, bf16x2_t);
  return __builtin_bit_cast(unsigned, b);
}
DI float bfr(float x) { return bflo(cvtpk(x, x)); }
DI unsigned cvtpkh(float lo, float hi) {
  f32x2 v = {lo, hi};
  h16x2 b = __builtin_convertvector(v, h16x2);
  return __builtin_bit_cast(unsigned, b);
}
DI f32x4 mfma_bf(v4u a, v4u b, f32x4 c) {
  return __builtin_amdgcn_mfma_f32_16x16x32_bf16(__builtin_bit_cast(s16x8, a), __builtin_bit_cast(s16x8, b), c, 0, 0, 0);
}
DI f32x4 mfma_h(v4u a, v4u b, f32x4 c) {
  return __builtin_amdgcn_mfma_f32_16x16x32_f16(__builtin_bit_cast(h16x8, a), __builtin_bit_cast(h16x8, b), c, 0, 0, 0);
}
DI float dot2bf(unsigned a, unsigned b, float acc) {
  return __builtin_amdgcn_fdot2_f32_bf16(__builtin_bit_cast(bf16x2_t, a), __builtin_bit_cast(bf16x2_t, b), acc, false);
}
DI v2u tr_read(LAS unsigned char* p) {
  typedef short v4i16_t __attribute__((ext_vector_type(4)));
  return __builtin_bit_cast(v2u, __builtin_amdgcn_ds_read_tr16_b64_v4i16((LAS v4i16_t*)p));
}
DI float gelu_f(float x) {
  const float c0 = -2.3022081986f, c1 = -0.10294324f;
  float x2 = x * x;
  float p = __builtin_fmaf(x2, c1, c0);
  float e = __builtin_amdgcn_exp2f(x * p);
  return x * __builtin_amdgcn_rcpf(1.0f + e);
}
DI void ln_scalars(float sum, float sumsq, float& m, float& s) {
  float mean = sum * (1.0f / D);
  float var = __builtin_fmaf(-mean, mean, sumsq * (1.0f / D));
  var = var < 0.f ? 0.f : var;
  m = bfr(mean);
  s = bfr(__builtin_amdgcn_sqrtf(bfr(bfr(var) + 1e-5f)));
}
constexpr int SC1 = 16;
DI v4u ld16(rsrc_t rs, int voff, int soff = 0) { return __builtin_amdgcn_raw_buffer_load_b128(rs, voff, soff, SC1); }
#define WAUX 16
DI v4u ldw(rsrc_t rs, int voff, int soff = 0) { return __builtin_amdgcn_raw_buffer_load_b128(rs, voff, soff, WAUX); }
DI v2u ld8(rsrc_t rs, int voff, int soff = 0) { return __builtin_amdgcn_raw_buffer_load_b64(rs, voff, soff, SC1); }
DI void st16(rsrc_t rs, v4u v, int voff, int soff = 0) { __builtin_amdgcn_raw_buffer_store_b128(v, rs, voff, soff, SC1); }
DI void st8(rsrc_t rs, v2u v, int voff, int soff = 0) { __builtin_amdgcn_raw_buffer_store_b64(v, rs, voff, soff, SC1); }
DI void st4(rsrc_t rs, unsigned v, int voff, int soff = 0) { __builtin_amdgcn_raw_buffer_store_b32(v, rs, voff, soff, SC1); }
#define DRAIN() asm volatile("s_waitcnt vmcnt(0)" ::: "memory")
#define ORDER() __builtin_amdgcn_fence(__ATOMIC_ACQUIRE, "wavefront")
constexpr unsigned SPIN_CAP = 1u << 15;

DI void wait_flags(unsigned char* ws, int off, int n, u64 tag, int lane) {
  gu64* p = (gu64*)(ws + off) + (lane < n ? lane : 0);
  for (unsigned spins = 0; spins < SPIN_CAP; ++spins) {
    if (__all(__hip_atomic_load(p, RLX) == tag)) break;
  }
  ORDER();
}

struct KIdx { unsigned short v[2080]; };
constexpr KIdx make_kidx() {
  KIdx t{};
  for (int y = 0; y < 2080; ++y) {
    if (y >= 2048) { t.v[y] = 767; continue; }
    int j = 2047 - y;
    int v = j;
    if (j >= 256) {
      int sh = j >= 1024 ? 3 : (j >= 512 ? 2 : 1);
      int step = 1 << sh, lo = (j >> sh) << sh, rem = j - lo, half = step >> 1;
      if (rem > half || (rem == half && (((lo >> sh) & 1) != 0))) lo += step;
      v = lo;
    }
    int seg = v >= 1024 ? 3 : (v >= 512 ? 2 : (v >= 256 ? 1 : 0));
    int u = seg == 0 ? v : v - (128 << seg);
    int rb = seg == 0 ? 0 : (seg == 1 ? 16 : (seg == 2 ? 24 : 32));
    int row = rb + ((u & 7) >> seg) + (u >> 7) * (8 >> seg);
    int col = (u >> 3) & 15;
    t.v[y] = (unsigned short)(row * 16 + col);
  }
  return t;
}
__device__ const KIdx kIdxTab = make_kidx();

struct cplx { float r, i; };
DI cplx cmul(cplx a, cplx b) { return cplx{__builtin_fmaf(a.r, b.r, -a.i * b.i), __builtin_fmaf(a.r, b.i, a.i * b.r)}; }
DI cplx cexp_r(float a2, float th, float r) {
  float m = __builtin_amdgcn_exp2f(a2 * r);
  float ang = __builtin_amdgcn_fractf(th * r);
  return cplx{m * __builtin_amdgcn_cosf(ang), m * __builtin_amdgcn_sinf(ang)};
}

constexpr int CV_TAB = 57 * 256;
constexpr int CV_CH = 2 * CV_TAB;
constexpr int CV_KC = 4160;
constexpr int CV_XCH = 4 * CV_KC;
constexpr int CV_KCT = 4 * CV_KC + 8192;
constexpr int CV_U = 4 * CV_CH;
constexpr int CV_UCH = (256 + 2048) * 2;
constexpr int CV_X = CV_U + 4 * CV_UCH;
constexpr int CV_MISC = CV_X + 4 * 4096;
static_assert(CV_KCT + 1536 <= CV_CH, "conv channel region");
constexpr int PR_ROW = 1168;
constexpr int PR_U = CV_U, PR_X = CV_U + 8 * PR_ROW;
constexpr int ROWB = 1152;
constexpr int G_A = 0;
constexpr int G_ST = 73728;
constexpr int G_X = G_ST + 4096;
constexpr int G_H = G_X + 12 * 2048;
constexpr int G_O = G_H + 12 * 1024;
constexpr int G_OROW = 208;
constexpr int ML_A = 0, ML_H = 64 * ROWB, ML_ST = 2 * 64 * ROWB;
constexpr int LDS_BYTES = 152 * 1024;
static_assert(CV_MISC + 128 <= LDS_BYTES && ML_ST + 528 <= LDS_BYTES && G_O + 2 * 64 * G_OROW <= LDS_BYTES && PR_X + 8 * PR_ROW <= CV_MISC, "LDS");

DI void lds_arrive_wait(LAS unsigned* ctr, unsigned target, int lane) {
  asm volatile("s_waitcnt lgkmcnt(0)" ::: "memory");
  if (lane == 0) __hip_atomic_fetch_add(ctr, 1u, __ATOMIC_RELAXED, __HIP_MEMORY_SCOPE_WORKGROUP);
  while (__hip_atomic_load(ctr, __ATOMIC_RELAXED, __HIP_MEMORY_SCOPE_WORKGROUP) < target) __builtin_amdgcn_s_sleep(1);
  asm volatile("" ::: "memory");
}

DI void lds_arrive(LAS unsigned* ctr, int lane) {
  asm volatile("s_waitcnt lgkmcnt(0)" ::: "memory");
  if (lane == 0) __hip_atomic_fetch_add(ctr, 1u, __ATOMIC_RELAXED, __HIP_MEMORY_SCOPE_WORKGROUP);
  asm volatile("" ::: "memory");
}
DI void lds_wait(LAS unsigned* ctr, unsigned target) {
  while (__hip_atomic_load(ctr, __ATOMIC_RELAXED, __HIP_MEMORY_SCOPE_WORKGROUP) < target) __builtin_amdgcn_s_sleep(1);
  asm volatile("" ::: "memory");
}
DI void pretile_unit(LAS unsigned char* lds, rsrc_t rs, unsigned char* ws, int unit, u64 tag, const unsigned short* __restrict__ w1, const unsigned short* __restrict__ w2,
                     const unsigned short* __restrict__ w3, const unsigned short* __restrict__ w4) {
  const int tid = threadIdx.x, lane = tid & 63, wv = __builtin_amdgcn_readfirstlane(tid >> 6);
  const unsigned short* w;
  int ldw, dst, ksn, cs = -1;
  if (unit < 9) { w = w1 + unit * 64; ldw = D; dst = WS_W1T + unit * 4 * 18 * 1024; ksn = 18; cs = WS_CS1 + unit * 256; }
  else if (unit < 18) { w = w2 + (unit - 9) * 64; ldw = D; dst = WS_W2T + (unit - 9) * 4 * 18 * 1024; ksn = 18; }
  else if (unit < 90) { w = w3 + (unit - 18) * 64; ldw = F; dst = WS_W3T + (unit - 18) * 4 * 18 * 1024; ksn = 18; cs = WS_CS3 + (unit - 18) * 256; }
  else { int u = unit - 90, kc = u / 9, cg = u % 9; w = w4 + (size_t)kc * 576 * D + cg * 64; ldw = D; dst = WS_W4T + (cg * 4 * 144 + kc * 18) * 1024; ksn = 144; }
  v4u a[6];
#pragma unroll
  for (int i = 0; i < 6; ++i) {
    int p = tid + NT * i, r = p >> 3, c = p & 7;
    a[i] = *(const GAS v4u*)(w + (size_t)r * ldw + c * 8);
  }
  __syncthreads();
#pragma unroll
  for (int i = 0; i < 6; ++i) {
    int p = tid + NT * i, r = p >> 3, c = p & 7;
    int f = ((r >> 1) & 1) | (((r >> 3) & 1) << 1);
    *(LAS v4u*)(lds + r * 128 + ((((c >> 1) ^ f) << 1 | (c & 1)) << 4)) = a[i];
  }
  __syncthreads();
  const int ct = wv & 3, k0 = 6 * (wv >> 2);
  const int g = lane >> 4, q = (lane & 15) >> 2, p = lane & 3;
  const int f = ((q >> 1) & 1) | ((g & 1) << 1);
  float csum = 0.f;
  const unsigned ones = 0x3F803F80u;
#pragma unroll
  for (int ks = 0; ks < 6; ++ks) {
    LAS unsigned char* pa = lds + (32 * (k0 + ks) + 8 * g + q) * 128 + ((ct ^ f) << 5) + p * 8;
    v2u lo = tr_read(pa), hi = tr_read(pa + 4 * 128);
    v4u o = {lo.x, lo.y, hi.x, hi.y};
    st16(rs, o, lane * 16, dst + (ct * ksn + k0 + ks) * 1024);
    csum = dot2bf(o.x, ones, csum); csum = dot2bf(o.y, ones, csum); csum = dot2bf(o.z, ones, csum); csum = dot2bf(o.w, ones, csum);
  }
  if (cs >= 0) {
    csum += __shfl_xor(csum, 16);
    csum += __shfl_xor(csum, 32);
    LAS float* pc = (LAS float*)(lds + 73728);
    if (lane < 16) pc[wv * 16 + lane] = csum;
    __syncthreads();
    if (tid < 64) st4(rs, __builtin_bit_cast(unsigned, pc[tid] + pc[64 + tid] + pc[128 + tid]), cs + tid * 4);
  }
  DRAIN();
  __syncthreads();
  if (tid < 4) __hip_atomic_store((gu64*)(ws + WS_FPT) + tid * 256 + unit, tag, RLX);
}

__global__ __launch_bounds__(NT, 3) void k_all(const unsigned short* __restrict__ x, const unsigned short* __restrict__ Are, const unsigned short* __restrict__ Aim,
                                               const unsigned short* __restrict__ Bre, const unsigned short* __restrict__ Bim, const unsigned short* __restrict__ Cre,
                                               const unsigned short* __restrict__ Cim, const unsigned short* __restrict__ logdt, const unsigned short* __restrict__ dskip,
                                               const unsigned short* __restrict__ w1, const unsigned short* __restrict__ w2, const unsigned short* __restrict__ w3,
                                               const unsigned short* __restrict__ w4, unsigned char* __restrict__ ws, unsigned short* __restrict__ out) {
  extern __shared__ __attribute__((aligned(16))) unsigned char smem[];
  LAS unsigned char* lds = (LAS unsigned char*)smem;
  const int tid = threadIdx.x, lane = tid & 63, wv = __builtin_amdgcn_readfirstlane(tid >> 6);
  const int b = blockIdx.x, xcd = b & 7, jj = b >> 3;
  const rsrc_t rs = __builtin_amdgcn_make_buffer_rsrc((void*)ws, 0, WS_END, 0x00020000);
  STAMP(0);
  STAMPC(30);
  const bool is_conv = jj < 18, is_gemm = jj < 24;
  const int c0 = 4 * (18 * xcd + jj);
  if (tid < 8) ((LAS unsigned*)(lds + CV_MISC))[tid] = 0;
  asm volatile("" ::"s"(x), "s"(Are), "s"(Aim), "s"(Bre), "s"(Bim), "s"(Cre), "s"(Cim), "s"(logdt), "s"(ws));
  __syncthreads();
  STAMP(42);
  const u64 epoch = __hip_atomic_load((gu64*)(ws + WS_CTRL), RLX);
#define GET_TAG() ((((u64)(unsigned)__builtin_amdgcn_readfirstlane((unsigned)(epoch >> 32)) << 32) | (unsigned)__builtin_amdgcn_readfirstlane((unsigned)epoch)) + 1)
  u64 tag;
  LAS unsigned* cnt = (LAS unsigned*)(lds + CV_MISC);
  LAS float* smx = (LAS float*)(lds + CV_MISC + 32);
  LAS float* kscs = (LAS float*)(lds + CV_MISC + 64);

  if (wv < 4) {
    if (wv < 3) {
      const int t8 = lane >> 3, sub = lane & 7;
      const GAS v4u* row = (const GAS v4u*)(x + (size_t)(b * 8 + t8) * D) + 24 * wv + sub;
      v4u a[3];
#pragma unroll
      for (int i = 0; i < 3; ++i) a[i] = row[8 * i];
      __builtin_amdgcn_sched_barrier(0);
      tag = GET_TAG();
      float s = 0.f, ss = 0.f;
      const unsigned ones = 0x3F803F80u;
#pragma unroll
      for (int i = 0; i < 3; ++i) {
#pragma unroll
        for (int j = 0; j < 4; ++j) { s = dot2bf(a[i][j], ones, s); ss = dot2bf(a[i][j], a[i][j], ss); }
      }
#pragma unroll
      for (int o = 1; o < 8; o <<= 1) { s += __shfl_xor(s, o); ss += __shfl_xor(ss, o); }
      STAMP(38);
      LAS f32x2* part = (LAS f32x2*)(lds + CV_MISC + 128);
      if (sub == 0) part[wv * 8 + t8] = f32x2{s, ss};
#pragma unroll
      for (int i = 0; i < 3; ++i) *(LAS v4u*)(lds + PR_X + t8 * PR_ROW + (24 * wv + 8 * i + sub) * 16) = a[i];
      lds_arrive_wait(cnt + 4, 3, lane);
      STAMP(39);
      {
        const f32x2 p0 = part[t8], p1 = part[8 + t8], p2 = part[16 + t8];
        float m, sd;
        ln_scalars(p0.x + p1.x + p2.x, p0.y + p1.y + p2.y, m, sd);
        const float r = __builtin_amdgcn_rcpf(sd);
#pragma unroll
        for (int i = 0; i < 3; ++i) {
          v4u up;
#pragma unroll
          for (int j = 0; j < 4; ++j) {
            const unsigned d = cvtpk(bflo(a[i][j]) - m, bfhi(a[i][j]) - m);
            const unsigned u2 = cvtpk(bflo(d) * r, bfhi(d) * r);
            up[j] = cvtpkh(bflo(u2), bfhi(u2));
          }
          *(LAS v4u*)(lds + PR_U + t8 * PR_ROW + (24 * wv + 8 * i + sub) * 16) = up;
        }
      }
      {
        const int g = lane >> 4, q = (lane & 15) >> 2, p = lane & 3;
        LAS unsigned char* pa = lds + ((p & 1) ? PR_X : PR_U) + q * PR_ROW + (192 * wv + 8 * g + 4 * (p >> 1)) * 2;
#pragma unroll
        for (int k = 0; k < 6; ++k) {
          v2u lo = tr_read(pa + k * 64), hi = tr_read(pa + k * 64 + 4 * PR_ROW);
          st16(rs, v4u{lo.x, lo.y, hi.x, hi.y}, lane * 16, WS_UX + b * 18432 + (6 * wv + k) * 1024);
        }
      }
      STAMP(40);
      DRAIN();
      STAMP(41);
      lds_arrive_wait(cnt + 4, 6, lane);
      if (tid < 8) __hip_atomic_store((gu64*)(ws + WS_F1) + tid * 256 + b, tag, RLX);
      STAMP(1);
    } else {
      tag = GET_TAG();
    }
    if (is_conv) {
      gu64* p = (gu64*)(ws + WS_F1 + (b & 7) * 2048) + 64 * wv + lane;
      for (unsigned spins = 0; spins < SPIN_CAP; ++spins) {
        if (__all(__hip_atomic_load(p, RLX) == tag)) break;
      }
      ORDER();
      STAMP(3);
      v4u a[8];
#pragma unroll
      for (int i = 0; i < 8; ++i) a[i] = ld16(rs, (lane >> 3) * 18432 + (lane & 7) * 16, WS_UX + (64 * wv + 8 * i) * 18432 + (c0 >> 2) * 128);
      if (wv == 3) lds_arrive_wait(cnt + 4, 6, 64);
      const int pc = lane & 7;
      LAS unsigned char* dst = lds + (pc < 4 ? CV_U + pc * CV_UCH + 512 : CV_X + (pc - 4) * 4096) + (64 * wv + (lane >> 3)) * 16;
#pragma unroll
      for (int i = 0; i < 8; ++i) *(LAS v4u*)(dst + i * 128) = a[i];
      if (tid < 128) *(LAS v4u*)(lds + CV_U + (tid >> 5) * CV_UCH + (tid & 31) * 16) = v4u{0, 0, 0, 0};
      STAMP(4);
    }
  } else if (is_conv) {
    const int cl = (wv - 4) >> 1, half = wv & 1, ch = c0 + cl, n = half * 64 + lane;
    const int gi = ch * NS + n;
    const int t2 = half * 64 + lane;
    const unsigned short rld = logdt[ch], rar = Are[gi], rai = Aim[gi], rbr = Bre[gi], rbi = Bim[gi], rcr = Cre[gi], rci = Cim[gi];
    v4u kid[3];
#pragma unroll
    for (int i = 0; i < 3; ++i) kid[i] = *(const GAS v4u*)(kIdxTab.v + (t2 + 128 * i < 260 ? t2 + 128 * i : 259) * 8);
    __builtin_amdgcn_sched_barrier(0);
    tag = GET_TAG();
    {
      const float dt = bfr(expf(bf2f(rld)));
      const float zr = dt * bf2f(rar), zi = dt * bf2f(rai);
      const float dBr = dt * bf2f(rbr), dBi = dt * bf2f(rbi);
      cplx W = cmul(cplx{bf2f(rcr), bf2f(rci)}, cplx{dBr, dBi});
      float smax = fmaxf(fabsf(W.r), fabsf(W.i));
#pragma unroll
      for (int o = 1; o < 64; o <<= 1) smax = fmaxf(smax, __shfl_xor(smax, o));
      const float inv = smax > 0.f ? 1.0f / smax : 0.f;
      W.r *= inv; W.i *= inv;
      const float a2 = zr * 1.44269504089f, th = zi * 0.15915494309f;
      const cplx e1 = cexp_r(a2, th, 1.f), e8 = cexp_r(a2, th, 8.f), e128 = cexp_r(a2, th, 128.f);
      const cplx e256 = cexp_r(a2, th, 256.f), e512 = cexp_r(a2, th, 512.f), e1024 = cexp_r(a2, th, 1024.f);
      STAMPW(4, 5);
      LAS unsigned char* chan = lds + cl * CV_CH;
      LAS unsigned char* tab = chan + half * CV_TAB;
      const int nsw = lane >> 2, nlo = (lane & 3) * 4;
#define PUT(row, val) *(LAS unsigned*)(tab + (row) * 256 + ((nsw ^ ((row) & 15)) << 4) + nlo) = (val)
#define PUTA(row, z) PUT(row, cvtpkh((z).r, -(z).i))
#define PUTB(row, z) PUT(41 + (row), cvtpkh((z).r, (z).i))
      {
        cplx c[16];
        c[0] = cplx{1.f, 0.f}; c[1] = e8; c[2] = cmul(e8, e8); c[3] = cmul(c[2], e8); c[4] = cmul(c[2], c[2]);
        c[5] = cmul(c[4], c[1]); c[6] = cmul(c[4], c[2]); c[7] = cmul(c[4], c[3]); c[8] = cmul(c[4], c[4]);
#pragma unroll
        for (int j = 9; j < 16; ++j) c[j] = cmul(c[8], c[j - 8]);
#pragma unroll
        for (int j = 0; j < 16; ++j) PUTB(j, c[j]);
      }
      {
        const cplx e2 = cmul(e1, e1), e4 = cmul(e2, e2);
        cplx p[8];
        p[0] = W; p[1] = cmul(W, e1); p[2] = cmul(W, e2); p[3] = cmul(p[1], e2);
#pragma unroll
        for (int c = 4; c < 8; ++c) p[c] = cmul(p[c - 4], e4);
#pragma unroll
        for (int c = 0; c < 8; ++c) {
          const cplx qv = cmul(p[c], e128);
          PUTA(c, p[c]);
          PUTA(8 + c, qv);
          if ((c & 1) == 0) {
            PUTA(16 + (c >> 1), cmul(p[c], e256));
            PUTA(20 + (c >> 1), cmul(qv, e256));
          }
        }
        const cplx e384 = cmul(e256, e128);
        const cplx s0 = cmul(p[0], e512), s1 = cmul(p[4], e512);
        PUTA(24, s0); PUTA(25, s1);
        PUTA(26, cmul(s0, e128)); PUTA(27, cmul(s1, e128));
        PUTA(28, cmul(s0, e256)); PUTA(29, cmul(s1, e256));
        PUTA(30, cmul(s0, e384)); PUTA(31, cmul(s1, e384));
        const cplx t0 = cmul(W, e1024), t4 = cmul(t0, e512);
        PUTA(32, t0); PUTA(33, cmul(t0, e128)); PUTA(34, cmul(t0, e256)); PUTA(35, cmul(t0, e384));
        PUTA(36, t4); PUTA(37, cmul(t4, e128)); PUTA(38, cmul(t4, e256)); PUTA(39, cmul(t4, e384));
        PUTA(40, cmul(t0, e1024));
      }
#undef PUT
#undef PUTA
#undef PUTB
      STAMPW(4, 6);
      f32x4 acc[3] = {f32x4{0, 0, 0, 0}, f32x4{0, 0, 0, 0}, f32x4{0, 0, 0, 0}};
      {
        const int r = lane & 15, h = lane >> 4;
#pragma unroll
        for (int ks = 0; ks < 4; ++ks) {
          v4u bf = *(LAS v4u*)(tab + (41 + r) * 256 + (((4 * ks + h) ^ ((41 + r) & 15)) << 4));
#pragma unroll
          for (int t = 0; t < 3; ++t) {
            v4u af = *(LAS v4u*)(tab + (16 * t + r) * 256 + (((4 * ks + h) ^ r) << 4));
            acc[t] = mfma_h(af, bf, acc[t]);
          }
        }
      }
      LAS float* kd = (LAS float*)tab;
      {
        const int j = lane & 15, q = lane >> 4;
#pragma unroll
        for (int t = 0; t < 3; ++t)
#pragma unroll
          for (int e = 0; e < 4; ++e) kd[(16 * t + 4 * q + e) * 16 + j] = acc[t][e] * smax;
      }
      if (lane == 0) smx[wv - 4] = smax;
      lds_arrive_wait(cnt + cl, 2, lane);
      LAS float* k0 = (LAS float*)chan;
      LAS float* k1 = (LAS float*)(chan + CV_TAB);
      const float sm = fmaxf(smx[2 * cl], smx[2 * cl + 1]);
      const float ksc = sm * (182.f / 1024.f);
      const float kinv = sm > 0.f ? 1.0f / ksc : 0.f;
      LAS unsigned short* kc = (LAS unsigned short*)(chan + CV_KCT);
#pragma unroll
      for (int i = 0; i < 6; ++i) {
        int idx = t2 + 128 * i;
        float v = (idx < 656) ? (k0[idx] + k1[idx]) * kinv : 0.f;
        kc[idx] = (unsigned short)(cvtpkh(v, v) & 0xFFFFu);
      }
      if (t2 == 0) kscs[cl] = ksc;
      lds_arrive_wait(cnt + cl, 4, lane);
#pragma unroll
      for (int i = 0; i < 3; ++i) {
        const int p = t2 + 128 * i;
        v4u o;
#pragma unroll
        for (int j = 0; j < 4; ++j) o[j] = (unsigned)kc[kid[i][j] & 0xFFFFu] | ((unsigned)kc[kid[i][j] >> 16] << 16);
        if (p < 260) *(LAS v4u*)(chan + p * 16) = o;
      }
      lds_arrive_wait(cnt + cl, 6, lane);
#pragma unroll
      for (int i = 0; i < 3; ++i) {
        const int z = t2 + 128 * i;
        if (z < 259) {
          v4u a = *(LAS v4u*)(chan + z * 16);
          v2u bb = *(LAS v2u*)(chan + z * 16 + 16);
          unsigned d[6] = {a.x, a.y, a.z, a.w, bb.x, bb.y};
          LAS unsigned char* base = chan + z * 16;
          *(LAS v4u*)(base + CV_KC) = v4u{__builtin_amdgcn_alignbit(d[1], d[0], 16), __builtin_amdgcn_alignbit(d[2], d[1], 16), __builtin_amdgcn_alignbit(d[3], d[2], 16), __builtin_amdgcn_alignbit(d[4], d[3], 16)};
          *(LAS v4u*)(base + 2 * CV_KC) = v4u{d[1], d[2], d[3], d[4]};
          *(LAS v4u*)(base + 3 * CV_KC) = v4u{__builtin_amdgcn_alignbit(d[2], d[1], 16), __builtin_amdgcn_alignbit(d[3], d[2], 16), __builtin_amdgcn_alignbit(d[4], d[3], 16), __builtin_amdgcn_alignbit(d[5], d[4], 16)};
        }
      }
      STAMPW(4, 7);
    }
  } else {
    tag = GET_TAG();
  }
  if (jj >= 24 && wv >= 4) {
    const int part = (jj - 24) * 8 + (wv - 4);
    constexpr int NP = (WS_END + TLBW - 1) / TLBW;
    unsigned sink = 0;
    for (int p = part * 64 + lane; p < NP; p += 4096) sink ^= __builtin_amdgcn_raw_buffer_load_b32(rs, p * TLBW, 0, 0);
    constexpr int NPO = (L * D * 2 + TLBW - 1) / TLBW;
    for (int p = part * 64 + lane; p < NPO; p += 4096) sink ^= *(const GAS unsigned*)((const GAS unsigned char*)out + (size_t)p * TLBW);
    asm volatile("" ::"v"(sink));
  }
  if (!is_conv) {
    const int idx = (jj - 18) * 8 + xcd;
#define PT_DELAY 4
    for (int i = 0; i < PT_DELAY; ++i) __builtin_amdgcn_s_sleep(32);
    pretile_unit(lds, rs, ws, idx, tag, w1, w2, w3, w4);
    if (idx >= 48 && idx < 98) pretile_unit(lds, rs, ws, 112 + idx - 48, tag, w1, w2, w3, w4);
    STAMP(2);
  }
  if (is_conv) {
    const float dsk = bf2f(dskip[c0 + (wv & 3)]);
    __syncthreads();
    STAMP(8);
    STAMPC(31);
    const int cl = wv & 3, kh = (wv >> 2) & 1;
    f32x4 acc[8];
    if (wv < 8) {
#pragma unroll
      for (int t = 0; t < 8; ++t) acc[t] = f32x4{0, 0, 0, 0};
      const int bb = lane & 15, h = lane >> 4;
      const int cpy = 3 - (bb & 3);
      LAS unsigned char* abase = lds + cl * CV_CH + cpy * CV_KC + (2047 - 16 - bb + 8 * h - cpy) * 2 - 64 * 4 * kh;
      LAS unsigned char* bbase = lds + CV_U + cl * CV_UCH + 512 + (16 * (bb - 1) + 8 * h) * 2 - 64 * 4 * kh;
      LAS unsigned char* abase2 = abase + 8;
      asm volatile("" : "+v"(abase2));
      v4u A[8], B[8];
#define LDA(i, rho) do { v2u lo_ = *(LAS v2u*)(abase - 64 * ((rho) + 8 * (i))), hi_ = *(LAS v2u*)(abase2 - 64 * ((rho) + 8 * (i))); A[i] = v4u{lo_.x, lo_.y, hi_.x, hi_.y}; } while (0)
#define LDB(m, rho) B[m] = *(LAS v4u*)(bbase + 32 * (16 * (m) - 2 * (rho)))
#pragma unroll
      for (int i = 0; i < 8; ++i) { LDB(i, 0); LDA(i, 0); }
#pragma unroll
      for (int rho = 0; rho < 4; ++rho) {
#pragma unroll
        for (int i = 0; i < 8; ++i) {
#pragma unroll
          for (int m = 7 - i; m >= 0; --m) acc[i + m] = mfma_h(A[i], B[m], acc[i + m]);
          if (rho < 3) { LDA(i, rho + 1); LDB(7 - i, rho + 1); }
          __builtin_amdgcn_sched_barrier(0);
        }
      }
#undef LDA
#undef LDB
      LAS unsigned char* xo = lds + cl * CV_CH + CV_XCH + kh * 4096 + lane * 16;
#pragma unroll
      for (int t = 0; t < 4; ++t) {
        f32x4 lo = acc[t], hi = acc[4 + t];
        asm volatile("" : "+v"(lo), "+v"(hi));
        *(LAS f32x4*)(xo + t * 1024) = kh ? lo : hi;
        acc[t] = kh ? hi : lo;
      }
    }
    STAMP(9);
    __syncthreads();
    STAMP(32);
    if (wv < 8) {
      const int ch = c0 + cl;
      const float ksc = kscs[cl];
      const int a = lane & 15, q = lane >> 4;
      LAS unsigned char* xi = lds + cl * CV_CH + CV_XCH + (kh ^ 1) * 4096 + lane * 16;
      unsigned pk[4][2];
#pragma unroll
      for (int t = 0; t < 4; ++t) {
        const f32x4 o = *(LAS f32x4*)(xi + t * 1024);
        const f32x4 y4 = acc[t] + o;
        const int tok = 256 * (4 * kh + t) + 16 * a + 4 * q;
        v2u uu = *(LAS v2u*)(lds + CV_U + cl * CV_UCH + 512 + tok * 2);
        v2u xx = *(LAS v2u*)(lds + CV_X + cl * 4096 + tok * 2);
#pragma unroll
        for (int e = 0; e < 2; ++e) {
          const unsigned uw = e ? uu.y : uu.x, xw = e ? xx.y : xx.x;
          const h16x2 u2 = __builtin_bit_cast(h16x2, uw);
          const unsigned y = cvtpk(y4[2 * e] * ksc, y4[2 * e + 1] * ksc);
          const unsigned ud = cvtpk((float)u2[0] * dsk, (float)u2[1] * dsk);
          const unsigned tt = cvtpk(bflo(y) + bflo(ud), bfhi(y) + bfhi(ud));
          pk[t][e] = cvtpk(bflo(xw) + bflo(tt), bfhi(xw) + bfhi(tt));
        }
      }
#pragma unroll
      for (int t = 0; t < 4; t += 2) {
        auto s0 = __builtin_amdgcn_permlane16_swap(pk[t][0], pk[t + 1][0], false, false);
        auto s1 = __builtin_amdgcn_permlane16_swap(pk[t][1], pk[t + 1][1], false, false);
        const int tok = 256 * (4 * kh + t + (q & 1)) + 16 * a + 8 * (q >> 1);
        st16(rs, v4u{s0[0], s1[0], s0[1], s1[1]}, WS_HT + (ch * L + tok) * 2);
      }
      STAMP(33);
      DRAIN();
      STAMP(34);
    }
    __syncthreads();
    if (tid < 8) __hip_atomic_store((gu64*)(ws + WS_F2) + tid * 256 + (18 * xcd + jj), tag, RLX);
    STAMP(10);
  }

  const int tt = 4 * xcd + jj / 6, sl = jj % 6;
  if (is_gemm) {
    const int cl = wv % 6, kh = wv / 6, ct = 6 * sl + cl;
    const int tok = lane & 15, q = lane >> 4;
    if (wv == 0) {
      const int u0 = (96 * sl) >> 6;
      gu64* p = (gu64*)(ws + WS_FPT + (b & 3) * 2048) + (lane & 1) + u0 + ((lane & 2) ? 9 : 0);
      for (unsigned spins = 0; spins < SPIN_CAP; ++spins) {
        if (__all(__hip_atomic_load(p, RLX) == tag)) break;
      }
      ORDER();
    }
    __syncthreads();
    v4u bw[9];
    if (wv != 0) {
#pragma unroll
      for (int ks = 0; ks < 9; ++ks) bw[ks] = ld16(rs, lane * 16, WS_W1T + (ct * 18 + 9 * kh + ks) * 1024);
    }
    const f32x4 cs = __builtin_bit_cast(f32x4, ld16(rs, WS_CS1 + (ct * 16 + 4 * q) * 4));
    if (wv == 0) {
      gu64* p = (gu64*)(ws + WS_F2 + (b & 7) * 2048);
      for (unsigned spins = 0; spins < SPIN_CAP; ++spins) {
        u64 a0 = __hip_atomic_load(p + lane, RLX), a1 = __hip_atomic_load(p + 64 + lane, RLX), a2 = __hip_atomic_load(p + 128 + (lane & 15), RLX);
        if (__all(a0 == tag && a1 == tag && a2 == tag)) break;
      }
      ORDER();
#pragma unroll
      for (int ks = 0; ks < 9; ++ks) bw[ks] = ld16(rs, lane * 16, WS_W1T + (ct * 18 + 9 * kh + ks) * 1024);
    }
    __syncthreads();
    STAMP(11);
    {
      v4u a[6];
#pragma unroll
      for (int i = 0; i < 6; ++i) {
        const int p = tid + NT * i, c = p >> 3, pc = p & 7;
        a[i] = ld16(rs, WS_HT + (c * L + tt * 64 + pc * 8) * 2);
      }
#pragma unroll
      for (int i = 0; i < 6; ++i) {
        const int p = tid + NT * i, c = p >> 3, pc = p & 7;
        const int f = ((c >> 1) & 1) | (((c >> 3) & 1) << 1);
        *(LAS v4u*)(lds + G_A + c * 128 + ((((pc >> 1) ^ f) << 1 | (pc & 1)) << 4)) = a[i];
      }
      if (tid == 0) *(LAS unsigned*)(lds + G_ST + 3584) = 0;
    }
    __syncthreads();
    v4u bw2[9];
#pragma unroll
    for (int ks = 0; ks < 9; ++ks) bw2[ks] = ld16(rs, lane * 16, WS_W2T + (ct * 18 + 9 * kh + ks) * 1024);
    const v4u hpiece = ld16(rs, WS_HT + ((ct * 16 + (lane >> 2)) * L + tt * 64 + kh * 32 + (lane & 3) * 8) * 2);
    __builtin_amdgcn_sched_barrier(0);
    STAMP(12);
    f32x4 acc[4];
#pragma unroll
    for (int r = 0; r < 4; ++r) acc[r] = f32x4{0, 0, 0, 0};
    {
      const int pq = (lane & 15) >> 2, pp = lane & 3;
      const int f = ((pq >> 1) & 1) | ((q & 1) << 1);
      const unsigned ones = 0x3F803F80u;
      LAS unsigned char* pa0 = lds + G_A + (8 * q + pq) * 128 + pp * 8;
      LAS unsigned char* par[4];
#pragma unroll
      for (int r = 0; r < 4; ++r) par[r] = pa0 + ((r ^ f) << 5);
      {
        float ps = 0.f, pv = 0.f;
        LAS unsigned char* ps0 = pa0 + 6 * (wv >> 2) * 4096 + (((wv & 3) ^ f) << 5);
        v2u lo[6], hi[6];
#pragma unroll
        for (int i = 0; i < 6; ++i) { lo[i] = tr_read(ps0 + i * 4096); hi[i] = tr_read(ps0 + i * 4096 + 4 * 128); }
#pragma unroll
        for (int i = 0; i < 6; ++i) {
          ps = dot2bf(lo[i].x, ones, ps); ps = dot2bf(lo[i].y, ones, ps); ps = dot2bf(hi[i].x, ones, ps); ps = dot2bf(hi[i].y, ones, ps);
          pv = dot2bf(lo[i].x, lo[i].x, pv); pv = dot2bf(lo[i].y, lo[i].y, pv); pv = dot2bf(hi[i].x, hi[i].x, pv); pv = dot2bf(hi[i].y, hi[i].y, pv);
        }
        ps += __shfl_xor(ps, 16); ps += __shfl_xor(ps, 32);
        pv += __shfl_xor(pv, 16); pv += __shfl_xor(pv, 32);
        if (lane < 16) *(LAS f32x2*)(lds + G_ST + (((wv & 3) * 16 + lane) * 3 + (wv >> 2)) * 8) = f32x2{ps, pv};
        lds_arrive((LAS unsigned*)(lds + G_ST + 3584), lane);
      }
      v4u af[2][4];
#pragma unroll
      for (int r = 0; r < 4; ++r) { v2u lo = tr_read(par[r] + 9 * kh * 4096), hi = tr_read(par[r] + 9 * kh * 4096 + 512); af[0][r] = v4u{lo.x, lo.y, hi.x, hi.y}; }
#pragma unroll
      for (int ks = 0; ks < 9; ++ks) {
        if (ks < 8) {
#pragma unroll
          for (int r = 0; r < 4; ++r) {
            v2u lo = tr_read(par[r] + (9 * kh + ks + 1) * 4096), hi = tr_read(par[r] + (9 * kh + ks + 1) * 4096 + 512);
            af[(ks + 1) & 1][r] = v4u{lo.x, lo.y, hi.x, hi.y};
          }
        }
#pragma unroll
        for (int r = 0; r < 4; ++r) acc[r] = mfma_bf(bw[ks], af[ks & 1][r], acc[r]);
        __builtin_amdgcn_sched_barrier(0);
      }
    }
    STAMP(35);
    {
      LAS unsigned char* xo = lds + G_X + wv * 2048 + lane * 16;
#pragma unroll
      for (int r = 0; r < 2; ++r) {
        f32x4 lo = acc[r], hi = acc[2 + r];
        asm volatile("" : "+v"(lo), "+v"(hi));
        *(LAS f32x4*)(xo + r * 1024) = kh ? lo : hi;
        acc[r] = kh ? hi : lo;
      }
    }
    if (wv == 11) {
      lds_wait((LAS unsigned*)(lds + G_ST + 3584), 12);
      float s_ = 0.f, ss = 0.f;
#pragma unroll
      for (int w = 0; w < 3; ++w) { f32x2 v = *(LAS f32x2*)(lds + G_ST + (lane * 3 + w) * 8); s_ += v.x; ss += v.y; }
      float m1, s1;
      ln_scalars(s_, ss, m1, s1);
      const float mean = s_ * (1.0f / D);
      float var = __builtin_fmaf(-mean, mean, ss * (1.0f / D));
      var = var < 0.f ? 0.f : var;
      const float r1 = __builtin_amdgcn_rcpf(s1);
      const float m2 = bfr((mean - m1) * r1);
      const float s2 = bfr(__builtin_amdgcn_sqrtf(bfr(bfr(var * r1 * r1) + 1e-5f)));
      *(LAS f32x4*)(lds + G_ST + 2048 + lane * 16) = f32x4{__builtin_fmaf(m2, s1, m1), r1 * __builtin_amdgcn_rcpf(s2), m1, r1};
    }
    __syncthreads();
    {
      LAS unsigned char* xi = lds + G_X + (kh ? wv - 6 : wv + 6) * 2048 + lane * 16;
#pragma unroll
      for (int r = 0; r < 2; ++r) acc[r] += *(LAS f32x4*)(xi + r * 1024);
    }
    STAMP(13);
    f32x2 st1[2];
#pragma unroll
    for (int r = 0; r < 2; ++r) {
      const int row = (2 * kh + r) * 16 + tok;
      const f32x4 sc = *(LAS f32x4*)(lds + G_ST + 2048 + row * 16);
      st1[r] = f32x2{sc[2], sc[3]};
      float v[4];
#pragma unroll
      for (int e = 0; e < 4; ++e) v[e] = (acc[r][e] - sc[0] * cs[e]) * sc[1];
      const unsigned p0 = cvtpk(v[0], v[1]), p1 = cvtpk(v[2], v[3]);
      *(LAS v2u*)(lds + G_O + row * G_OROW + cl * 32 + q * 8) = v2u{cvtpk(gelu_f(bflo(p0)), gelu_f(bfhi(p0))), cvtpk(gelu_f(bflo(p1)), gelu_f(bfhi(p1)))};
    }
    __syncthreads();
    const int orow = tid / 12, ocol = tid % 12;
    st16(rs, *(LAS v4u*)(lds + G_O + orow * G_OROW + ocol * 16), WS_H1 + ((tt * 64 + orow) * D + sl * 96 + ocol * 8) * 2);
    STAMP(36);
    DRAIN();
    STAMP(37);
    __syncthreads();
    if (tid == 0) __hip_atomic_store((gu64*)(ws + WS_F3) + tt * 8 + sl, tag, RLX);
    STAMP(14);

    {
      const int c = lane >> 2, pc = lane & 3;
      *(LAS v4u*)(lds + G_H + wv * 1024 + c * 64 + ((((pc >> 1) ^ ((c >> 3) & 1)) << 1 | (pc & 1)) << 4)) = hpiece;
    }
    if (wv == 0) {
      int t = tt - 1 + (lane < 18 ? lane / 6 : 1);
      t = t < 0 ? 0 : (t > 31 ? 31 : t);
      gu64* p = (gu64*)(ws + WS_F3) + t * 8 + (lane < 18 ? lane % 6 : 0);
      for (unsigned spins = 0; spins < SPIN_CAP; ++spins) {
        if (__all(__hip_atomic_load(p, RLX) == tag)) break;
      }
      ORDER();
    }
    __syncthreads();
    STAMP(15);
    {
      v4u a[6];
#pragma unroll
      for (int i = 0; i < 6; ++i) {
        const int p = tid + NT * i, r = p / 72, c = p % 72;
        const int cg = c / 18;
        const int sh = cg == 0 ? -1 : (cg == 1 ? 1 : (cg == 2 ? -2 : 2));
        const int t = tt * 64 + r + sh;
        a[i] = ld16(rs, (t >= 0 && t < L) ? WS_H1 + (t * D + c * 8) * 2 : WS_END);
      }
#pragma unroll
      for (int i = 0; i < 6; ++i) {
        const int p = tid + NT * i, r = p / 72, c = p % 72;
        *(LAS v4u*)(lds + G_A + r * ROWB + ((c ^ ((r >> 1) & 7)) << 4)) = a[i];
      }
    }
    __syncthreads();
    STAMP(16);
#pragma unroll
    for (int r = 0; r < 4; ++r) acc[r] = f32x4{0, 0, 0, 0};
    {
      const int sw = (tok >> 1) & 7;
      LAS unsigned char* pa = lds + G_A + tok * ROWB;
      v4u af[2][4];
#pragma unroll
      for (int r = 0; r < 4; ++r) af[0][r] = *(LAS v4u*)(pa + r * 16 * ROWB + (((4 * 9 * kh + q) ^ sw) << 4));
#pragma unroll
      for (int ks = 0; ks < 9; ++ks) {
        if (ks < 8) {
#pragma unroll
          for (int r = 0; r < 4; ++r) af[(ks + 1) & 1][r] = *(LAS v4u*)(pa + r * 16 * ROWB + (((4 * (9 * kh + ks + 1) + q) ^ sw) << 4));
        }
#pragma unroll
        for (int r = 0; r < 4; ++r) acc[r] = mfma_bf(bw2[ks], af[ks & 1][r], acc[r]);
        __builtin_amdgcn_sched_barrier(0);
      }
    }
    {
      LAS unsigned char* xo = lds + G_X + wv * 2048 + lane * 16;
#pragma unroll
      for (int r = 0; r < 2; ++r) {
        f32x4 lo = acc[r], hi = acc[2 + r];
        asm volatile("" : "+v"(lo), "+v"(hi));
        *(LAS f32x4*)(xo + r * 1024) = kh ? lo : hi;
        acc[r] = kh ? hi : lo;
      }
    }
    __syncthreads();
    STAMP(17);
    {
      LAS unsigned char* xi = lds + G_X + (kh ? wv - 6 : wv + 6) * 2048 + lane * 16;
#pragma unroll
      for (int r = 0; r < 2; ++r) {
        acc[r] += *(LAS f32x4*)(xi + r * 1024);
        const int c = 4 * q + ((lane & 15) >> 2);
        const v2u hh = tr_read(lds + G_H + wv * 1024 + c * 64 + ((r ^ ((c >> 3) & 1)) << 5) + (lane & 3) * 8);
        const float m1 = st1[r][0], r1 = st1[r][1];
        unsigned o[2];
#pragma unroll
        for (int e = 0; e < 2; ++e) {
          const unsigned hw = e ? hh.y : hh.x;
          const unsigned d = cvtpk(bflo(hw) - m1, bfhi(hw) - m1);
          const unsigned z = cvtpk(bflo(d) * r1, bfhi(d) * r1);
          const unsigned hs = cvtpk(acc[r][2 * e], acc[r][2 * e + 1]);
          o[e] = cvtpk(bflo(z) + bflo(hs), bfhi(z) + bfhi(hs));
        }
        const int row = (2 * kh + r) * 16 + tok;
        *(LAS v2u*)(lds + G_O + row * G_OROW + cl * 32 + q * 8) = v2u{o[0], o[1]};
        *(LAS v2u*)(lds + G_O + 64 * G_OROW + row * G_OROW + cl * 32 + q * 8) = hh;
      }
    }
    __syncthreads();
    {
      const int off = ((tt * 64 + orow) * D + sl * 96 + ocol * 8) * 2;
      st16(rs, *(LAS v4u*)(lds + G_O + orow * G_OROW + ocol * 16), WS_X1 + off);
      st16(rs, *(LAS v4u*)(lds + G_O + 64 * G_OROW + orow * G_OROW + ocol * 16), WS_HR + off);
    }
    DRAIN();
    __syncthreads();
    if (tid == 0) __hip_atomic_store((gu64*)(ws + WS_F4) + tt * 8 + sl, tag, RLX);
    STAMP(18);
  }

  if (!is_gemm) {
    const int sb = 2 * (xcd & 3), part = jj - 24;
    if (wv == 0) {
      gu64* p = (gu64*)(ws + WS_FPT + (b & 3) * 2048) + (lane < 18 ? 18 + 9 * sb + lane : (lane < 36 ? 90 + 9 * sb + lane - 18 : 18 + 9 * sb));
      for (unsigned spins = 0; spins < SPIN_CAP; ++spins) {
        if (__all(__hip_atomic_load(p, RLX) == tag)) break;
        __builtin_amdgcn_s_sleep(8);
      }
      ORDER();
    }
    __syncthreads();
    unsigned sink = 0;
    const int n3 = 72 * 18 / 8, n4 = 36 * 36 / 8;
#pragma unroll 7
    for (int f = wv; f < n3; f += 12) { v4u v = ldw(rs, lane * 16, WS_W3T + (36 * sb * 18 + part * n3 + f) * 1024); sink ^= v.x; }
#pragma unroll 7
    for (int f = wv; f < n4; f += 12) { const int g = part * n4 + f; v4u v = ldw(rs, lane * 16, WS_W4T + ((g / 36) * 144 + 18 * sb + g % 36) * 1024); sink ^= v.x; }
    asm volatile("" ::"v"(sink));
    STAMP(2);
  }
  const int grp = 2 * (jj >> 1) + (xcd >> 2), s = 2 * (xcd & 3) + (jj & 1);
  {
    constexpr int DEPTH = 3;
    if (wv == 0) {
      gu64* p = (gu64*)(ws + WS_FPT + (b & 3) * 2048) + (lane < 9 ? 18 + 9 * s + lane : (lane < 18 ? 90 + 9 * s + lane - 9 : 18 + 9 * s));
      for (unsigned spins = 0; spins < SPIN_CAP; ++spins) {
        if (__all(__hip_atomic_load(p, RLX) == tag)) break;
      }
      ORDER();
    }
    __syncthreads();
    const int o3 = WS_W3T + (36 * s + 3 * wv) * 18 * 1024, o4 = WS_W4T + (3 * wv * 144 + 18 * s) * 1024;
    v4u bw[DEPTH][3];
    if (wv != 0) {
#pragma unroll
      for (int d = 0; d < DEPTH; ++d)
#pragma unroll
        for (int c = 0; c < 3; ++c) bw[d][c] = ldw(rs, lane * 16, o3 + (c * 18 + d) * 1024);
    }
    if (wv == 0) {
      gu64* p = (gu64*)(ws + WS_F4) + grp * 8 + (lane < 6 ? lane : 0);
      for (unsigned spins = 0; spins < SPIN_CAP; ++spins) {
        if (__all(__hip_atomic_load(p, RLX) == tag)) break;
      }
      ORDER();
#pragma unroll
      for (int d = 0; d < DEPTH; ++d)
#pragma unroll
        for (int c = 0; c < 3; ++c) bw[d][c] = ldw(rs, lane * 16, o3 + (c * 18 + d) * 1024);
    }
    __syncthreads();
    STAMP(19);
    f32x4 cs3[3];
#pragma unroll
    for (int c = 0; c < 3; ++c) cs3[c] = __builtin_bit_cast(f32x4, ld16(rs, WS_CS3 + ((36 * s + 3 * wv + c) * 16 + 4 * (lane >> 4)) * 4));
    if (tid < 4) ((LAS unsigned*)(lds + ML_ST + 512))[tid] = 0;
    {
      v4u a[6];
#pragma unroll
      for (int i = 0; i < 6; ++i) a[i] = ld16(rs, WS_X1 + grp * 64 * ROWB + (tid + NT * i) * 16);
#pragma unroll
      for (int i = 0; i < 6; ++i) {
        int p = tid + NT * i, r = p / 72, c = p % 72;
        *(LAS v4u*)(lds + ML_A + r * ROWB + ((c ^ ((r >> 1) & 7)) << 4)) = a[i];
      }
    }
    __syncthreads();
    STAMP(20);
    STAMPC(28);
    LAS unsigned* mcnt = (LAS unsigned*)(lds + ML_ST + 512);
    if (wv >= 8) {
#pragma unroll
      for (int ps = 0; ps < 2; ++ps) {
        const int r = (wv - 8) * 16 + ps * 8 + (lane >> 3), sub = lane & 7;
        float sm = 0.f, ss = 0.f;
        const unsigned ones = 0x3F803F80u;
#pragma unroll
        for (int i = 0; i < 9; ++i) {
          v4u a = *(LAS v4u*)(lds + ML_A + r * ROWB + (sub + 8 * i) * 16);
#pragma unroll
          for (int j = 0; j < 4; ++j) { sm = dot2bf(a[j], ones, sm); ss = dot2bf(a[j], a[j], ss); }
        }
#pragma unroll
        for (int o = 1; o < 8; o <<= 1) { sm += __shfl_xor(sm, o); ss += __shfl_xor(ss, o); }
        float m, sd;
        ln_scalars(sm, ss, m, sd);
        if (sub == 0) *(LAS f32x2*)(lds + ML_ST + r * 8) = f32x2{m, __builtin_amdgcn_rcpf(sd)};
      }
      lds_arrive(mcnt, lane);
    }
    const int tok = lane & 15, q = lane >> 4;
    const int sw = (tok >> 1) & 7;
    LAS unsigned char* abase = lds + ML_A + tok * ROWB;
    f32x4 acc[4][3];
#pragma unroll
    for (int r = 0; r < 4; ++r)
#pragma unroll
      for (int c = 0; c < 3; ++c) acc[r][c] = f32x4{0, 0, 0, 0};
    __builtin_amdgcn_s_setprio(2);
    v4u af[2][4];
#pragma unroll
    for (int r = 0; r < 4; ++r) af[0][r] = *(LAS v4u*)(abase + r * 16 * ROWB + ((q ^ sw) << 4));
#pragma unroll
    for (int ks = 0; ks < 18; ++ks) {
      if (ks < 17) {
#pragma unroll
        for (int r = 0; r < 4; ++r) af[(ks + 1) & 1][r] = *(LAS v4u*)(abase + r * 16 * ROWB + (((4 * (ks + 1) + q) ^ sw) << 4));
      }
      v4u cur[3];
#pragma unroll
      for (int c = 0; c < 3; ++c) {
        cur[c] = bw[ks % DEPTH][c];
        int nk = ks + DEPTH;
        if (nk < 18) bw[ks % DEPTH][c] = ldw(rs, lane * 16, o3 + (c * 18 + nk) * 1024);
        else bw[ks % DEPTH][c] = ldw(rs, lane * 16, o4 + (c * 144 + nk - 18) * 1024);
      }
#pragma unroll
      for (int c = 0; c < 3; ++c)
#pragma unroll
        for (int r = 0; r < 4; ++r) acc[r][c] = mfma_bf(cur[c], af[ks & 1][r], acc[r][c]);
      __builtin_amdgcn_sched_barrier(0);
    }
    __builtin_amdgcn_s_setprio(0);
    STAMP(21);
    lds_wait(mcnt, 4);
    STAMP(22);
    LAS unsigned char* H = lds + ML_H;
    {
      f32x2 st[4];
#pragma unroll
      for (int r = 0; r < 4; ++r) st[r] = *(LAS f32x2*)(lds + ML_ST + (r * 16 + tok) * 8);
#pragma unroll
      for (int c = 0; c < 3; ++c) {
#pragma unroll
        for (int r = 0; r < 4; ++r) {
          float v[4];
#pragma unroll
          for (int e = 0; e < 4; ++e) v[e] = gelu_f((acc[r][c][e] - st[r].x * cs3[c][e]) * st[r].y);
          v2u o = {cvtpk(v[0], v[1]), cvtpk(v[2], v[3])};
          const int chunk = 2 * (3 * wv + c) + (q >> 1);
          *(LAS v2u*)(H + (r * 16 + tok) * ROWB + ((chunk ^ sw) << 4) + (q & 1) * 8) = o;
          acc[r][c] = f32x4{0, 0, 0, 0};
        }
      }
    }
    lds_arrive(mcnt + 1 + (wv >> 2), lane);
    STAMP(23);
    LAS unsigned char* hbase = H + tok * ROWB;
#pragma unroll
    for (int k2 = 0; k2 < 18; ++k2) {
      const int ks = 18 + k2;
      if (k2 % 6 == 0) {
        lds_wait(mcnt + 1 + k2 / 6, 4);
#pragma unroll
        for (int r = 0; r < 4; ++r) af[k2 & 1][r] = *(LAS v4u*)(hbase + r * 16 * ROWB + (((4 * k2 + q) ^ sw) << 4));
      }
      if (k2 % 6 != 5) {
#pragma unroll
        for (int r = 0; r < 4; ++r) af[(k2 + 1) & 1][r] = *(LAS v4u*)(hbase + r * 16 * ROWB + (((4 * (k2 + 1) + q) ^ sw) << 4));
      }
      v4u cur[3];
#pragma unroll
      for (int c = 0; c < 3; ++c) {
        cur[c] = bw[ks % DEPTH][c];
        int nk = k2 + DEPTH;
        if (nk < 18) bw[ks % DEPTH][c] = ldw(rs, lane * 16, o4 + (c * 144 + nk) * 1024);
      }
#pragma unroll
      for (int c = 0; c < 3; ++c)
#pragma unroll
        for (int r = 0; r < 4; ++r) acc[r][c] = mfma_bf(cur[c], af[k2 & 1][r], acc[r][c]);
      __builtin_amdgcn_sched_barrier(0);
    }
    STAMP(24);
    {
      const int po = WS_PART + (s * 32 + grp) * 72 * 1024;
#pragma unroll
      for (int c = 0; c < 3; ++c)
#pragma unroll
        for (int rp = 0; rp < 2; ++rp) {
          unsigned x0 = cvtpk(acc[2 * rp][c][0], acc[2 * rp][c][1]), x1 = cvtpk(acc[2 * rp][c][2], acc[2 * rp][c][3]);
          unsigned y0 = cvtpk(acc[2 * rp + 1][c][0], acc[2 * rp + 1][c][1]), y1 = cvtpk(acc[2 * rp + 1][c][2], acc[2 * rp + 1][c][3]);
          auto s0 = __builtin_amdgcn_permlane16_swap(x0, y0, false, false);
          auto s1 = __builtin_amdgcn_permlane16_swap(x1, y1, false, false);
          st16(rs, v4u{s0[0], s1[0], s0[1], s1[1]}, lane * 16, po + (rp * 36 + 3 * wv + c) * 1024);
        }
    }
    DRAIN();
    __syncthreads();
    if (tid == 0) __hip_atomic_store((gu64*)(ws + WS_F5) + grp * 8 + s, tag, RLX);
    STAMP(25);
    STAMPC(29);
  }

  {
    if (wv == 0) wait_flags(ws, WS_F5 + grp * 64, 8, tag, lane);
    __syncthreads();
    STAMP(26);
    if (wv < 9) {
      const int rp = s & 1, oc = 9 * (s >> 1) + wv, u = rp * 36 + oc;
      const int q = lane >> 4, tok = lane & 15;
      const int off = ((grp * 64 + rp * 32 + (q & 1) * 16 + tok) * D + oc * 16 + 8 * (q >> 1)) * 2;
      v4u p[8];
#pragma unroll
      for (int k = 0; k < 8; ++k) p[k] = ld16(rs, lane * 16, WS_PART + ((k * 32 + grp) * 72 + u) * 1024);
      const v4u xv = ld16(rs, WS_X1 + off), hv = ld16(rs, WS_HR + off);
      v4u o;
#pragma unroll
      for (int j = 0; j < 4; ++j) {
        float lo = 0.f, hi = 0.f;
#pragma unroll
        for (int k = 0; k < 8; ++k) { lo += bflo(p[k][j]); hi += bfhi(p[k][j]); }
        lo = bflo(hv[j]) + bfr(bflo(xv[j]) + bfr(lo));
        hi = bfhi(hv[j]) + bfr(bfhi(xv[j]) + bfr(hi));
        o[j] = cvtpk(lo, hi);
      }
      *(GAS v4u*)((GAS unsigned char*)out + off) = o;
    }
    if (b == 0 && tid == 0) __hip_atomic_store((gu64*)(ws + WS_CTRL), tag, RLX);
    STAMP(27);
  }
}

extern "C" void kernel_launch(void* const* d_in, const int* in_sizes, int n_in, void* d_out, int out_size, void* d_ws, size_t ws_size,
                              hipStream_t stream) {
  (void)hipFuncSetAttribute((const void*)k_all, hipFuncAttributeMaxDynamicSharedMemorySize, LDS_BYTES);
  const unsigned short* const* in = (const unsigned short* const*)d_in;
  k_all<<<256, NT, LDS_BYTES, stream>>>(in[0], in[1], in[2], in[3], in[4], in[5], in[6], in[7], in[8], in[10], in[11], in[12], in[13], (unsigned char*)d_ws,
                                        (unsigned short*)d_out);
}
```

```cpp
#include <hip/hip_runtime.h>
#include <hip/hip_bf16.h>
#include <stdint.h>

typedef unsigned long long u64;
typedef unsigned v4u __attribute__((ext_vector_type(4)));
typedef unsigned v2u __attribute__((ext_vector_type(2)));
typedef float f32x4 __attribute__((ext_vector_type(4)));
typedef float f32x2 __attribute__((ext_vector_type(2)));
typedef short s16x8 __attribute__((ext_vector_type(8)));
typedef _Float16 h16x8 __attribute__((ext_vector_type(8)));
typedef _Float16 h16x2 __attribute__((ext_vector_type(2)));
typedef __bf16 bf16x2_t __attribute__((ext_vector_type(2)));
#define GAS __attribute__((address_space(1)))
#define LAS __attribute__((address_space(3)))
#define DI __device__ __forceinline__
#define RLX __ATOMIC_RELAXED, __HIP_MEMORY_SCOPE_AGENT
typedef GAS u64 gu64;
typedef __amdgpu_buffer_rsrc_t rsrc_t;

#define NLAUNCH 1

constexpr int L = 2048, D = 576, NS = 128, F = 4608;
constexpr int NT = 768;

constexpr int WS_CTRL = 0;
constexpr int WS_F1 = 4096;
constexpr int WS_FPT = WS_F1 + 8 * 2048;
constexpr int WS_F2 = WS_FPT + 4 * 2048;
constexpr int WS_F3 = WS_F2 + 8 * 2048;
constexpr int WS_F4 = WS_F3 + 32 * 64;
constexpr int WS_F5 = WS_F4 + 32 * 64;
constexpr int WS_W1T = 131072;
constexpr int WS_W2T = WS_W1T + 36 * 18 * 1024;
constexpr int WS_W3T = WS_W2T + 36 * 18 * 1024;
constexpr int WS_W4T = WS_W3T + 288 * 18 * 1024;
constexpr int WS_CS1 = WS_W4T + 36 * 144 * 1024;
constexpr int WS_CS3 = WS_CS1 + 4096;
constexpr int WS_HT = WS_CS3 + 4608 * 4;
constexpr int WS_H1 = WS_HT + D * L * 2;
constexpr int WS_ST2 = WS_H1 + D * L * 2;
constexpr int WS_X1 = WS_ST2 + 2048 * 8;
constexpr int WS_HR = WS_X1 + D * L * 2;
constexpr int WS_PART = WS_HR + D * L * 2;
constexpr int WS_UX = WS_PART + 8 * D * L * 2;
constexpr int WS_END = WS_UX + 256 * 144 * 128;
static_assert(WS_F5 + 32 * 64 <= WS_W1T, "control block");

#define STAMP(i)
#define STAMPW(w, i)
#define STAMPC(i)
#define STAMPV(i)
#define STAMPL(i)

DI float bf2f(unsigned short b) { return __builtin_bit_cast(float, (unsigned)b << 16); }
DI float asf(unsigned u) { return __builtin_bit_cast(float, u); }
DI unsigned asu(float f) { return __builtin_bit_cast(unsigned, f); }
DI float bflo(unsigned u) { return __builtin_bit_cast(float, u << 16); }
DI float bfhi(unsigned u) { return __builtin_bit_cast(float, u & 0xFFFF0000u); }
DI unsigned cvtpk(float lo, float hi) {
  f32x2 v = {lo, hi};
  bf16x2_t b = __builtin_convertvector(v, bf16x2_t);
  return __builtin_bit_cast(unsigned, b);
}
DI float bfr(float x) { return bflo(cvtpk(x, x)); }
DI unsigned cvtpkh(float lo, float hi) {
  f32x2 v = {lo, hi};
  h16x2 b = __builtin_convertvector(v, h16x2);
  return __builtin_bit_cast(unsigned, b);
}
DI f32x4 mfma_bf(v4u a, v4u b, f32x4 c) {
  return __builtin_amdgcn_mfma_f32_16x16x32_bf16(__builtin_bit_cast(s16x8, a), __builtin_bit_cast(s16x8, b), c, 0, 0, 0);
}
DI f32x4 mfma_h(v4u a, v4u b, f32x4 c) {
  return __builtin_amdgcn_mfma_f32_16x16x32_f16(__builtin_bit_cast(h16x8, a), __builtin_bit_cast(h16x8, b), c, 0, 0, 0);
}
DI float dot2bf(unsigned a, unsigned b, float acc) {
  return __builtin_amdgcn_fdot2_f32_bf16(__builtin_bit_cast(bf16x2_t, a), __builtin_bit_cast(bf16x2_t, b), acc, false);
}
DI v2u tr_read(LAS unsigned char* p) {
  typedef short v4i16_t __attribute__((ext_vector_type(4)));
  return __builtin_bit_cast(v2u, __builtin_amdgcn_ds_read_tr16_b64_v4i16((LAS v4i16_t*)p));
}
DI float gelu_f(float x) {
  const float c0 = -2.3022081986f, c1 = -0.10294324f;
  float x2 = x * x;
  float p = __builtin_fmaf(x2, c1, c0);
  float e = __builtin_amdgcn_exp2f(x * p);
  return x * __builtin_amdgcn_rcpf(1.0f + e);
}
DI f32x2 gelu2(f32x2 x) {
  const f32x2 c0 = {-2.3022081986f, -2.3022081986f}, c1 = {-0.10294324f, -0.10294324f}, one = {1.0f, 1.0f};
  const f32x2 e = x * __builtin_elementwise_fma(x * x, c1, c0);
  const f32x2 d = f32x2{__builtin_amdgcn_exp2f(e[0]), __builtin_amdgcn_exp2f(e[1])} + one;
  return x * f32x2{__builtin_amdgcn_rcpf(d[0]), __builtin_amdgcn_rcpf(d[1])};
}
DI void ln_scalars(float sum, float sumsq, float& m, float& s) {
  float mean = sum * (1.0f / D);
  float var = __builtin_fmaf(-mean, mean, sumsq * (1.0f / D));
  var = var < 0.f ? 0.f : var;
  m = bfr(mean);
  s = bfr(__builtin_amdgcn_sqrtf(bfr(bfr(var) + 1e-5f)));
}
constexpr int SC1 = 16;
DI v4u ld16(rsrc_t rs, int voff, int soff = 0) { return __builtin_amdgcn_raw_buffer_load_b128(rs, voff, soff, SC1); }
#define WAUX 16
DI v4u ldw(rsrc_t rs, int voff, int soff = 0) { return __builtin_amdgcn_raw_buffer_load_b128(rs, voff, soff, WAUX); }
DI v2u ld8(rsrc_t rs, int voff, int soff = 0) { return __builtin_amdgcn_raw_buffer_load_b64(rs, voff, soff, SC1); }
DI void st16(rsrc_t rs, v4u v, int voff, int soff = 0) { __builtin_amdgcn_raw_buffer_store_b128(v, rs, voff, soff, SC1); }
DI void st8(rsrc_t rs, v2u v, int voff, int soff = 0) { __builtin_amdgcn_raw_buffer_store_b64(v, rs, voff, soff, SC1); }
DI void st4(rsrc_t rs, unsigned v, int voff, int soff = 0) { __builtin_amdgcn_raw_buffer_store_b32(v, rs, voff, soff, SC1); }
#define DRAIN() asm volatile("s_waitcnt vmcnt(0)" ::: "memory")
#define ORDER() __builtin_amdgcn_fence(__ATOMIC_ACQUIRE, "wavefront")
constexpr unsigned SPIN_CAP = 1u << 15;

#define PSLEEP 8
DI void poll_tag(gu64* p, u64 tag) {
  for (unsigned spins = 0; spins < SPIN_CAP; ++spins) {
    if (__all(__hip_atomic_load(p, RLX) == tag)) break;
    if (PSLEEP) __builtin_amdgcn_s_sleep(PSLEEP);
  }
  ORDER();
}

struct KIdx { unsigned short v[2080]; };
constexpr KIdx make_kidx() {
  KIdx t{};
  for (int y = 0; y < 2080; ++y) {
    if (y >= 2048) { t.v[y] = 768; continue; }
    int j = 2047 - y;
    int v = j;
    if (j >= 256) {
      int sh = j >= 1024 ? 3 : (j >= 512 ? 2 : 1);
      int step = 1 << sh, lo = (j >> sh) << sh, rem = j - lo, half = step >> 1;
      if (rem > half || (rem == half && (((lo >> sh) & 1) != 0))) lo += step;
      v = lo;
    }
    int seg = v >= 1024 ? 3 : (v >= 512 ? 2 : (v >= 256 ? 1 : 0));
    int u = seg == 0 ? v : v - (128 << seg);
    int rb = seg == 0 ? 0 : (seg == 1 ? 16 : (seg == 2 ? 24 : 32));
    int row = rb + ((u & 7) >> seg) + (u >> 7) * (8 >> seg);
    int col = (u >> 3) & 15;
    t.v[y] = (unsigned short)(row * 16 + col);
  }
  return t;
}
__device__ const KIdx kIdxTab = make_kidx();

struct cplx { float r, i; };
DI cplx cmul(cplx a, cplx b) {
  const f32x2 t = f32x2{-a.i, a.i} * f32x2{b.i, b.r};
  const f32x2 r = __builtin_elementwise_fma(f32x2{a.r, a.r}, f32x2{b.r, b.i}, t);
  return cplx{r[0], r[1]};
}
DI cplx cexp_r(float a2, float th, float r) {
  float m = __builtin_amdgcn_exp2f(a2 * r);
  float ang = __builtin_amdgcn_fractf(th * r);
  return cplx{m * __builtin_amdgcn_cosf(ang), m * __builtin_amdgcn_sinf(ang)};
}

constexpr int CV_TAB = 57 * 256;
constexpr int CV_CH = 2 * CV_TAB;
constexpr int CV_KC = 4160;
constexpr int CV_XCH = 4 * CV_KC;
constexpr int CV_KCT = 152 * 1024;
constexpr int CV_U = 4 * CV_CH;
constexpr int CV_UCH = (256 + 2048) * 2;
constexpr int CV_X = CV_U + 4 * CV_UCH;
constexpr int LDS_BYTES = 160 * 1024;
constexpr int CV_MISC = LDS_BYTES - 512;
static_assert(4 * CV_KC + 8192 <= CV_CH, "conv channel region");
constexpr int PR_ROW = 1168;
constexpr int PR_U = CV_U, PR_X = CV_U + 8 * PR_ROW;
constexpr int ROWB = 1152;
constexpr int G_A = 0;
constexpr int G_ST = 73728;
constexpr int G_X = G_ST + 4096;
constexpr int G_H = G_X + 12 * 2048;
constexpr int G_O = G_H + 12 * 1024;
constexpr int G_OROW = 208;
constexpr int ML_A = 0, ML_H = 64 * ROWB, ML_ST = 2 * 64 * ROWB;
constexpr int ML_HR = ML_ST + 544;
constexpr int ML_CS = ML_HR + 9216;
static_assert(CV_X + 4 * 4096 <= CV_KCT && CV_KCT + 4 * 1600 <= CV_MISC && ML_CS + 2304 <= CV_MISC && G_O + 2 * 64 * G_OROW <= CV_MISC && PR_X + 8 * PR_ROW <= CV_MISC, "LDS");

DI void lds_arrive_wait(LAS unsigned* ctr, unsigned target, int lane) {
  asm volatile("s_waitcnt lgkmcnt(0)" ::: "memory");
  if (lane == 0) __hip_atomic_fetch_add(ctr, 1u, __ATOMIC_RELAXED, __HIP_MEMORY_SCOPE_WORKGROUP);
  while (__hip_atomic_load(ctr, __ATOMIC_RELAXED, __HIP_MEMORY_SCOPE_WORKGROUP) < target) __builtin_amdgcn_s_sleep(1);
  asm volatile("" ::: "memory");
}

DI void lds_arrive(LAS unsigned* ctr, int lane) {
  asm volatile("s_waitcnt lgkmcnt(0)" ::: "memory");
  if (lane == 0) __hip_atomic_fetch_add(ctr, 1u, __ATOMIC_RELAXED, __HIP_MEMORY_SCOPE_WORKGROUP);
  asm volatile("" ::: "memory");
}
DI bool drain_arrive_last(LAS unsigned* ctr, unsigned n, int lane) {
  DRAIN();
  unsigned old = 0;
  if (lane == 0) old = __hip_atomic_fetch_add(ctr, 1u, __ATOMIC_RELAXED, __HIP_MEMORY_SCOPE_WORKGROUP);
  return (unsigned)__builtin_amdgcn_readfirstlane(old) == n - 1;
}
DI void lds_wait(LAS unsigned* ctr, unsigned target) {
  while (__hip_atomic_load(ctr, __ATOMIC_RELAXED, __HIP_MEMORY_SCOPE_WORKGROUP) < target) __builtin_amdgcn_s_sleep(1);
  asm volatile("" ::: "memory");
}
DI void pretile_unit(LAS unsigned char* lds, rsrc_t rs, unsigned char* ws, int unit, u64 tag, const unsigned short* __restrict__ w1, const unsigned short* __restrict__ w2,
                     const unsigned short* __restrict__ w3, const unsigned short* __restrict__ w4) {
  const int tid = threadIdx.x, lane = tid & 63, wv = __builtin_amdgcn_readfirstlane(tid >> 6);
  const unsigned short* w;
  int ldw, dst, ksn, cs = -1;
  if (unit < 9) { w = w1 + unit * 64; ldw = D; dst = WS_W1T + unit * 4 * 18 * 1024; ksn = 18; cs = WS_CS1 + unit * 256; }
  else if (unit < 18) { w = w2 + (unit - 9) * 64; ldw = D; dst = WS_W2T + (unit - 9) * 4 * 18 * 1024; ksn = 18; }
  else if (unit < 90) { w = w3 + (unit - 18) * 64; ldw = F; dst = WS_W3T + (unit - 18) * 4 * 18 * 1024; ksn = 18; cs = WS_CS3 + (unit - 18) * 256; }
  else { int u = unit - 90, kc = u / 9, cg = u % 9; w = w4 + (size_t)kc * 576 * D + cg * 64; ldw = D; dst = WS_W4T + (cg * 4 * 144 + kc * 18) * 1024; ksn = 144; }
  v4u a[6];
#pragma unroll
  for (int i = 0; i < 6; ++i) {
    int p = tid + NT * i, r = p >> 3, c = p & 7;
    a[i] = *(const GAS v4u*)(w + (size_t)r * ldw + c * 8);
  }
  __syncthreads();
#pragma unroll
  for (int i = 0; i < 6; ++i) {
    int p = tid + NT * i, r = p >> 3, c = p & 7;
    int f = ((r >> 1) & 1) | (((r >> 3) & 1) << 1);
    *(LAS v4u*)(lds + r * 128 + ((((c >> 1) ^ f) << 1 | (c & 1)) << 4)) = a[i];
  }
  __syncthreads();
  const int ct = wv & 3, k0 = 6 * (wv >> 2);
  const int g = lane >> 4, q = (lane & 15) >> 2, p = lane & 3;
  const int f = ((q >> 1) & 1) | ((g & 1) << 1);
  float csum = 0.f;
  const unsigned ones = 0x3F803F80u;
#pragma unroll
  for (int ks = 0; ks < 6; ++ks) {
    LAS unsigned char* pa = lds + (32 * (k0 + ks) + 8 * g + q) * 128 + ((ct ^ f) << 5) + p * 8;
    v2u lo = tr_read(pa), hi = tr_read(pa + 4 * 128);
    v4u o = {lo.x, lo.y, hi.x, hi.y};
    st16(rs, o, lane * 16, dst + (ct * ksn + k0 + ks) * 1024);
    csum = dot2bf(o.x, ones, csum); csum = dot2bf(o.y, ones, csum); csum = dot2bf(o.z, ones, csum); csum = dot2bf(o.w, ones, csum);
  }
  if (cs >= 0) {
    csum += __shfl_xor(csum, 16);
    csum += __shfl_xor(csum, 32);
    LAS float* pc = (LAS float*)(lds + 73728);
    if (lane < 16) pc[wv * 16 + lane] = csum;
    __syncthreads();
    if (tid < 64) st4(rs, __builtin_bit_cast(unsigned, pc[tid] + pc[64 + tid] + pc[128 + tid]), cs + tid * 4);
  }
  DRAIN();
  __syncthreads();
  if (tid < 4) __hip_atomic_store((gu64*)(ws + WS_FPT) + tid * 256 + unit, tag, RLX);
}

__global__ __launch_bounds__(NT, 3) void k_all(const unsigned short* __restrict__ x, const unsigned short* __restrict__ Are, const unsigned short* __restrict__ Aim,
                                               const unsigned short* __restrict__ Bre, const unsigned short* __restrict__ Bim, const unsigned short* __restrict__ Cre,
                                               const unsigned short* __restrict__ Cim, const unsigned short* __restrict__ logdt, const unsigned short* __restrict__ dskip,
                                               const unsigned short* __restrict__ w1, const unsigned short* __restrict__ w2, const unsigned short* __restrict__ w3,
                                               const unsigned short* __restrict__ w4, unsigned char* __restrict__ ws, unsigned short* __restrict__ out) {
  extern __shared__ __attribute__((aligned(16))) unsigned char smem[];
  LAS unsigned char* lds = (LAS unsigned char*)smem;
  const int tid = threadIdx.x, lane = tid & 63, wv = __builtin_amdgcn_readfirstlane(tid >> 6);
  const int b = blockIdx.x, xcd = b & 7, jj = b >> 3;
  const rsrc_t rs = __builtin_amdgcn_make_buffer_rsrc((void*)ws, 0, WS_END, 0x00020000);
  STAMP(0);
  STAMPC(30);
  const bool is_conv = jj < 18, is_gemm = jj < 24;
  const int c0 = 4 * (18 * xcd + jj);
  if (tid < 16) ((LAS unsigned*)(lds + CV_MISC))[tid] = 0;
  asm volatile("s_waitcnt lgkmcnt(0)" ::: "memory");
  asm volatile("" ::"s"(x), "s"(Are), "s"(Aim), "s"(Bre), "s"(Bim), "s"(Cre), "s"(Cim), "s"(logdt), "s"(ws));
  const u64 epoch = __hip_atomic_load((gu64*)(ws + WS_CTRL), RLX);
#define GET_TAG() ((((u64)(unsigned)__builtin_amdgcn_readfirstlane((unsigned)(epoch >> 32)) << 32) | (unsigned)__builtin_amdgcn_readfirstlane((unsigned)epoch)) + 1)
  u64 tag;
  LAS unsigned* cnt = (LAS unsigned*)(lds + CV_MISC);
  LAS float* smx = (LAS float*)(lds + CV_MISC + 64);
  LAS float* kscs = (LAS float*)(lds + CV_MISC + 96);

  if (wv < 4) {
    if (wv < 3) {
      const int t8 = lane >> 3, sub = lane & 7;
      const GAS v4u* row = (const GAS v4u*)(x + (size_t)(b * 8 + t8) * D) + 24 * wv + sub;
      v4u a[3];
#pragma unroll
      for (int i = 0; i < 3; ++i) a[i] = row[8 * i];
      __builtin_amdgcn_sched_barrier(0);
      __builtin_amdgcn_s_barrier();
      tag = GET_TAG();
      float s = 0.f, ss = 0.f;
      const unsigned ones = 0x3F803F80u;
#pragma unroll
      for (int i = 0; i < 3; ++i) {
#pragma unroll
        for (int j = 0; j < 4; ++j) { s = dot2bf(a[i][j], ones, s); ss = dot2bf(a[i][j], a[i][j], ss); }
      }
#pragma unroll
      for (int o = 1; o < 8; o <<= 1) { s += __shfl_xor(s, o); ss += __shfl_xor(ss, o); }
      STAMP(38);
      LAS f32x2* part = (LAS f32x2*)(lds + CV_MISC + 128);
      if (sub == 0) part[wv * 8 + t8] = f32x2{s, ss};
#pragma unroll
      for (int i = 0; i < 3; ++i) *(LAS v4u*)(lds + PR_X + t8 * PR_ROW + (24 * wv + 8 * i + sub) * 16) = a[i];
      lds_arrive_wait(cnt + 4, 3, lane);
      STAMP(39);
      {
        const f32x2 p0 = part[t8], p1 = part[8 + t8], p2 = part[16 + t8];
        float m, sd;
        ln_scalars(p0.x + p1.x + p2.x, p0.y + p1.y + p2.y, m, sd);
        const float r = __builtin_amdgcn_rcpf(sd);
#pragma unroll
        for (int i = 0; i < 3; ++i) {
          v4u up;
#pragma unroll
          for (int j = 0; j < 4; ++j) {
            const unsigned d = cvtpk(bflo(a[i][j]) - m, bfhi(a[i][j]) - m);
            const unsigned u2 = cvtpk(bflo(d) * r, bfhi(d) * r);
            up[j] = cvtpkh(bflo(u2), bfhi(u2));
          }
          *(LAS v4u*)(lds + PR_U + t8 * PR_ROW + (24 * wv + 8 * i + sub) * 16) = up;
        }
      }
      {
        const int g = lane >> 4, q = (lane & 15) >> 2, p = lane & 3;
        LAS unsigned char* pa = lds + ((p & 1) ? PR_X : PR_U) + q * PR_ROW + (192 * wv + 8 * g + 4 * (p >> 1)) * 2;
#pragma unroll
        for (int k = 0; k < 6; ++k) {
          v2u lo = tr_read(pa + k * 64), hi = tr_read(pa + k * 64 + 4 * PR_ROW);
          st16(rs, v4u{lo.x, lo.y, hi.x, hi.y}, lane * 16, WS_UX + b * 18432 + (6 * wv + k) * 1024);
        }
      }
      STAMP(40);
      DRAIN();
      STAMP(41);
      lds_arrive_wait(cnt + 4, 6, lane);
      if (tid < 8) __hip_atomic_store((gu64*)(ws + WS_F1) + tid * 256 + b, tag, RLX);
      STAMP(1);
    } else {
      __builtin_amdgcn_s_barrier();
      tag = GET_TAG();
    }
    if (is_conv) {
      poll_tag((gu64*)(ws + WS_F1 + (b & 7) * 2048) + 64 * wv + lane, tag);
      STAMP(3);
      v4u a[8];
#pragma unroll
      for (int i = 0; i < 8; ++i) a[i] = ld16(rs, (lane >> 3) * 18432 + (lane & 7) * 16, WS_UX + (64 * wv + 8 * i) * 18432 + (c0 >> 2) * 128);
      if (wv == 3) lds_arrive_wait(cnt + 4, 6, 64);
      const int pc = lane & 7;
      LAS unsigned char* dst = lds + (pc < 4 ? CV_U + pc * CV_UCH + 512 : CV_X + (pc - 4) * 4096) + (64 * wv + (lane >> 3)) * 16;
#pragma unroll
      for (int i = 0; i < 8; ++i) *(LAS v4u*)(dst + i * 128) = a[i];
      if (tid < 128) *(LAS v4u*)(lds + CV_U + (tid >> 5) * CV_UCH + (tid & 31) * 16) = v4u{0, 0, 0, 0};
      STAMP(4);
      STAMPV(10);
    }
  } else if (is_conv) {
    const int cl = (wv - 4) >> 1, half = wv & 1, ch = c0 + cl, n = half * 64 + lane;
    const int gi = ch * NS + n;
    const int t2 = half * 64 + lane;
    const unsigned short rld = logdt[ch], rar = Are[gi], rai = Aim[gi], rbr = Bre[gi], rbi = Bim[gi], rcr = Cre[gi], rci = Cim[gi];
    v4u kid[3];
    v2u kid2[3];
#pragma unroll
    for (int i = 0; i < 3; ++i) {
      const int p = t2 + 128 * i < 259 ? t2 + 128 * i : 258;
      kid[i] = *(const GAS v4u*)(kIdxTab.v + p * 8);
      kid2[i] = *(const GAS v2u*)(kIdxTab.v + p * 8 + 8);
    }
    __builtin_amdgcn_sched_barrier(0);
    __builtin_amdgcn_s_barrier();
    tag = GET_TAG();
    {
      const float dt = bfr(expf(bf2f(rld)));
      const float zr = dt * bf2f(rar), zi = dt * bf2f(rai);
      const float dBr = dt * bf2f(rbr), dBi = dt * bf2f(rbi);
      cplx W = cmul(cplx{bf2f(rcr), bf2f(rci)}, cplx{dBr, dBi});
      float smax = fmaxf(fabsf(W.r), fabsf(W.i));
#pragma unroll
      for (int o = 1; o < 64; o <<= 1) smax = fmaxf(smax, __shfl_xor(smax, o));
      const float inv = smax > 0.f ? 1.0f / smax : 0.f;
      W.r *= inv; W.i *= inv;
      const float a2 = zr * 1.44269504089f, th = zi * 0.15915494309f;
      const cplx e1 = cexp_r(a2, th, 1.f), e8 = cexp_r(a2, th, 8.f), e128 = cexp_r(a2, th, 128.f);
      const cplx e256 = cexp_r(a2, th, 256.f), e512 = cexp_r(a2, th, 512.f), e1024 = cexp_r(a2, th, 1024.f);
      STAMPW(4, 5);
      STAMPV(15);
      LAS unsigned char* chan = lds + cl * CV_CH;
      LAS unsigned char* tab = chan + half * CV_TAB;
      const int nsw = lane >> 2, nlo = (lane & 3) * 4;
#define PUT(row, val) *(LAS unsigned*)(tab + (row) * 256 + ((nsw ^ ((row) & 15)) << 4) + nlo) = (val)
#define PUTA(row, z) PUT(row, cvtpkh((z).r, -(z).i))
#define PUTB(row, z) PUT(41 + (row), cvtpkh((z).r, (z).i))
      {
        cplx c[16];
        c[0] = cplx{1.f, 0.f}; c[1] = e8; c[2] = cmul(e8, e8); c[3] = cmul(c[2], e8); c[4] = cmul(c[2], c[2]);
        c[5] = cmul(c[4], c[1]); c[6] = cmul(c[4], c[2]); c[7] = cmul(c[4], c[3]); c[8] = cmul(c[4], c[4]);
#pragma unroll
        for (int j = 9; j < 16; ++j) c[j] = cmul(c[8], c[j - 8]);
#pragma unroll
        for (int j = 0; j < 16; ++j) PUTB(j, c[j]);
      }
      {
        const cplx e2 = cmul(e1, e1), e4 = cmul(e2, e2);
        cplx p[8];
        p[0] = W; p[1] = cmul(W, e1); p[2] = cmul(W, e2); p[3] = cmul(p[1], e2);
#pragma unroll
        for (int c = 4; c < 8; ++c) p[c] = cmul(p[c - 4], e4);
#pragma unroll
        for (int c = 0; c < 8; ++c) {
          const cplx qv = cmul(p[c], e128);
          PUTA(c, p[c]);
          PUTA(8 + c, qv);
          if ((c & 1) == 0) {
            PUTA(16 + (c >> 1), cmul(p[c], e256));
            PUTA(20 + (c >> 1), cmul(qv, e256));
          }
        }
        const cplx e384 = cmul(e256, e128);
        const cplx s0 = cmul(p[0], e512), s1 = cmul(p[4], e512);
        PUTA(24, s0); PUTA(25, s1);
        PUTA(26, cmul(s0, e128)); PUTA(27, cmul(s1, e128));
        PUTA(28, cmul(s0, e256)); PUTA(29, cmul(s1, e256));
        PUTA(30, cmul(s0, e384)); PUTA(31, cmul(s1, e384));
        const cplx t0 = cmul(W, e1024), t4 = cmul(t0, e512);
        PUTA(32, t0); PUTA(33, cmul(t0, e128)); PUTA(34, cmul(t0, e256)); PUTA(35, cmul(t0, e384));
        PUTA(36, t4); PUTA(37, cmul(t4, e128)); PUTA(38, cmul(t4, e256)); PUTA(39, cmul(t4, e384));
        PUTA(40, cmul(t0, e1024));
      }
#undef PUT
#undef PUTA
#undef PUTB
      STAMPW(4, 6);
      STAMPV(11);
      if (lane == 0) smx[wv - 4] = smax;
      lds_arrive_wait(cnt + cl, 2, lane);
      STAMPV(12);
      const float s0_ = smx[2 * cl], s1_ = smx[2 * cl + 1];
      const float sm = fmaxf(s0_, s1_);
      const float ksc = sm * (182.f / 1024.f);
      const float kinv = sm > 0.f ? 1.0f / ksc : 0.f;
      LAS unsigned short* kc = (LAS unsigned short*)(lds + CV_KCT + cl * 1600);
      {
        const int r = lane & 15, h = lane >> 4;
        const float f0 = s0_ * kinv, f1 = s1_ * kinv;
#pragma unroll
        for (int tI = 0; tI < 2; ++tI) {
          if (tI == 1 && half == 1) break;
          const int t = half ? 1 : 2 * tI;
          f32x4 d0 = {0, 0, 0, 0}, d1 = {0, 0, 0, 0};
#pragma unroll
          for (int ks = 0; ks < 4; ++ks) {
            const int ob = (41 + r) * 256 + (((4 * ks + h) ^ ((41 + r) & 15)) << 4), oa = (16 * t + r) * 256 + (((4 * ks + h) ^ r) << 4);
            d0 = mfma_h(*(LAS v4u*)(chan + oa), *(LAS v4u*)(chan + ob), d0);
            d1 = mfma_h(*(LAS v4u*)(chan + CV_TAB + oa), *(LAS v4u*)(chan + CV_TAB + ob), d1);
          }
#pragma unroll
          for (int e = 0; e < 4; ++e) {
            const float v = __builtin_fmaf(d0[e], f0, d1[e] * f1);
            kc[(16 * t + 4 * h + e) * 16 + r] = (unsigned short)(cvtpkh(v, v) & 0xFFFFu);
          }
        }
        if (half == 1 && lane == 0) { kc[768] = 0; kscs[cl] = ksc; }
      }
      STAMPV(13);
      lds_arrive_wait(cnt + cl, 4, lane);
      STAMPV(14);
#pragma unroll
      for (int i = 0; i < 3; ++i) {
        const int z = t2 + 128 * i;
        unsigned d[6];
#pragma unroll
        for (int j = 0; j < 4; ++j) d[j] = (unsigned)kc[kid[i][j] & 0xFFFFu] | ((unsigned)kc[kid[i][j] >> 16] << 16);
        d[4] = (unsigned)kc[kid2[i].x & 0xFFFFu] | ((unsigned)kc[kid2[i].x >> 16] << 16);
        d[5] = (unsigned)kc[kid2[i].y & 0xFFFFu];
        if (z < 259) {
          LAS unsigned char* base = chan + z * 16;
          *(LAS v4u*)(base) = v4u{d[0], d[1], d[2], d[3]};
          *(LAS v4u*)(base + CV_KC) = v4u{__builtin_amdgcn_alignbit(d[1], d[0], 16), __builtin_amdgcn_alignbit(d[2], d[1], 16), __builtin_amdgcn_alignbit(d[3], d[2], 16), __builtin_amdgcn_alignbit(d[4], d[3], 16)};
          *(LAS v4u*)(base + 2 * CV_KC) = v4u{d[1], d[2], d[3], d[4]};
          *(LAS v4u*)(base + 3 * CV_KC) = v4u{__builtin_amdgcn_alignbit(d[2], d[1], 16), __builtin_amdgcn_alignbit(d[3], d[2], 16), __builtin_amdgcn_alignbit(d[4], d[3], 16), __builtin_amdgcn_alignbit(d[5], d[4], 16)};
        }
      }
      STAMPW(4, 7);
      STAMPV(10);
    }
  } else {
    __builtin_amdgcn_s_barrier();
    tag = GET_TAG();
  }
  if (!is_conv) {
    const int idx = (jj - 18) * 8 + xcd;
#define PT_DELAY 4
    for (int i = 0; i < PT_DELAY; ++i) __builtin_amdgcn_s_sleep(32);
    pretile_unit(lds, rs, ws, idx, tag, w1, w2, w3, w4);
    if (idx >= 48 && idx < 98) pretile_unit(lds, rs, ws, 112 + idx - 48, tag, w1, w2, w3, w4);
    STAMP(2);
  }
  if (is_conv) {
    const float dsk = bf2f(dskip[c0 + (wv & 3)]);
    __syncthreads();
    STAMP(8);
    STAMPC(31);
    const int cl = wv & 3, kh = (wv >> 2) & 1;
    f32x4 acc[8];
    if (wv < 8) {
#pragma unroll
      for (int t = 0; t < 8; ++t) acc[t] = f32x4{0, 0, 0, 0};
      const int bb = lane & 15, h = lane >> 4;
      const int cpy = 3 - (bb & 3);
      LAS unsigned char* abase = lds + cl * CV_CH + cpy * CV_KC + (2047 - 16 - bb + 8 * h - cpy) * 2 - 64 * 4 * kh;
      LAS unsigned char* bbase = lds + CV_U + cl * CV_UCH + 512 + (16 * (bb - 1) + 8 * h) * 2 - 64 * 4 * kh;
      LAS unsigned char* abase2 = abase + 8;
      asm volatile("" : "+v"(abase2));
      v4u A[8], B[8];
#define LDA(i, rho) do { v2u lo_ = *(LAS v2u*)(abase - 64 * ((rho) + 8 * (i))), hi_ = *(LAS v2u*)(abase2 - 64 * ((rho) + 8 * (i))); A[i] = v4u{lo_.x, lo_.y, hi_.x, hi_.y}; } while (0)
#define LDB(m, rho) B[m] = *(LAS v4u*)(bbase + 32 * (16 * (m) - 2 * (rho)))
#pragma unroll
      for (int i = 0; i < 8; ++i) { LDB(i, 0); LDA(i, 0); }
#pragma unroll
      for (int rho = 0; rho < 4; ++rho) {
#pragma unroll
        for (int i = 0; i < 8; ++i) {
#pragma unroll
          for (int m = 7 - i; m >= 0; --m) acc[i + m] = mfma_h(A[i], B[m], acc[i + m]);
          if (rho < 3) { LDA(i, rho + 1); LDB(7 - i, rho + 1); }
          __builtin_amdgcn_sched_barrier(0);
        }
      }
#undef LDA
#undef LDB
      LAS unsigned char* xo = lds + cl * CV_CH + CV_XCH + kh * 4096 + lane * 16;
#pragma unroll
      for (int t = 0; t < 4; ++t) {
        f32x4 lo = acc[t], hi = acc[4 + t];
        asm volatile("" : "+v"(lo), "+v"(hi));
        *(LAS f32x4*)(xo + t * 1024) = kh ? lo : hi;
        acc[t] = kh ? hi : lo;
      }
    }
    STAMP(9);
    STAMPV(0);
    __syncthreads();
    STAMP(32);
    if (wv < 8) {
      const int ch = c0 + cl;
      const float ksc = kscs[cl];
      const int a = lane & 15, q = lane >> 4;
      LAS unsigned char* xi = lds + cl * CV_CH + CV_XCH + (kh ^ 1) * 4096 + lane * 16;
      unsigned pk[4][2];
#pragma unroll
      for (int t = 0; t < 4; ++t) {
        const f32x4 o = *(LAS f32x4*)(xi + t * 1024);
        const f32x4 y4 = acc[t] + o;
        const int tok = 256 * (4 * kh + t) + 16 * a + 4 * q;
        v2u uu = *(LAS v2u*)(lds + CV_U + cl * CV_UCH + 512 + tok * 2);
        v2u xx = *(LAS v2u*)(lds + CV_X + cl * 4096 + tok * 2);
#pragma unroll
        for (int e = 0; e < 2; ++e) {
          const unsigned uw = e ? uu.y : uu.x, xw = e ? xx.y : xx.x;
          const h16x2 u2 = __builtin_bit_cast(h16x2, uw);
          const unsigned y = cvtpk(y4[2 * e] * ksc, y4[2 * e + 1] * ksc);
          const unsigned ud = cvtpk((float)u2[0] * dsk, (float)u2[1] * dsk);
          const unsigned tt = cvtpk(bflo(y) + bflo(ud), bfhi(y) + bfhi(ud));
          pk[t][e] = cvtpk(bflo(xw) + bflo(tt), bfhi(xw) + bfhi(tt));
        }
      }
#pragma unroll
      for (int t = 0; t < 4; t += 2) {
        auto s0 = __builtin_amdgcn_permlane16_swap(pk[t][0], pk[t + 1][0], false, false);
        auto s1 = __builtin_amdgcn_permlane16_swap(pk[t][1], pk[t + 1][1], false, false);
        const int tok = 256 * (4 * kh + t + (q & 1)) + 16 * a + 8 * (q >> 1);
        st16(rs, v4u{s0[0], s1[0], s0[1], s1[1]}, WS_HT + (ch * L + tok) * 2);
      }
      STAMP(33);
      STAMPV(1);
      if (drain_arrive_last(cnt + 8, 8, lane)) { if (lane < 8) __hip_atomic_store((gu64*)(ws + WS_F2) + lane * 256 + (18 * xcd + jj), tag, RLX); STAMPL(43); }
    }
    STAMP(10);
  }

  const int tt = 4 * xcd + jj / 6, sl = jj % 6;
  if (is_gemm) {
    const int cl = wv % 6, kh = wv / 6, ct = 6 * sl + cl;
    const int tok = lane & 15, q = lane >> 4;
    if (wv == 0) {
      const int u0 = (96 * sl) >> 6;
      poll_tag((gu64*)(ws + WS_FPT + (b & 3) * 2048) + (lane & 1) + u0 + ((lane & 2) ? 9 : 0), tag);
    }
    __syncthreads();
    v4u bw[9];
    if (wv != 0) {
#pragma unroll
      for (int ks = 0; ks < 9; ++ks) bw[ks] = ld16(rs, lane * 16, WS_W1T + (ct * 18 + 9 * kh + ks) * 1024);
    }
    if (wv == 0) {
      const int fo = WS_F2 + (b & 7) * 2048 + (lane < 36 ? lane : 0) * 32;
      const unsigned tl = (unsigned)tag, th = (unsigned)(tag >> 32);
      for (unsigned spins = 0; spins < SPIN_CAP; ++spins) {
        asm volatile("" ::: "memory");
        const v4u f0 = ld16(rs, fo), f1 = ld16(rs, fo + 16);
        if (__all(f0.x == tl && f0.y == th && f0.z == tl && f0.w == th && f1.x == tl && f1.y == th && f1.z == tl && f1.w == th)) break;
        if (PSLEEP) __builtin_amdgcn_s_sleep(PSLEEP);
      }
      ORDER();
#pragma unroll
      for (int ks = 0; ks < 9; ++ks) bw[ks] = ld16(rs, lane * 16, WS_W1T + (ct * 18 + 9 * kh + ks) * 1024);
    }
    const f32x4 cs = __builtin_bit_cast(f32x4, ld16(rs, WS_CS1 + (ct * 16 + 4 * q) * 4));
    __syncthreads();
    STAMP(11);
    {
      v4u a[6];
#pragma unroll
      for (int i = 0; i < 6; ++i) {
        const int p = tid + NT * i, c = p >> 3, pc = p & 7;
        a[i] = ld16(rs, WS_HT + (c * L + tt * 64 + pc * 8) * 2);
      }
#pragma unroll
      for (int i = 0; i < 6; ++i) {
        const int p = tid + NT * i, c = p >> 3, pc = p & 7;
        const int f = ((c >> 1) & 1) | (((c >> 3) & 1) << 1);
        *(LAS v4u*)(lds + G_A + c * 128 + ((((pc >> 1) ^ f) << 1 | (pc & 1)) << 4)) = a[i];
      }
      if (tid == 0) *(LAS unsigned*)(lds + G_ST + 3584) = 0;
    }
    __syncthreads();
    v4u bw2[9];
    v4u hpiece;
    STAMP(12);
    STAMPV(2);
    f32x4 acc[4];
#pragma unroll
    for (int r = 0; r < 4; ++r) acc[r] = f32x4{0, 0, 0, 0};
    {
      const int pq = (lane & 15) >> 2, pp = lane & 3;
      const int f = ((pq >> 1) & 1) | ((q & 1) << 1);
      const unsigned ones = 0x3F803F80u;
      LAS unsigned char* pa0 = lds + G_A + (8 * q + pq) * 128 + pp * 8;
      LAS unsigned char* par[4];
#pragma unroll
      for (int r = 0; r < 4; ++r) par[r] = pa0 + ((r ^ f) << 5);
      {
        float ps = 0.f, pv = 0.f;
        LAS unsigned char* ps0 = pa0 + 6 * (wv >> 2) * 4096 + (((wv & 3) ^ f) << 5);
        v2u lo[6], hi[6];
#pragma unroll
        for (int i = 0; i < 6; ++i) { lo[i] = tr_read(ps0 + i * 4096); hi[i] = tr_read(ps0 + i * 4096 + 4 * 128); }
#pragma unroll
        for (int i = 0; i < 6; ++i) {
          ps = dot2bf(lo[i].x, ones, ps); ps = dot2bf(lo[i].y, ones, ps); ps = dot2bf(hi[i].x, ones, ps); ps = dot2bf(hi[i].y, ones, ps);
          pv = dot2bf(lo[i].x, lo[i].x, pv); pv = dot2bf(lo[i].y, lo[i].y, pv); pv = dot2bf(hi[i].x, hi[i].x, pv); pv = dot2bf(hi[i].y, hi[i].y, pv);
        }
        ps += __shfl_xor(ps, 16); ps += __shfl_xor(ps, 32);
        pv += __shfl_xor(pv, 16); pv += __shfl_xor(pv, 32);
        if (lane < 16) *(LAS f32x2*)(lds + G_ST + (((wv & 3) * 16 + lane) * 3 + (wv >> 2)) * 8) = f32x2{ps, pv};
        lds_arrive((LAS unsigned*)(lds + G_ST + 3584), lane);
      }
      v4u af[2][4];
#pragma unroll
      for (int r = 0; r < 4; ++r) { v2u lo = tr_read(par[r] + 9 * kh * 4096), hi = tr_read(par[r] + 9 * kh * 4096 + 512); af[0][r] = v4u{lo.x, lo.y, hi.x, hi.y}; }
#pragma unroll
      for (int ks = 0; ks < 9; ++ks) {
        if (ks < 8) {
#pragma unroll
          for (int r = 0; r < 4; ++r) {
            v2u lo = tr_read(par[r] + (9 * kh + ks + 1) * 4096), hi = tr_read(par[r] + (9 * kh + ks + 1) * 4096 + 512);
            af[(ks + 1) & 1][r] = v4u{lo.x, lo.y, hi.x, hi.y};
          }
        }
        __builtin_amdgcn_sched_barrier(0);
#pragma unroll
        for (int r = 0; r < 4; ++r) acc[r] = mfma_bf(bw[ks], af[ks & 1][r], acc[r]);
        bw2[ks] = ld16(rs, lane * 16, WS_W2T + (ct * 18 + 9 * kh + ks) * 1024);
        if (ks == 8) hpiece = ld16(rs, WS_HT + ((ct * 16 + (lane >> 2)) * L + tt * 64 + kh * 32 + (lane & 3) * 8) * 2);
        __builtin_amdgcn_sched_barrier(0);
      }
    }
    STAMP(35);
    STAMPV(3);
    {
      LAS unsigned char* xo = lds + G_X + wv * 2048 + lane * 16;
#pragma unroll
      for (int r = 0; r < 2; ++r) {
        f32x4 lo = acc[r], hi = acc[2 + r];
        asm volatile("" : "+v"(lo), "+v"(hi));
        *(LAS f32x4*)(xo + r * 1024) = kh ? lo : hi;
        acc[r] = kh ? hi : lo;
      }
    }
    if (wv == 11) {
      lds_wait((LAS unsigned*)(lds + G_ST + 3584), 12);
      float s_ = 0.f, ss = 0.f;
#pragma unroll
      for (int w = 0; w < 3; ++w) { f32x2 v = *(LAS f32x2*)(lds + G_ST + (lane * 3 + w) * 8); s_ += v.x; ss += v.y; }
      float m1, s1;
      ln_scalars(s_, ss, m1, s1);
      const float mean = s_ * (1.0f / D);
      float var = __builtin_fmaf(-mean, mean, ss * (1.0f / D));
      var = var < 0.f ? 0.f : var;
      const float r1 = __builtin_amdgcn_rcpf(s1);
      const float m2 = bfr((mean - m1) * r1);
      const float s2 = bfr(__builtin_amdgcn_sqrtf(bfr(bfr(var * r1 * r1) + 1e-5f)));
      *(LAS f32x4*)(lds + G_ST + 2048 + lane * 16) = f32x4{__builtin_fmaf(m2, s1, m1), r1 * __builtin_amdgcn_rcpf(s2), m1, r1};
    }
    __syncthreads();
    {
      LAS unsigned char* xi = lds + G_X + (kh ? wv - 6 : wv + 6) * 2048 + lane * 16;
#pragma unroll
      for (int r = 0; r < 2; ++r) acc[r] += *(LAS f32x4*)(xi + r * 1024);
    }
    STAMP(13);
    STAMPV(4);
    f32x2 st1[2];
#pragma unroll
    for (int r = 0; r < 2; ++r) {
      const int row = (2 * kh + r) * 16 + tok;
      const f32x4 sc = *(LAS f32x4*)(lds + G_ST + 2048 + row * 16);
      st1[r] = f32x2{sc[2], sc[3]};
      float v[4];
#pragma unroll
      for (int e = 0; e < 4; ++e) v[e] = (acc[r][e] - sc[0] * cs[e]) * sc[1];
      const unsigned p0 = cvtpk(v[0], v[1]), p1 = cvtpk(v[2], v[3]);
      const f32x2 ga = gelu2(f32x2{bflo(p0), bfhi(p0)}), gb = gelu2(f32x2{bflo(p1), bfhi(p1)});
      const unsigned g0 = cvtpk(ga[0], ga[1]), g1 = cvtpk(gb[0], gb[1]);
      *(LAS v2u*)(lds + G_O + row * G_OROW + cl * 32 + q * 8) = v2u{g0, g1};
    }
    __syncthreads();
    const int orow = tid / 12, ocol = tid % 12;
    st16(rs, *(LAS v4u*)(lds + G_O + orow * G_OROW + ocol * 16), WS_H1 + ((tt * 64 + orow) * D + sl * 96 + ocol * 8) * 2);
    STAMP(36);
    STAMPV(5);
    if (drain_arrive_last(cnt + 9, 12, lane)) { if (lane == 0) __hip_atomic_store((gu64*)(ws + WS_F3) + tt * 8 + sl, tag, RLX); STAMPL(44); }
    STAMP(14);

    {
      const int c = lane >> 2, pc = lane & 3;
      *(LAS v4u*)(lds + G_H + wv * 1024 + c * 64 + ((((pc >> 1) ^ ((c >> 3) & 1)) << 1 | (pc & 1)) << 4)) = hpiece;
    }
    if (wv == 0) {
      int t = tt - 1 + (lane < 18 ? lane / 6 : 1);
      t = t < 0 ? 0 : (t > 31 ? 31 : t);
      poll_tag((gu64*)(ws + WS_F3) + t * 8 + (lane < 18 ? lane % 6 : 0), tag);
    }
    __syncthreads();
    STAMP(15);
    {
      v4u a[6];
#pragma unroll
      for (int i = 0; i < 6; ++i) {
        const int p = tid + NT * i, r = p / 72, c = p % 72;
        const int cg = c / 18;
        const int sh = cg == 0 ? -1 : (cg == 1 ? 1 : (cg == 2 ? -2 : 2));
        const int t = tt * 64 + r + sh;
        a[i] = ld16(rs, (t >= 0 && t < L) ? WS_H1 + (t * D + c * 8) * 2 : WS_END);
      }
#pragma unroll
      for (int i = 0; i < 6; ++i) {
        const int p = tid + NT * i, r = p / 72, c = p % 72;
        *(LAS v4u*)(lds + G_A + r * ROWB + ((c ^ ((r >> 1) & 7)) << 4)) = a[i];
      }
    }
    __syncthreads();
    STAMP(16);
#pragma unroll
    for (int r = 0; r < 4; ++r) acc[r] = f32x4{0, 0, 0, 0};
    {
      const int sw = (tok >> 1) & 7;
      LAS unsigned char* pa = lds + G_A + tok * ROWB;
      v4u af[2][4];
#pragma unroll
      for (int r = 0; r < 4; ++r) af[0][r] = *(LAS v4u*)(pa + r * 16 * ROWB + (((4 * 9 * kh + q) ^ sw) << 4));
#pragma unroll
      for (int ks = 0; ks < 9; ++ks) {
        if (ks < 8) {
#pragma unroll
          for (int r = 0; r < 4; ++r) af[(ks + 1) & 1][r] = *(LAS v4u*)(pa + r * 16 * ROWB + (((4 * (9 * kh + ks + 1) + q) ^ sw) << 4));
        }
        __builtin_amdgcn_sched_barrier(0);
#pragma unroll
        for (int r = 0; r < 4; ++r) acc[r] = mfma_bf(bw2[ks], af[ks & 1][r], acc[r]);
        __builtin_amdgcn_sched_barrier(0);
      }
    }
    {
      LAS unsigned char* xo = lds + G_X + wv * 2048 + lane * 16;
#pragma unroll
      for (int r = 0; r < 2; ++r) {
        f32x4 lo = acc[r], hi = acc[2 + r];
        asm volatile("" : "+v"(lo), "+v"(hi));
        *(LAS f32x4*)(xo + r * 1024) = kh ? lo : hi;
        acc[r] = kh ? hi : lo;
      }
    }
    __syncthreads();
    STAMP(17);
    {
      LAS unsigned char* xi = lds + G_X + (kh ? wv - 6 : wv + 6) * 2048 + lane * 16;
#pragma unroll
      for (int r = 0; r < 2; ++r) {
        acc[r] += *(LAS f32x4*)(xi + r * 1024);
        const int c = 4 * q + ((lane & 15) >> 2);
        const v2u hh = tr_read(lds + G_H + wv * 1024 + c * 64 + ((r ^ ((c >> 3) & 1)) << 5) + (lane & 3) * 8);
        const float m1 = st1[r][0], r1 = st1[r][1];
        unsigned o[2];
#pragma unroll
        for (int e = 0; e < 2; ++e) {
          const unsigned hw = e ? hh.y : hh.x;
          const unsigned d = cvtpk(bflo(hw) - m1, bfhi(hw) - m1);
          const unsigned z = cvtpk(bflo(d) * r1, bfhi(d) * r1);
          const unsigned hs = cvtpk(acc[r][2 * e], acc[r][2 * e + 1]);
          o[e] = cvtpk(bflo(z) + bflo(hs), bfhi(z) + bfhi(hs));
        }
        const int row = (2 * kh + r) * 16 + tok;
        *(LAS v2u*)(lds + G_O + row * G_OROW + cl * 32 + q * 8) = v2u{o[0], o[1]};
        *(LAS v2u*)(lds + G_O + 64 * G_OROW + row * G_OROW + cl * 32 + q * 8) = hh;
      }
    }
    __syncthreads();
    {
      const int off = ((tt * 64 + orow) * D + sl * 96 + ocol * 8) * 2;
      st16(rs, *(LAS v4u*)(lds + G_O + orow * G_OROW + ocol * 16), WS_X1 + off);
      st16(rs, *(LAS v4u*)(lds + G_O + 64 * G_OROW + orow * G_OROW + ocol * 16), WS_HR + off);
    }
    if (drain_arrive_last(cnt + 10, 12, lane)) { if (lane == 0) __hip_atomic_store((gu64*)(ws + WS_F4) + tt * 8 + sl, tag, RLX); STAMPL(45); }
    STAMP(18);
  }

  if (!is_gemm) {
    const int sb = 2 * (xcd & 3), part = jj - 24;
    if (wv == 0) {
      gu64* p = (gu64*)(ws + WS_FPT + (b & 3) * 2048) + (lane < 18 ? 18 + 9 * sb + lane : (lane < 36 ? 90 + 9 * sb + lane - 18 : 18 + 9 * sb));
      for (unsigned spins = 0; spins < SPIN_CAP; ++spins) {
        if (__all(__hip_atomic_load(p, RLX) == tag)) break;
        __builtin_amdgcn_s_sleep(8);
      }
      ORDER();
    }
    __syncthreads();
    unsigned sink = 0;
    const int n3 = 72 * 18 / 8, n4 = 36 * 36 / 8;
#pragma unroll 7
    for (int f = wv; f < n3; f += 12) { v4u v = ldw(rs, lane * 16, WS_W3T + (36 * sb * 18 + part * n3 + f) * 1024); sink ^= v.x; }
#pragma unroll 7
    for (int f = wv; f < n4; f += 12) { const int g = part * n4 + f; v4u v = ldw(rs, lane * 16, WS_W4T + ((g / 36) * 144 + 18 * sb + g % 36) * 1024); sink ^= v.x; }
    asm volatile("" ::"v"(sink));
    STAMP(2);
  }
  const int grp = 2 * (jj >> 1) + (xcd >> 2), s = 2 * (xcd & 3) + (jj & 1);
  {
    constexpr int DEPTH = 3;
    if (wv == 0) {
      poll_tag((gu64*)(ws + WS_FPT + (b & 3) * 2048) + (lane < 9 ? 18 + 9 * s + lane : (lane < 18 ? 90 + 9 * s + lane - 9 : 18 + 9 * s)), tag);
    }
    __syncthreads();
    const int o3 = WS_W3T + (36 * s + 3 * wv) * 18 * 1024, o4 = WS_W4T + (3 * wv * 144 + 18 * s) * 1024;
    v4u bw[DEPTH][3];
    if (wv != 0) {
#pragma unroll
      for (int d = 0; d < DEPTH; ++d)
#pragma unroll
        for (int c = 0; c < 3; ++c) bw[d][c] = ldw(rs, lane * 16, o3 + (c * 18 + d) * 1024);
    }
    if (wv == 0) {
      poll_tag((gu64*)(ws + WS_F4) + grp * 8 + (lane < 6 ? lane : 0), tag);
#pragma unroll
      for (int d = 0; d < DEPTH; ++d)
#pragma unroll
        for (int c = 0; c < 3; ++c) bw[d][c] = ldw(rs, lane * 16, o3 + (c * 18 + d) * 1024);
    }
    __syncthreads();
    STAMP(19);
    const v4u csl = ld16(rs, WS_CS3 + 36 * 16 * 4 * s + (tid < 144 ? tid : 0) * 16);
    if (tid < 4) ((LAS unsigned*)(lds + ML_ST + 512))[tid] = 0;
    {
      v4u a[6];
#pragma unroll
      for (int i = 0; i < 6; ++i) a[i] = ld16(rs, WS_X1 + grp * 64 * ROWB + (tid + NT * i) * 16);
      const int hoc = 9 * (s >> 1) + (wv < 9 ? wv : 0);
      const v4u hv = ld16(rs, WS_HR + ((grp * 64 + (s & 1) * 32 + ((lane >> 4) & 1) * 16 + (lane & 15)) * D + hoc * 16 + 8 * (lane >> 5)) * 2);
#pragma unroll
      for (int i = 0; i < 6; ++i) {
        int p = tid + NT * i, r = p / 72, c = p % 72;
        *(LAS v4u*)(lds + ML_A + r * ROWB + ((c ^ ((r >> 1) & 7)) << 4)) = a[i];
      }
      if (wv < 9) *(LAS v4u*)(lds + ML_HR + wv * 1024 + lane * 16) = hv;
      if (tid < 144) *(LAS v4u*)(lds + ML_CS + tid * 16) = csl;
    }
    __syncthreads();
    STAMP(20);
    STAMPV(6);
    STAMPC(28);
    LAS unsigned* mcnt = (LAS unsigned*)(lds + ML_ST + 512);
    if (wv >= 8) {
      __builtin_amdgcn_s_setprio(3);
#pragma unroll
      for (int ps = 0; ps < 2; ++ps) {
        const int r = (wv - 8) * 16 + ps * 8 + (lane >> 3), sub = lane & 7;
        float sm = 0.f, ss = 0.f;
        const unsigned ones = 0x3F803F80u;
#pragma unroll
        for (int i = 0; i < 9; ++i) {
          v4u a = *(LAS v4u*)(lds + ML_A + r * ROWB + (sub + 8 * i) * 16);
#pragma unroll
          for (int j = 0; j < 4; ++j) { sm = dot2bf(a[j], ones, sm); ss = dot2bf(a[j], a[j], ss); }
        }
#pragma unroll
        for (int o = 1; o < 8; o <<= 1) { sm += __shfl_xor(sm, o); ss += __shfl_xor(ss, o); }
        float m, sd;
        ln_scalars(sm, ss, m, sd);
        if (sub == 0) *(LAS f32x2*)(lds + ML_ST + r * 8) = f32x2{m, __builtin_amdgcn_rcpf(sd)};
      }
      lds_arrive(mcnt, lane);
    }
    const int tok = lane & 15, q = lane >> 4;
    const int sw = (tok >> 1) & 7;
    LAS unsigned char* abase = lds + ML_A + tok * ROWB;
    f32x4 acc[4][3];
#pragma unroll
    for (int r = 0; r < 4; ++r)
#pragma unroll
      for (int c = 0; c < 3; ++c) acc[r][c] = f32x4{0, 0, 0, 0};
#define PRIO_P1 2
    __builtin_amdgcn_s_setprio(PRIO_P1);
    v4u af[2][4];
#pragma unroll
    for (int r = 0; r < 4; ++r) af[0][r] = *(LAS v4u*)(abase + r * 16 * ROWB + ((q ^ sw) << 4));
#pragma unroll
    for (int ks = 0; ks < 18; ++ks) {
      if (ks < 17) {
#pragma unroll
        for (int r = 0; r < 4; ++r) af[(ks + 1) & 1][r] = *(LAS v4u*)(abase + r * 16 * ROWB + (((4 * (ks + 1) + q) ^ sw) << 4));
      }
      __builtin_amdgcn_sched_barrier(0);
#pragma unroll
      for (int c = 0; c < 3; ++c) {
#pragma unroll
        for (int r = 0; r < 4; ++r) acc[r][c] = mfma_bf(bw[ks % DEPTH][c], af[ks & 1][r], acc[r][c]);
        const int nk = ks + DEPTH;
        if (nk < 18) bw[ks % DEPTH][c] = ldw(rs, lane * 16, o3 + (c * 18 + nk) * 1024);
        else bw[ks % DEPTH][c] = ldw(rs, lane * 16, o4 + (c * 144 + nk - 18) * 1024);
      }
      __builtin_amdgcn_sched_barrier(0);
    }
#define PRIO_GELU 3
#define PRIO_P2 0
    __builtin_amdgcn_s_setprio(PRIO_GELU);
    STAMP(21);
    STAMPV(7);
    lds_wait(mcnt, 4);
    STAMP(22);
    LAS unsigned char* H = lds + ML_H;
    {
      f32x2 st[4];
#pragma unroll
      for (int r = 0; r < 4; ++r) st[r] = *(LAS f32x2*)(lds + ML_ST + (r * 16 + tok) * 8);
#pragma unroll
      for (int c = 0; c < 3; ++c) {
        const f32x4 cs = *(LAS f32x4*)(lds + ML_CS + ((3 * wv + c) * 16 + 4 * q) * 4);
#pragma unroll
        for (int r = 0; r < 4; ++r) {
          const f32x2 nm = {-st[r].x, -st[r].x}, rr = {st[r].y, st[r].y};
          const f32x2 ga = gelu2(__builtin_elementwise_fma(nm, f32x2{cs[0], cs[1]}, f32x2{acc[r][c][0], acc[r][c][1]}) * rr);
          const f32x2 gb = gelu2(__builtin_elementwise_fma(nm, f32x2{cs[2], cs[3]}, f32x2{acc[r][c][2], acc[r][c][3]}) * rr);
          v2u o = {cvtpk(ga[0], ga[1]), cvtpk(gb[0], gb[1])};
          const int chunk = 2 * (3 * wv + c) + (q >> 1);
          *(LAS v2u*)(H + (r * 16 + tok) * ROWB + ((chunk ^ sw) << 4) + (q & 1) * 8) = o;
          acc[r][c] = f32x4{0, 0, 0, 0};
        }
      }
    }
    lds_arrive(mcnt + 1 + (wv >> 2), lane);
    __builtin_amdgcn_s_setprio(PRIO_P2);
    STAMP(23);
    STAMPV(8);
    LAS unsigned char* hbase = H + tok * ROWB;
#pragma unroll
    for (int k2 = 0; k2 < 18; ++k2) {
      const int ks = 18 + k2;
      if (k2 % 6 == 0) {
        lds_wait(mcnt + 1 + k2 / 6, 4);
#pragma unroll
        for (int r = 0; r < 4; ++r) af[k2 & 1][r] = *(LAS v4u*)(hbase + r * 16 * ROWB + (((4 * k2 + q) ^ sw) << 4));
      }
      if (k2 % 6 != 5) {
#pragma unroll
        for (int r = 0; r < 4; ++r) af[(k2 + 1) & 1][r] = *(LAS v4u*)(hbase + r * 16 * ROWB + (((4 * (k2 + 1) + q) ^ sw) << 4));
      }
      __builtin_amdgcn_sched_barrier(0);
#pragma unroll
      for (int c = 0; c < 3; ++c) {
#pragma unroll
        for (int r = 0; r < 4; ++r) acc[r][c] = mfma_bf(bw[ks % DEPTH][c], af[k2 & 1][r], acc[r][c]);
        const int nk = k2 + DEPTH;
        if (nk < 18) bw[ks % DEPTH][c] = ldw(rs, lane * 16, o4 + (c * 144 + nk) * 1024);
      }
      __builtin_amdgcn_sched_barrier(0);
    }
    STAMP(24);
    STAMPV(9);
    {
      const int po = WS_PART + (s * 32 + grp) * 72 * 1024;
#pragma unroll
      for (int c = 0; c < 3; ++c)
#pragma unroll
        for (int rp = 0; rp < 2; ++rp) {
          unsigned x0 = cvtpk(acc[2 * rp][c][0], acc[2 * rp][c][1]), x1 = cvtpk(acc[2 * rp][c][2], acc[2 * rp][c][3]);
          unsigned y0 = cvtpk(acc[2 * rp + 1][c][0], acc[2 * rp + 1][c][1]), y1 = cvtpk(acc[2 * rp + 1][c][2], acc[2 * rp + 1][c][3]);
          auto s0 = __builtin_amdgcn_permlane16_swap(x0, y0, false, false);
          auto s1 = __builtin_amdgcn_permlane16_swap(x1, y1, false, false);
          const v4u o = {s0[0], s1[0], s0[1], s1[1]};
          const int oc = 3 * wv + c;
          if (rp == (s & 1) && oc / 9 == (s >> 1)) *(LAS v4u*)(lds + ML_A + (rp ^ 1) * 32 * ROWB + (oc % 9) * 1024 + lane * 16) = o;
          else st16(rs, o, lane * 16, po + (rp * 36 + oc) * 1024);
        }
    }
    if (drain_arrive_last(cnt + 11, 12, lane)) { if (lane == 0) __hip_atomic_store((gu64*)(ws + WS_F5) + grp * 8 + s, tag, RLX); STAMPL(46); }
    STAMP(25);
    STAMPC(29);
  }

  {
    if (wv == 0) poll_tag((gu64*)(ws + WS_F5) + grp * 8 + (lane & 7), tag);
    __syncthreads();
    STAMP(26);
    if (wv < 9) {
      const int rp = s & 1, oc = 9 * (s >> 1) + wv, u = rp * 36 + oc;
      const int q = lane >> 4, tok = lane & 15;
      const int row = rp * 32 + (q & 1) * 16 + tok;
      v4u p[8];
#pragma unroll
      for (int k = 0; k < 7; ++k) p[k] = ld16(rs, lane * 16, WS_PART + ((((s + 1 + k) & 7) * 32 + grp) * 72 + u) * 1024);
      p[7] = *(LAS v4u*)(lds + ML_A + (rp ^ 1) * 32 * ROWB + wv * 1024 + lane * 16);
      const v4u xv = *(LAS v4u*)(lds + ML_A + row * ROWB + (((2 * oc + (q >> 1)) ^ ((row >> 1) & 7)) << 4));
      const v4u hv = *(LAS v4u*)(lds + ML_HR + wv * 1024 + lane * 16);
      v4u o;
#pragma unroll
      for (int j = 0; j < 4; ++j) {
        float lo = 0.f, hi = 0.f;
#pragma unroll
        for (int k = 0; k < 8; ++k) { lo += bflo(p[k][j]); hi += bfhi(p[k][j]); }
        const unsigned h2 = cvtpk(lo, hi);
        const unsigned y = cvtpk(bflo(xv[j]) + bflo(h2), bfhi(xv[j]) + bfhi(h2));
        o[j] = cvtpk(bflo(hv[j]) + bflo(y), bfhi(hv[j]) + bfhi(y));
      }
      *(GAS v4u*)((GAS unsigned char*)out + ((grp * 64 + row) * D + oc * 16 + 8 * (q >> 1)) * 2) = o;
    }
    if (b == 0 && tid == 0) __hip_atomic_store((gu64*)(ws + WS_CTRL), tag, RLX);
    STAMP(27);
  }
}

extern "C" void kernel_launch(void* const* d_in, const int* in_sizes, int n_in, void* d_out, int out_size, void* d_ws, size_t ws_size,
                              hipStream_t stream) {
  (void)hipFuncSetAttribute((const void*)k_all, hipFuncAttributeMaxDynamicSharedMemorySize, LDS_BYTES);
  const unsigned short* const* in = (const unsigned short* const*)d_in;
  k_all<<<256, NT, LDS_BYTES, stream>>>(in[0], in[1], in[2], in[3], in[4], in[5], in[6], in[7], in[8], in[10], in[11], in[12], in[13], (unsigned char*)d_ws,
                                        (unsigned short*)d_out);
}
```

```cpp
#include <hip/hip_runtime.h>
#include <hip/hip_bf16.h>
#include <stdint.h>

typedef unsigned long long u64;
typedef unsigned v4u __attribute__((ext_vector_type(4)));
typedef unsigned v2u __attribute__((ext_vector_type(2)));
typedef float f32x4 __attribute__((ext_vector_type(4)));
typedef float f32x2 __attribute__((ext_vector_type(2)));
typedef short s16x8 __attribute__((ext_vector_type(8)));
typedef _Float16 h16x8 __attribute__((ext_vector_type(8)));
typedef _Float16 h16x2 __attribute__((ext_vector_type(2)));
typedef __bf16 bf16x2_t __attribute__((ext_vector_type(2)));
#define GAS __attribute__((address_space(1)))
#define LAS __attribute__((address_space(3)))
#define DI __device__ __forceinline__
#define RLX __ATOMIC_RELAXED, __HIP_MEMORY_SCOPE_AGENT
typedef GAS u64 gu64;
typedef __amdgpu_buffer_rsrc_t rsrc_t;

#define NLAUNCH 1

constexpr int L = 2048, D = 576, NS = 128, F = 4608;
constexpr int NT = 768;

constexpr int WS_CTRL = 0;
constexpr int WS_F1 = 4096;
constexpr int WS_FPT = WS_F1 + 8 * 2048;
constexpr int WS_F2 = WS_FPT + 4 * 2048;
constexpr int WS_F3 = WS_F2 + 8 * 2048;
constexpr int WS_F4 = WS_F3 + 32 * 64;
constexpr int WS_F5 = WS_F4 + 32 * 64;
constexpr int WS_W1T = 131072;
constexpr int WS_W2T = WS_W1T + 36 * 18 * 1024;
constexpr int WS_W3T = WS_W2T + 36 * 18 * 1024;
constexpr int WS_W4T = WS_W3T + 288 * 18 * 1024;
constexpr int WS_CS1 = WS_W4T + 36 * 144 * 1024;
constexpr int WS_CS3 = WS_CS1 + 4096;
constexpr int WS_HT = WS_CS3 + 4608 * 4;
constexpr int WS_H1 = WS_HT + D * L * 2;
constexpr int WS_ST2 = WS_H1 + D * L * 2;
constexpr int WS_X1 = WS_ST2 + 2048 * 8;
constexpr int WS_HR = WS_X1 + D * L * 2;
constexpr int WS_PART = WS_HR + D * L * 2;
constexpr int WS_UX = WS_PART + 8 * D * L * 2;
constexpr int WS_END = WS_UX + 256 * 144 * 128;
static_assert(WS_F5 + 32 * 192 <= WS_W1T, "control block");

#define STAMP(i)
#define STAMPW(w, i)
#define STAMPC(i)
#define STAMPV(i)
#define STAMPL(i)

DI float bf2f(unsigned short b) { return __builtin_bit_cast(float, (unsigned)b << 16); }
DI float asf(unsigned u) { return __builtin_bit_cast(float, u); }
DI unsigned asu(float f) { return __builtin_bit_cast(unsigned, f); }
DI float bflo(unsigned u) { return __builtin_bit_cast(float, u << 16); }
DI float bfhi(unsigned u) { return __builtin_bit_cast(float, u & 0xFFFF0000u); }
DI unsigned cvtpk(float lo, float hi) {
  f32x2 v = {lo, hi};
  bf16x2_t b = __builtin_convertvector(v, bf16x2_t);
  return __builtin_bit_cast(unsigned, b);
}
DI float bfr(float x) { return bflo(cvtpk(x, x)); }
DI unsigned cvtpkh(float lo, float hi) {
  f32x2 v = {lo, hi};
  h16x2 b = __builtin_convertvector(v, h16x2);
  return __builtin_bit_cast(unsigned, b);
}
DI f32x4 mfma_bf(v4u a, v4u b, f32x4 c) {
  return __builtin_amdgcn_mfma_f32_16x16x32_bf16(__builtin_bit_cast(s16x8, a), __builtin_bit_cast(s16x8, b), c, 0, 0, 0);
}
DI f32x4 mfma_h(v4u a, v4u b, f32x4 c) {
  return __builtin_amdgcn_mfma_f32_16x16x32_f16(__builtin_bit_cast(h16x8, a), __builtin_bit_cast(h16x8, b), c, 0, 0, 0);
}
DI float dot2bf(unsigned a, unsigned b, float acc) {
  return __builtin_amdgcn_fdot2_f32_bf16(__builtin_bit_cast(bf16x2_t, a), __builtin_bit_cast(bf16x2_t, b), acc, false);
}
DI v2u tr_read(LAS unsigned char* p) {
  typedef short v4i16_t __attribute__((ext_vector_type(4)));
  return __builtin_bit_cast(v2u, __builtin_amdgcn_ds_read_tr16_b64_v4i16((LAS v4i16_t*)p));
}
template <int CTRL> DI float dppf(float v) { return asf((unsigned)__builtin_amdgcn_update_dpp(0, (int)asu(v), CTRL, 0xF, 0xF, true)); }
DI float sum8(float v) { v += dppf<0xB1>(v); v += dppf<0x4E>(v); v += dppf<0x141>(v); return v; }
DI float sum_rows(float v) {
  auto a = __builtin_amdgcn_permlane16_swap(asu(v), asu(v), false, false);
  v = asf(a[0]) + asf(a[1]);
  auto c = __builtin_amdgcn_permlane32_swap(asu(v), asu(v), false, false);
  return asf(c[0]) + asf(c[1]);
}
DI float wave_max(float m) {
  m = fmaxf(m, dppf<0xB1>(m)); m = fmaxf(m, dppf<0x4E>(m)); m = fmaxf(m, dppf<0x141>(m)); m = fmaxf(m, dppf<0x140>(m));
  m = fmaxf(m, asf((unsigned)__builtin_amdgcn_update_dpp((int)asu(m), (int)asu(m), 0x142, 0xA, 0xF, false)));
  m = fmaxf(m, asf((unsigned)__builtin_amdgcn_update_dpp((int)asu(m), (int)asu(m), 0x143, 0xC, 0xF, false)));
  return asf((unsigned)__builtin_amdgcn_readlane((int)asu(m), 63));
}
DI float gelu_f(float x) {
  const float c0 = -2.3022081986f, c1 = -0.10294324f;
  float x2 = x * x;
  float p = __builtin_fmaf(x2, c1, c0);
  float e = __builtin_amdgcn_exp2f(x * p);
  return x * __builtin_amdgcn_rcpf(1.0f + e);
}
DI f32x2 gelu2(f32x2 x) {
  const f32x2 c0 = {-2.3022081986f, -2.3022081986f}, c1 = {-0.10294324f, -0.10294324f}, one = {1.0f, 1.0f};
  const f32x2 e = x * __builtin_elementwise_fma(x * x, c1, c0);
  const f32x2 d = f32x2{__builtin_amdgcn_exp2f(e[0]), __builtin_amdgcn_exp2f(e[1])} + one;
  return x * f32x2{__builtin_amdgcn_rcpf(d[0]), __builtin_amdgcn_rcpf(d[1])};
}
DI void ln_scalars(float sum, float sumsq, float& m, float& s) {
  float mean = sum * (1.0f / D);
  float var = __builtin_fmaf(-mean, mean, sumsq * (1.0f / D));
  var = var < 0.f ? 0.f : var;
  m = bfr(mean);
  s = bfr(__builtin_amdgcn_sqrtf(bfr(bfr(var) + 1e-5f)));
}
constexpr int SC1 = 16;
DI v4u ld16(rsrc_t rs, int voff, int soff = 0) { return __builtin_amdgcn_raw_buffer_load_b128(rs, voff, soff, SC1); }
#define WAUX 16
DI v4u ldw(rsrc_t rs, int voff, int soff = 0) { return __builtin_amdgcn_raw_buffer_load_b128(rs, voff, soff, WAUX); }
DI void dma16(rsrc_t rs, LAS unsigned char* dst, int voff) { __builtin_amdgcn_raw_ptr_buffer_load_lds(rs, dst, 16, voff, 0, 0, SC1); }
DI v2u ld8(rsrc_t rs, int voff, int soff = 0) { return __builtin_amdgcn_raw_buffer_load_b64(rs, voff, soff, SC1); }
DI void st16(rsrc_t rs, v4u v, int voff, int soff = 0) { __builtin_amdgcn_raw_buffer_store_b128(v, rs, voff, soff, SC1); }
DI void st8(rsrc_t rs, v2u v, int voff, int soff = 0) { __builtin_amdgcn_raw_buffer_store_b64(v, rs, voff, soff, SC1); }
DI void st4(rsrc_t rs, unsigned v, int voff, int soff = 0) { __builtin_amdgcn_raw_buffer_store_b32(v, rs, voff, soff, SC1); }
#define DRAIN() asm volatile("s_waitcnt vmcnt(0)" ::: "memory")
#define ORDER() __builtin_amdgcn_fence(__ATOMIC_ACQUIRE, "wavefront")
constexpr unsigned SPIN_CAP = 1u << 15;

#define PSLEEP 8
DI void poll_tag(gu64* p, u64 tag) {
  for (unsigned spins = 0; spins < SPIN_CAP; ++spins) {
    if (__all(__hip_atomic_load(p, RLX) == tag)) break;
    if (PSLEEP) __builtin_amdgcn_s_sleep(PSLEEP);
  }
  ORDER();
}

struct KIdx { unsigned short v[2080]; };
constexpr KIdx make_kidx() {
  KIdx t{};
  for (int y = 0; y < 2080; ++y) {
    if (y >= 2048) { t.v[y] = 768; continue; }
    int j = 2047 - y;
    int v = j;
    if (j >= 256) {
      int sh = j >= 1024 ? 3 : (j >= 512 ? 2 : 1);
      int step = 1 << sh, lo = (j >> sh) << sh, rem = j - lo, half = step >> 1;
      if (rem > half || (rem == half && (((lo >> sh) & 1) != 0))) lo += step;
      v = lo;
    }
    int seg = v >= 1024 ? 3 : (v >= 512 ? 2 : (v >= 256 ? 1 : 0));
    int u = seg == 0 ? v : v - (128 << seg);
    int rb = seg == 0 ? 0 : (seg == 1 ? 16 : (seg == 2 ? 24 : 32));
    int row = rb + ((u & 7) >> seg) + (u >> 7) * (8 >> seg);
    int col = (u >> 3) & 15;
    t.v[y] = (unsigned short)(row * 16 + col);
  }
  return t;
}
__device__ const KIdx kIdxTab = make_kidx();

struct cplx { float r, i; };
DI cplx cmul(cplx a, cplx b) {
  const f32x2 t = f32x2{-a.i, a.i} * f32x2{b.i, b.r};
  const f32x2 r = __builtin_elementwise_fma(f32x2{a.r, a.r}, f32x2{b.r, b.i}, t);
  return cplx{r[0], r[1]};
}
DI cplx cexp_r(float a2, float th, float r) {
  float m = __builtin_amdgcn_exp2f(a2 * r);
  float ang = __builtin_amdgcn_fractf(th * r);
  return cplx{m * __builtin_amdgcn_cosf(ang), m * __builtin_amdgcn_sinf(ang)};
}

constexpr int CV_TAB = 57 * 256;
constexpr int CV_CH = 2 * CV_TAB;
constexpr int CV_KC = 4160;
constexpr int CV_XCH = 4 * CV_KC;
constexpr int CV_KCT = 152 * 1024;
constexpr int CV_U = 4 * CV_CH;
constexpr int CV_UCH = (256 + 2048) * 2;
constexpr int CV_X = CV_U + 4 * CV_UCH;
constexpr int LDS_BYTES = 160 * 1024;
constexpr int CV_MISC = LDS_BYTES - 512;
static_assert(4 * CV_KC + 8192 <= CV_CH, "conv channel region");
constexpr int PR_ROW = 1168;
constexpr int PR_U = CV_U, PR_X = CV_U + 8 * PR_ROW;
constexpr int ROWB = 1152;
constexpr int G_A = 0;
constexpr int G_ST = 73728;
constexpr int G_X = G_ST + 4096;
constexpr int G_H = G_X + 12 * 2048;
constexpr int G_O = G_H + 12 * 1024;
constexpr int G_OROW = 208;
constexpr int ML_A = 0, ML_H = 64 * ROWB, ML_ST = 2 * 64 * ROWB;
constexpr int ML_CS = ML_ST + 576;
static_assert(CV_X + 4 * 4096 <= CV_KCT && CV_KCT + 4 * 1600 <= CV_MISC && ML_CS + 2304 <= CV_MISC && G_O + 2 * 64 * G_OROW <= CV_MISC && PR_X + 8 * PR_ROW <= CV_MISC, "LDS");

DI void lds_arrive_wait(LAS unsigned* ctr, unsigned target, int lane) {
  asm volatile("s_waitcnt lgkmcnt(0)" ::: "memory");
  if (lane == 0) __hip_atomic_fetch_add(ctr, 1u, __ATOMIC_RELAXED, __HIP_MEMORY_SCOPE_WORKGROUP);
  while (__hip_atomic_load(ctr, __ATOMIC_RELAXED, __HIP_MEMORY_SCOPE_WORKGROUP) < target) __builtin_amdgcn_s_sleep(1);
  asm volatile("" ::: "memory");
}

DI void lds_arrive(LAS unsigned* ctr, int lane) {
  asm volatile("s_waitcnt lgkmcnt(0)" ::: "memory");
  if (lane == 0) __hip_atomic_fetch_add(ctr, 1u, __ATOMIC_RELAXED, __HIP_MEMORY_SCOPE_WORKGROUP);
  asm volatile("" ::: "memory");
}
DI bool drain_arrive_last(LAS unsigned* ctr, unsigned n, int lane) {
  DRAIN();
  unsigned old = 0;
  if (lane == 0) old = __hip_atomic_fetch_add(ctr, 1u, __ATOMIC_RELAXED, __HIP_MEMORY_SCOPE_WORKGROUP);
  return (unsigned)__builtin_amdgcn_readfirstlane(old) == n - 1;
}
DI void lds_wait(LAS unsigned* ctr, unsigned target) {
  while (__hip_atomic_load(ctr, __ATOMIC_RELAXED, __HIP_MEMORY_SCOPE_WORKGROUP) < target) __builtin_amdgcn_s_sleep(1);
  asm volatile("" ::: "memory");
}
DI void pretile_unit(LAS unsigned char* lds, rsrc_t rs, unsigned char* ws, int unit, u64 tag, const unsigned short* __restrict__ w1, const unsigned short* __restrict__ w2,
                     const unsigned short* __restrict__ w3, const unsigned short* __restrict__ w4) {
  const int tid = threadIdx.x, lane = tid & 63, wv = __builtin_amdgcn_readfirstlane(tid >> 6);
  const unsigned short* w;
  int ldw, dst, ksn, cs = -1;
  if (unit < 9) { w = w1 + unit * 64; ldw = D; dst = WS_W1T + unit * 4 * 18 * 1024; ksn = 18; cs = WS_CS1 + unit * 256; }
  else if (unit < 18) { w = w2 + (unit - 9) * 64; ldw = D; dst = WS_W2T + (unit - 9) * 4 * 18 * 1024; ksn = 18; }
  else if (unit < 90) { w = w3 + (unit - 18) * 64; ldw = F; dst = WS_W3T + (unit - 18) * 4 * 18 * 1024; ksn = 18; cs = WS_CS3 + (unit - 18) * 256; }
  else { int u = unit - 90, kc = u / 9, cg = u % 9; w = w4 + (size_t)kc * 576 * D + cg * 64; ldw = D; dst = WS_W4T + (cg * 4 * 144 + kc * 18) * 1024; ksn = 144; }
  v4u a[6];
#pragma unroll
  for (int i = 0; i < 6; ++i) {
    int p = tid + NT * i, r = p >> 3, c = p & 7;
    a[i] = *(const GAS v4u*)(w + (size_t)r * ldw + c * 8);
  }
  __syncthreads();
#pragma unroll
  for (int i = 0; i < 6; ++i) {
    int p = tid + NT * i, r = p >> 3, c = p & 7;
    int f = ((r >> 1) & 1) | (((r >> 3) & 1) << 1);
    *(LAS v4u*)(lds + r * 128 + ((((c >> 1) ^ f) << 1 | (c & 1)) << 4)) = a[i];
  }
  __syncthreads();
  const int ct = wv & 3, k0 = 6 * (wv >> 2);
  const int g = lane >> 4, q = (lane & 15) >> 2, p = lane & 3;
  const int f = ((q >> 1) & 1) | ((g & 1) << 1);
  float csum = 0.f;
  const unsigned ones = 0x3F803F80u;
#pragma unroll
  for (int ks = 0; ks < 6; ++ks) {
    LAS unsigned char* pa = lds + (32 * (k0 + ks) + 8 * g + q) * 128 + ((ct ^ f) << 5) + p * 8;
    v2u lo = tr_read(pa), hi = tr_read(pa + 4 * 128);
    v4u o = {lo.x, lo.y, hi.x, hi.y};
    st16(rs, o, lane * 16, dst + (ct * ksn + k0 + ks) * 1024);
    csum = dot2bf(o.x, ones, csum); csum = dot2bf(o.y, ones, csum); csum = dot2bf(o.z, ones, csum); csum = dot2bf(o.w, ones, csum);
  }
  if (cs >= 0) {
    csum = sum_rows(csum);
    LAS float* pc = (LAS float*)(lds + 73728);
    if (lane < 16) pc[wv * 16 + lane] = csum;
    __syncthreads();
    if (tid < 64) st4(rs, __builtin_bit_cast(unsigned, pc[tid] + pc[64 + tid] + pc[128 + tid]), cs + tid * 4);
  }
  DRAIN();
  __syncthreads();
  if (tid < 4) __hip_atomic_store((gu64*)(ws + WS_FPT) + tid * 256 + unit, tag, RLX);
}

template <int NC>
DI void mlp_second(LAS unsigned char* lds, rsrc_t rs, unsigned char* ws, unsigned short* __restrict__ out, u64 tag, int grp, int s, int oc0, int wi, int g,
                   LAS unsigned* mcnt, int lane, int tok, int q, int sw) {
  constexpr int DEP = NC == 4 ? 2 : 3;
  const int o4 = WS_W4T + (oc0 * 144 + 18 * s) * 1024;
  v4u bw[DEP][NC];
#pragma unroll
  for (int d = 0; d < DEP; ++d)
#pragma unroll
    for (int c = 0; c < NC; ++c) bw[d][c] = ldw(rs, lane * 16, o4 + (c * 144 + d) * 1024);
  f32x4 acc[4][NC];
#pragma unroll
  for (int r = 0; r < 4; ++r)
#pragma unroll
    for (int c = 0; c < NC; ++c) acc[r][c] = f32x4{0, 0, 0, 0};
  LAS unsigned char* hbase = lds + ML_H + tok * ROWB;
  v4u af[2][4];
#pragma unroll
  for (int k2 = 0; k2 < 18; ++k2) {
    if (k2 % 6 == 0) {
      lds_wait(mcnt + 1 + k2 / 6, 4);
#pragma unroll
      for (int r = 0; r < 4; ++r) af[k2 & 1][r] = *(LAS v4u*)(hbase + r * 16 * ROWB + (((4 * k2 + q) ^ sw) << 4));
    }
    if (k2 % 6 != 5) {
#pragma unroll
      for (int r = 0; r < 4; ++r) af[(k2 + 1) & 1][r] = *(LAS v4u*)(hbase + r * 16 * ROWB + (((4 * (k2 + 1) + q) ^ sw) << 4));
    }
    __builtin_amdgcn_sched_barrier(0);
#pragma unroll
    for (int c = 0; c < NC; ++c) {
#pragma unroll
      for (int r = 0; r < 4; ++r) acc[r][c] = mfma_bf(bw[k2 % DEP][c], af[k2 & 1][r], acc[r][c]);
      const int nk = k2 + DEP;
      if (nk < 18) bw[k2 % DEP][c] = ldw(rs, lane * 16, o4 + (c * 144 + nk) * 1024);
    }
    __builtin_amdgcn_sched_barrier(0);
  }
  STAMP(24);
  STAMPV(9);
  const int rpo = s >> 2, slot0 = g == 0 ? 0 : (g == 1 ? 4 : 7);
  LAS unsigned char* own = lds + ML_A + (rpo ^ 1) * 32 * ROWB + slot0 * 1024;
  {
    const int po = WS_PART + (s * 32 + grp) * 72 * 1024;
#pragma unroll
    for (int c = 0; c < NC; ++c)
#pragma unroll
      for (int rp = 0; rp < 2; ++rp) {
        unsigned x0 = cvtpk(acc[2 * rp][c][0], acc[2 * rp][c][1]), x1 = cvtpk(acc[2 * rp][c][2], acc[2 * rp][c][3]);
        unsigned y0 = cvtpk(acc[2 * rp + 1][c][0], acc[2 * rp + 1][c][1]), y1 = cvtpk(acc[2 * rp + 1][c][2], acc[2 * rp + 1][c][3]);
        auto s0 = __builtin_amdgcn_permlane16_swap(x0, y0, false, false);
        auto s1 = __builtin_amdgcn_permlane16_swap(x1, y1, false, false);
        const v4u o = {s0[0], s1[0], s0[1], s1[1]};
        if (rp == rpo && wi == (s & 3)) *(LAS v4u*)(own + c * 1024 + lane * 16) = o;
        else st16(rs, o, lane * 16, po + (rp * 36 + oc0 + c) * 1024);
      }
  }
  asm volatile("s_waitcnt lgkmcnt(0)" ::: "memory");
  if (drain_arrive_last(mcnt + 4 + g, 4, lane)) { if (lane == 0) __hip_atomic_store((gu64*)(ws + WS_F5) + (grp * 3 + g) * 8 + s, tag, RLX); if (g == 2) STAMPL(46); }
  STAMP(25);
  STAMPC(29);
  if (wi < NC) {
    if (wi == 0) { poll_tag((gu64*)(ws + WS_F5) + (grp * 3 + g) * 8 + (lane & 7), tag); lds_arrive(mcnt + 7 + g, lane); }
    else lds_wait(mcnt + 7 + g, 1);
    lds_wait(mcnt + 4 + g, 4);
    if (g == 2) STAMP(26);
    const int oc = (g == 0 ? 0 : (g == 1 ? 16 : 28)) + (s & 3) * NC + wi, u = rpo * 36 + oc;
    const int row = rpo * 32 + (q & 1) * 16 + tok;
    const int off = ((grp * 64 + row) * D + oc * 16 + 8 * (q >> 1)) * 2;
    v4u p[8];
#pragma unroll
    for (int k = 0; k < 7; ++k) p[k] = ld16(rs, lane * 16, WS_PART + ((((s + 1 + k) & 7) * 32 + grp) * 72 + u) * 1024);
    const v4u hv = ld16(rs, WS_HR + off);
    p[7] = *(LAS v4u*)(own + wi * 1024 + lane * 16);
    const v4u xv = *(LAS v4u*)(lds + ML_A + row * ROWB + (((2 * oc + (q >> 1)) ^ ((row >> 1) & 7)) << 4));
    v4u o;
#pragma unroll
    for (int j = 0; j < 4; ++j) {
      f32x2 sum = f32x2{bflo(xv[j]), bfhi(xv[j])} + f32x2{bflo(hv[j]), bfhi(hv[j])};
#pragma unroll
      for (int k = 0; k < 8; ++k) sum += f32x2{bflo(p[k][j]), bfhi(p[k][j])};
      o[j] = cvtpk(sum[0], sum[1]);
    }
    *(GAS v4u*)((GAS unsigned char*)out + off) = o;
  }
}

__global__ __launch_bounds__(NT, 3) void k_all(const unsigned short* __restrict__ x, const unsigned short* __restrict__ Are, const unsigned short* __restrict__ Aim,
                                               const unsigned short* __restrict__ Bre, const unsigned short* __restrict__ Bim, const unsigned short* __restrict__ Cre,
                                               const unsigned short* __restrict__ Cim, const unsigned short* __restrict__ logdt, const unsigned short* __restrict__ dskip,
                                               const unsigned short* __restrict__ w1, const unsigned short* __restrict__ w2, const unsigned short* __restrict__ w3,
                                               const unsigned short* __restrict__ w4, unsigned char* __restrict__ ws, unsigned short* __restrict__ out) {
  extern __shared__ __attribute__((aligned(16))) unsigned char smem[];
  LAS unsigned char* lds = (LAS unsigned char*)smem;
  const int tid = threadIdx.x, lane = tid & 63, wv = __builtin_amdgcn_readfirstlane(tid >> 6);
  const int b = blockIdx.x, xcd = b & 7, jj = b >> 3;
  const rsrc_t rs = __builtin_amdgcn_make_buffer_rsrc((void*)ws, 0, WS_END, 0x00020000);
  STAMP(0);
  STAMPC(30);
  const bool is_conv = jj < 18, is_gemm = jj < 24;
  const int c0 = 4 * (18 * xcd + jj);
  if (tid < 16) ((LAS unsigned*)(lds + CV_MISC))[tid] = 0;
  asm volatile("s_waitcnt lgkmcnt(0)" ::: "memory");
  asm volatile("" ::"s"(x), "s"(Are), "s"(Aim), "s"(Bre), "s"(Bim), "s"(Cre), "s"(Cim), "s"(logdt), "s"(ws));
  const u64 epoch = __hip_atomic_load((gu64*)(ws + WS_CTRL), RLX);
#define GET_TAG() ((((u64)(unsigned)__builtin_amdgcn_readfirstlane((unsigned)(epoch >> 32)) << 32) | (unsigned)__builtin_amdgcn_readfirstlane((unsigned)epoch)) + 1)
  u64 tag;
  LAS unsigned* cnt = (LAS unsigned*)(lds + CV_MISC);
  LAS float* smx = (LAS float*)(lds + CV_MISC + 64);
  LAS float* kscs = (LAS float*)(lds + CV_MISC + 96);

  if (wv < 4) {
    if (wv < 3) {
      const int t8 = lane >> 3, sub = lane & 7;
      const GAS v4u* row = (const GAS v4u*)(x + (size_t)(b * 8 + t8) * D) + 24 * wv + sub;
      v4u a[3];
#pragma unroll
      for (int i = 0; i < 3; ++i) a[i] = row[8 * i];
      __builtin_amdgcn_sched_barrier(0);
      __builtin_amdgcn_s_barrier();
      tag = GET_TAG();
      float s = 0.f, ss = 0.f;
      const unsigned ones = 0x3F803F80u;
#pragma unroll
      for (int i = 0; i < 3; ++i) {
#pragma unroll
        for (int j = 0; j < 4; ++j) { s = dot2bf(a[i][j], ones, s); ss = dot2bf(a[i][j], a[i][j], ss); }
      }
#pragma unroll
      for (int o = 0; o < 1; ++o) { s = sum8(s); ss = sum8(ss); }
      STAMP(38);
      LAS f32x2* part = (LAS f32x2*)(lds + CV_MISC + 128);
      if (sub == 0) part[wv * 8 + t8] = f32x2{s, ss};
#pragma unroll
      for (int i = 0; i < 3; ++i) *(LAS v4u*)(lds + PR_X + t8 * PR_ROW + (24 * wv + 8 * i + sub) * 16) = a[i];
      lds_arrive_wait(cnt + 4, 3, lane);
      STAMP(39);
      {
        const f32x2 p0 = part[t8], p1 = part[8 + t8], p2 = part[16 + t8];
        float m, sd;
        ln_scalars(p0.x + p1.x + p2.x, p0.y + p1.y + p2.y, m, sd);
        const float r = __builtin_amdgcn_rcpf(sd), nmr = -m * r;
#pragma unroll
        for (int i = 0; i < 3; ++i) {
          v4u up;
#pragma unroll
          for (int j = 0; j < 4; ++j) {
            const f32x2 xv = {bflo(a[i][j]), bfhi(a[i][j])};
            const f32x2 uv = __builtin_elementwise_fma(xv, f32x2{r, r}, f32x2{nmr, nmr});
            up[j] = cvtpkh(uv[0], uv[1]);
          }
          *(LAS v4u*)(lds + PR_U + t8 * PR_ROW + (24 * wv + 8 * i + sub) * 16) = up;
        }
      }
      {
        const int g = lane >> 4, q = (lane & 15) >> 2, p = lane & 3;
        LAS unsigned char* pa = lds + ((p & 1) ? PR_X : PR_U) + q * PR_ROW + (192 * wv + 8 * g + 4 * (p >> 1)) * 2;
#pragma unroll
        for (int k = 0; k < 6; ++k) {
          v2u lo = tr_read(pa + k * 64), hi = tr_read(pa + k * 64 + 4 * PR_ROW);
          st16(rs, v4u{lo.x, lo.y, hi.x, hi.y}, lane * 16, WS_UX + b * 18432 + (6 * wv + k) * 1024);
        }
      }
      STAMP(40);
      DRAIN();
      STAMP(41);
      lds_arrive_wait(cnt + 4, 6, lane);
      if (tid < 8) __hip_atomic_store((gu64*)(ws + WS_F1) + tid * 256 + b, tag, RLX);
      STAMP(1);
    } else {
      __builtin_amdgcn_s_barrier();
      tag = GET_TAG();
    }
    if (is_conv) {
      poll_tag((gu64*)(ws + WS_F1 + (b & 7) * 2048) + 64 * wv + lane, tag);
      STAMP(3);
      v4u a[8];
#pragma unroll
      for (int i = 0; i < 8; ++i) a[i] = ld16(rs, (lane >> 3) * 18432 + (lane & 7) * 16, WS_UX + (64 * wv + 8 * i) * 18432 + (c0 >> 2) * 128);
      if (wv == 3) lds_arrive_wait(cnt + 4, 6, 64);
      const int pc = lane & 7;
      LAS unsigned char* dst = lds + (pc < 4 ? CV_U + pc * CV_UCH + 512 : CV_X + (pc - 4) * 4096) + (64 * wv + (lane >> 3)) * 16;
#pragma unroll
      for (int i = 0; i < 8; ++i) *(LAS v4u*)(dst + i * 128) = a[i];
      if (tid < 128) *(LAS v4u*)(lds + CV_U + (tid >> 5) * CV_UCH + (tid & 31) * 16) = v4u{0, 0, 0, 0};
      STAMP(4);
      STAMPV(10);
    }
  } else if (is_conv) {
    const int cl = (wv - 4) >> 1, half = wv & 1, ch = c0 + cl, n = half * 64 + lane;
    const int gi = ch * NS + n;
    const int t2 = half * 64 + lane;
    const unsigned short rld = logdt[ch], rar = Are[gi], rai = Aim[gi], rbr = Bre[gi], rbi = Bim[gi], rcr = Cre[gi], rci = Cim[gi];
    v4u kid[3];
    v2u kid2[3];
#pragma unroll
    for (int i = 0; i < 3; ++i) {
      const int p = t2 + 128 * i < 259 ? t2 + 128 * i : 258;
      kid[i] = *(const GAS v4u*)(kIdxTab.v + p * 8);
      kid2[i] = *(const GAS v2u*)(kIdxTab.v + p * 8 + 8);
    }
    __builtin_amdgcn_sched_barrier(0);
    __builtin_amdgcn_s_barrier();
    tag = GET_TAG();
    {
      const float dt = bfr(expf(bf2f(rld)));
      const float zr = dt * bf2f(rar), zi = dt * bf2f(rai);
      const float dBr = dt * bf2f(rbr), dBi = dt * bf2f(rbi);
      cplx W = cmul(cplx{bf2f(rcr), bf2f(rci)}, cplx{dBr, dBi});
      float smax = fmaxf(fabsf(W.r), fabsf(W.i));
      smax = wave_max(smax);
      const float inv = smax > 0.f ? 1.0f / smax : 0.f;
      W.r *= inv; W.i *= inv;
      const float a2 = zr * 1.44269504089f, th = zi * 0.15915494309f;
      const cplx e1 = cexp_r(a2, th, 1.f), e8 = cexp_r(a2, th, 8.f), e128 = cexp_r(a2, th, 128.f);
      const cplx e256 = cexp_r(a2, th, 256.f), e512 = cexp_r(a2, th, 512.f), e1024 = cexp_r(a2, th, 1024.f);
      STAMPW(4, 5);
      STAMPV(15);
      LAS unsigned char* chan = lds + cl * CV_CH;
      LAS unsigned char* tab = chan + half * CV_TAB;
      const int nsw = lane >> 2, nlo = (lane & 3) * 4;
#define PUT(row, val) *(LAS unsigned*)(tab + (row) * 256 + ((nsw ^ ((row) & 15)) << 4) + nlo) = (val)
#define PUTA(row, z) PUT(row, cvtpkh((z).r, -(z).i))
#define PUTB(row, z) PUT(41 + (row), cvtpkh((z).r, (z).i))
      {
        cplx c[16];
        c[0] = cplx{1.f, 0.f}; c[1] = e8; c[2] = cmul(e8, e8); c[3] = cmul(c[2], e8); c[4] = cmul(c[2], c[2]);
        c[5] = cmul(c[4], c[1]); c[6] = cmul(c[4], c[2]); c[7] = cmul(c[4], c[3]); c[8] = cmul(c[4], c[4]);
#pragma unroll
        for (int j = 9; j < 16; ++j) c[j] = cmul(c[8], c[j - 8]);
#pragma unroll
        for (int j = 0; j < 16; ++j) PUTB(j, c[j]);
      }
      {
        const cplx e2 = cmul(e1, e1), e4 = cmul(e2, e2);
        cplx p[8];
        p[0] = W; p[1] = cmul(W, e1); p[2] = cmul(W, e2); p[3] = cmul(p[1], e2);
#pragma unroll
        for (int c = 4; c < 8; ++c) p[c] = cmul(p[c - 4], e4);
#pragma unroll
        for (int c = 0; c < 8; ++c) {
          const cplx qv = cmul(p[c], e128);
          PUTA(c, p[c]);
          PUTA(8 + c, qv);
          if ((c & 1) == 0) {
            PUTA(16 + (c >> 1), cmul(p[c], e256));
            PUTA(20 + (c >> 1), cmul(qv, e256));
          }
        }
        const cplx e384 = cmul(e256, e128);
        const cplx s0 = cmul(p[0], e512), s1 = cmul(p[4], e512);
        PUTA(24, s0); PUTA(25, s1);
        PUTA(26, cmul(s0, e128)); PUTA(27, cmul(s1, e128));
        PUTA(28, cmul(s0, e256)); PUTA(29, cmul(s1, e256));
        PUTA(30, cmul(s0, e384)); PUTA(31, cmul(s1, e384));
        const cplx t0 = cmul(W, e1024), t4 = cmul(t0, e512);
        PUTA(32, t0); PUTA(33, cmul(t0, e128)); PUTA(34, cmul(t0, e256)); PUTA(35, cmul(t0, e384));
        PUTA(36, t4); PUTA(37, cmul(t4, e128)); PUTA(38, cmul(t4, e256)); PUTA(39, cmul(t4, e384));
        PUTA(40, cmul(t0, e1024));
      }
#undef PUT
#undef PUTA
#undef PUTB
      STAMPW(4, 6);
      STAMPV(11);
      if (lane == 0) smx[wv - 4] = smax;
      lds_arrive_wait(cnt + cl, 2, lane);
      STAMPV(12);
      const float s0_ = smx[2 * cl], s1_ = smx[2 * cl + 1];
      const float sm = fmaxf(s0_, s1_);
      const float ksc = sm * (182.f / 1024.f);
      const float kinv = sm > 0.f ? 1.0f / ksc : 0.f;
      LAS unsigned short* kc = (LAS unsigned short*)(lds + CV_KCT + cl * 1600);
      {
        const int r = lane & 15, h = lane >> 4;
        const float f0 = s0_ * kinv, f1 = s1_ * kinv;
#pragma unroll
        for (int tI = 0; tI < 2; ++tI) {
          if (tI == 1 && half == 1) break;
          const int t = half ? 1 : 2 * tI;
          f32x4 d0 = {0, 0, 0, 0}, d1 = {0, 0, 0, 0};
#pragma unroll
          for (int ks = 0; ks < 4; ++ks) {
            const int ob = (41 + r) * 256 + (((4 * ks + h) ^ ((41 + r) & 15)) << 4), oa = (16 * t + r) * 256 + (((4 * ks + h) ^ r) << 4);
            d0 = mfma_h(*(LAS v4u*)(chan + oa), *(LAS v4u*)(chan + ob), d0);
            d1 = mfma_h(*(LAS v4u*)(chan + CV_TAB + oa), *(LAS v4u*)(chan + CV_TAB + ob), d1);
          }
#pragma unroll
          for (int e = 0; e < 4; ++e) {
            const float v = __builtin_fmaf(d0[e], f0, d1[e] * f1);
            kc[(16 * t + 4 * h + e) * 16 + r] = (unsigned short)(cvtpkh(v, v) & 0xFFFFu);
          }
        }
        if (half == 1 && lane == 0) { kc[768] = 0; kscs[cl] = ksc; }
      }
      STAMPV(13);
      lds_arrive_wait(cnt + cl, 4, lane);
      STAMPV(14);
#pragma unroll
      for (int i = 0; i < 3; ++i) {
        const int z = t2 + 128 * i;
        unsigned d[6];
#pragma unroll
        for (int j = 0; j < 4; ++j) d[j] = (unsigned)kc[kid[i][j] & 0xFFFFu] | ((unsigned)kc[kid[i][j] >> 16] << 16);
        d[4] = (unsigned)kc[kid2[i].x & 0xFFFFu] | ((unsigned)kc[kid2[i].x >> 16] << 16);
        d[5] = (unsigned)kc[kid2[i].y & 0xFFFFu];
        if (z < 259) {
          LAS unsigned char* base = chan + z * 16;
          *(LAS v4u*)(base) = v4u{d[0], d[1], d[2], d[3]};
          *(LAS v4u*)(base + CV_KC) = v4u{__builtin_amdgcn_alignbit(d[1], d[0], 16), __builtin_amdgcn_alignbit(d[2], d[1], 16), __builtin_amdgcn_alignbit(d[3], d[2], 16), __builtin_amdgcn_alignbit(d[4], d[3], 16)};
          *(LAS v4u*)(base + 2 * CV_KC) = v4u{d[1], d[2], d[3], d[4]};
          *(LAS v4u*)(base + 3 * CV_KC) = v4u{__builtin_amdgcn_alignbit(d[2], d[1], 16), __builtin_amdgcn_alignbit(d[3], d[2], 16), __builtin_amdgcn_alignbit(d[4], d[3], 16), __builtin_amdgcn_alignbit(d[5], d[4], 16)};
        }
      }
      STAMPW(4, 7);
      STAMPV(10);
    }
  } else {
    __builtin_amdgcn_s_barrier();
    tag = GET_TAG();
  }
  if (!is_conv) {
    const int idx = (jj - 18) * 8 + xcd;
#define PT_DELAY 4
    for (int i = 0; i < PT_DELAY; ++i) __builtin_amdgcn_s_sleep(32);
    pretile_unit(lds, rs, ws, idx, tag, w1, w2, w3, w4);
    if (idx >= 48 && idx < 98) pretile_unit(lds, rs, ws, 112 + idx - 48, tag, w1, w2, w3, w4);
    STAMP(2);
  }
  if (is_conv) {
    const float dsk = bf2f(dskip[c0 + (wv & 3)]);
    __syncthreads();
    STAMP(8);
    STAMPC(31);
    const int cl = wv & 3, kh = (wv >> 2) & 1;
    f32x4 acc[8], base[4];
    float ksc = 0.f;
    if (wv < 8) {
#pragma unroll
      for (int t = 0; t < 8; ++t) acc[t] = f32x4{0, 0, 0, 0};
      const int bb = lane & 15, h = lane >> 4;
      const int cpy = 3 - (bb & 3);
      LAS unsigned char* abase = lds + cl * CV_CH + cpy * CV_KC + (2047 - 16 - bb + 8 * h - cpy) * 2 - 64 * 4 * kh;
      LAS unsigned char* bbase = lds + CV_U + cl * CV_UCH + 512 + (16 * (bb - 1) + 8 * h) * 2 - 64 * 4 * kh;
      LAS unsigned char* abase2 = abase + 8;
      asm volatile("" : "+v"(abase2));
      v4u A[8], B[8];
#define LDA(i, rho) do { v2u lo_ = *(LAS v2u*)(abase - 64 * ((rho) + 8 * (i))), hi_ = *(LAS v2u*)(abase2 - 64 * ((rho) + 8 * (i))); A[i] = v4u{lo_.x, lo_.y, hi_.x, hi_.y}; } while (0)
#define LDB(m, rho) B[m] = *(LAS v4u*)(bbase + 32 * (16 * (m) - 2 * (rho)))
#pragma unroll
      for (int i = 0; i < 8; ++i) { LDB(i, 0); LDA(i, 0); }
#pragma unroll
      for (int rho = 0; rho < 4; ++rho) {
#pragma unroll
        for (int i = 0; i < 8; ++i) {
#pragma unroll
          for (int m = 7 - i; m >= 0; --m) acc[i + m] = mfma_h(A[i], B[m], acc[i + m]);
          if (rho < 3) { LDA(i, rho + 1); LDB(7 - i, rho + 1); }
          __builtin_amdgcn_sched_barrier(0);
        }
      }
#undef LDA
#undef LDB
      LAS unsigned char* xo = lds + cl * CV_CH + CV_XCH + kh * 4096 + lane * 16;
#pragma unroll
      for (int t = 0; t < 4; ++t) {
        f32x4 lo = acc[t], hi = acc[4 + t];
        asm volatile("" : "+v"(lo), "+v"(hi));
        *(LAS f32x4*)(xo + t * 1024) = kh ? lo : hi;
        acc[t] = kh ? hi : lo;
      }
      const int a = lane & 15, q = lane >> 4;
#pragma unroll
      for (int t = 0; t < 4; ++t) {
        const int tok = 256 * (4 * kh + t) + 16 * a + 4 * q;
        const v2u uu = *(LAS v2u*)(lds + CV_U + cl * CV_UCH + 512 + tok * 2);
        const v2u xx = *(LAS v2u*)(lds + CV_X + cl * 4096 + tok * 2);
#pragma unroll
        for (int e = 0; e < 2; ++e) {
          const unsigned uw = e ? uu.y : uu.x, xw = e ? xx.y : xx.x;
          const h16x2 u2 = __builtin_bit_cast(h16x2, uw);
          const f32x2 bv = __builtin_elementwise_fma(f32x2{(float)u2[0], (float)u2[1]}, f32x2{dsk, dsk}, f32x2{bflo(xw), bfhi(xw)});
          base[t][2 * e] = bv[0]; base[t][2 * e + 1] = bv[1];
        }
      }
      ksc = kscs[cl];
#pragma unroll
      for (int t = 0; t < 4; ++t) base[t] += acc[t] * ksc;
    }
    STAMP(9);
    STAMPV(0);
    __syncthreads();
    STAMP(32);
    if (wv < 8) {
      const int ch = c0 + cl;
      const int a = lane & 15, q = lane >> 4;
      LAS unsigned char* xi = lds + cl * CV_CH + CV_XCH + (kh ^ 1) * 4096 + lane * 16;
      f32x4 o[4];
#pragma unroll
      for (int t = 0; t < 4; ++t) o[t] = *(LAS f32x4*)(xi + t * 1024);
      unsigned pk[4][2];
#pragma unroll
      for (int t = 0; t < 4; ++t) {
        const f32x4 hv = base[t] + o[t] * ksc;
        pk[t][0] = cvtpk(hv[0], hv[1]);
        pk[t][1] = cvtpk(hv[2], hv[3]);
      }
#pragma unroll
      for (int t = 0; t < 4; t += 2) {
        auto s0 = __builtin_amdgcn_permlane16_swap(pk[t][0], pk[t + 1][0], false, false);
        auto s1 = __builtin_amdgcn_permlane16_swap(pk[t][1], pk[t + 1][1], false, false);
        const int tok = 256 * (4 * kh + t + (q & 1)) + 16 * a + 8 * (q >> 1);
        st16(rs, v4u{s0[0], s1[0], s0[1], s1[1]}, WS_HT + (ch * L + tok) * 2);
      }
      STAMP(33);
      STAMPV(1);
      if (drain_arrive_last(cnt + 8, 8, lane)) { if (lane < 8) __hip_atomic_store((gu64*)(ws + WS_F2) + lane * 256 + (18 * xcd + jj), tag, RLX); STAMPL(43); }
    }
    STAMP(10);
  }

  const int tt = 4 * xcd + jj / 6, sl = jj % 6;
  if (is_gemm) {
    const int cl = wv % 6, kh = wv / 6, ct = 6 * sl + cl;
    const int tok = lane & 15, q = lane >> 4;
    if (wv == 0) {
      const int u0 = (96 * sl) >> 6;
      poll_tag((gu64*)(ws + WS_FPT + (b & 3) * 2048) + (lane & 1) + u0 + ((lane & 2) ? 9 : 0), tag);
    }
    __syncthreads();
    v4u bw[9];
    if (wv != 0) {
#pragma unroll
      for (int ks = 0; ks < 9; ++ks) bw[ks] = ld16(rs, lane * 16, WS_W1T + (ct * 18 + 9 * kh + ks) * 1024);
    }
    if (wv == 0) {
      const int fo = WS_F2 + (b & 7) * 2048 + (lane < 36 ? lane : 0) * 32;
      const unsigned tl = (unsigned)tag, th = (unsigned)(tag >> 32);
      for (unsigned spins = 0; spins < SPIN_CAP; ++spins) {
        asm volatile("" ::: "memory");
        const v4u f0 = ld16(rs, fo), f1 = ld16(rs, fo + 16);
        if (__all(f0.x == tl && f0.y == th && f0.z == tl && f0.w == th && f1.x == tl && f1.y == th && f1.z == tl && f1.w == th)) break;
        if (PSLEEP) __builtin_amdgcn_s_sleep(PSLEEP);
      }
      ORDER();
#pragma unroll
      for (int ks = 0; ks < 9; ++ks) bw[ks] = ld16(rs, lane * 16, WS_W1T + (ct * 18 + 9 * kh + ks) * 1024);
    }
    const f32x4 cs = __builtin_bit_cast(f32x4, ld16(rs, WS_CS1 + (ct * 16 + 4 * q) * 4));
    __syncthreads();
    STAMP(11);
    {
#pragma unroll
      for (int i = 0; i < 6; ++i) {
        const int p = tid + NT * i, c = p >> 3, pp = p & 7;
        const int f = ((c >> 1) & 1) | (((c >> 3) & 1) << 1);
        dma16(rs, lds + G_A + (wv * 64 + NT * i) * 16, WS_HT + (c * L + tt * 64 + ((((pp >> 1) ^ f) << 1) | (pp & 1)) * 8) * 2);
      }
      if (tid == 0) *(LAS unsigned*)(lds + G_ST + 3584) = 0;
    }
    __syncthreads();
    v4u bw2[9];
    v4u hpiece;
    STAMP(12);
    STAMPV(2);
    f32x4 acc[4];
#pragma unroll
    for (int r = 0; r < 4; ++r) acc[r] = f32x4{0, 0, 0, 0};
    {
      const int pq = (lane & 15) >> 2, pp = lane & 3;
      const int f = ((pq >> 1) & 1) | ((q & 1) << 1);
      const unsigned ones = 0x3F803F80u;
      LAS unsigned char* pa0 = lds + G_A + (8 * q + pq) * 128 + pp * 8;
      LAS unsigned char* par[4];
#pragma unroll
      for (int r = 0; r < 4; ++r) par[r] = pa0 + ((r ^ f) << 5);
      {
        float ps = 0.f, pv = 0.f;
        LAS unsigned char* ps0 = pa0 + 6 * (wv >> 2) * 4096 + (((wv & 3) ^ f) << 5);
        v2u lo[6], hi[6];
#pragma unroll
        for (int i = 0; i < 6; ++i) { lo[i] = tr_read(ps0 + i * 4096); hi[i] = tr_read(ps0 + i * 4096 + 4 * 128); }
#pragma unroll
        for (int i = 0; i < 6; ++i) {
          ps = dot2bf(lo[i].x, ones, ps); ps = dot2bf(lo[i].y, ones, ps); ps = dot2bf(hi[i].x, ones, ps); ps = dot2bf(hi[i].y, ones, ps);
          pv = dot2bf(lo[i].x, lo[i].x, pv); pv = dot2bf(lo[i].y, lo[i].y, pv); pv = dot2bf(hi[i].x, hi[i].x, pv); pv = dot2bf(hi[i].y, hi[i].y, pv);
        }
        ps = sum_rows(ps);
        pv = sum_rows(pv);
        if (lane < 16) *(LAS f32x2*)(lds + G_ST + (((wv & 3) * 16 + lane) * 3 + (wv >> 2)) * 8) = f32x2{ps, pv};
        lds_arrive((LAS unsigned*)(lds + G_ST + 3584), lane);
      }
      v4u af[2][4];
#pragma unroll
      for (int r = 0; r < 4; ++r) { v2u lo = tr_read(par[r] + 9 * kh * 4096), hi = tr_read(par[r] + 9 * kh * 4096 + 512); af[0][r] = v4u{lo.x, lo.y, hi.x, hi.y}; }
#pragma unroll
      for (int ks = 0; ks < 9; ++ks) {
        if (ks < 8) {
#pragma unroll
          for (int r = 0; r < 4; ++r) {
            v2u lo = tr_read(par[r] + (9 * kh + ks + 1) * 4096), hi = tr_read(par[r] + (9 * kh + ks + 1) * 4096 + 512);
            af[(ks + 1) & 1][r] = v4u{lo.x, lo.y, hi.x, hi.y};
          }
        }
        __builtin_amdgcn_sched_barrier(0);
#pragma unroll
        for (int r = 0; r < 4; ++r) acc[r] = mfma_bf(bw[ks], af[ks & 1][r], acc[r]);
        bw2[ks] = ld16(rs, lane * 16, WS_W2T + (ct * 18 + 9 * kh + ks) * 1024);
        if (ks == 8) hpiece = ld16(rs, WS_HT + ((ct * 16 + (lane >> 2)) * L + tt * 64 + kh * 32 + (lane & 3) * 8) * 2);
        __builtin_amdgcn_sched_barrier(0);
      }
    }
    STAMP(35);
    STAMPV(3);
    {
      LAS unsigned char* xo = lds + G_X + wv * 2048 + lane * 16;
#pragma unroll
      for (int r = 0; r < 2; ++r) {
        f32x4 lo = acc[r], hi = acc[2 + r];
        asm volatile("" : "+v"(lo), "+v"(hi));
        *(LAS f32x4*)(xo + r * 1024) = kh ? lo : hi;
        acc[r] = kh ? hi : lo;
      }
    }
    if (wv == 0) {
      lds_wait((LAS unsigned*)(lds + G_ST + 3584), 12);
      float s_ = 0.f, ss = 0.f;
#pragma unroll
      for (int w = 0; w < 3; ++w) { f32x2 v = *(LAS f32x2*)(lds + G_ST + (lane * 3 + w) * 8); s_ += v.x; ss += v.y; }
      float m1, s1;
      ln_scalars(s_, ss, m1, s1);
      const float mean = s_ * (1.0f / D);
      float var = __builtin_fmaf(-mean, mean, ss * (1.0f / D));
      var = var < 0.f ? 0.f : var;
      const float r1 = __builtin_amdgcn_rcpf(s1);
      const float m2 = bfr((mean - m1) * r1);
      const float s2 = bfr(__builtin_amdgcn_sqrtf(bfr(bfr(var * r1 * r1) + 1e-5f)));
      *(LAS f32x4*)(lds + G_ST + 2048 + lane * 16) = f32x4{__builtin_fmaf(m2, s1, m1), r1 * __builtin_amdgcn_rcpf(s2), m1, r1};
    }
    __syncthreads();
    STAMP(13);
    STAMPV(4);
    f32x2 st1[2];
    {
      LAS unsigned char* xi = lds + G_X + (kh ? wv - 6 : wv + 6) * 2048 + lane * 16;
      f32x4 o[2], sc[2];
#pragma unroll
      for (int r = 0; r < 2; ++r) { o[r] = *(LAS f32x4*)(xi + r * 1024); sc[r] = *(LAS f32x4*)(lds + G_ST + 2048 + ((2 * kh + r) * 16 + tok) * 16); }
      __builtin_amdgcn_sched_barrier(0);
#pragma unroll
      for (int r = 0; r < 2; ++r) {
        st1[r] = f32x2{sc[r][2], sc[r][3]};
        const f32x4 v = (acc[r] + o[r] - cs * sc[r][0]) * sc[r][1];
        const f32x2 ga = gelu2(f32x2{v[0], v[1]}), gb = gelu2(f32x2{v[2], v[3]});
        *(LAS v2u*)(lds + G_O + ((2 * kh + r) * 16 + tok) * G_OROW + cl * 32 + q * 8) = v2u{cvtpk(ga[0], ga[1]), cvtpk(gb[0], gb[1])};
      }
    }
    __syncthreads();
    const int orow = tid / 12, ocol = tid % 12;
    st16(rs, *(LAS v4u*)(lds + G_O + orow * G_OROW + ocol * 16), WS_H1 + ((tt * 64 + orow) * D + sl * 96 + ocol * 8) * 2);
    STAMP(36);
    STAMPV(5);
    if (drain_arrive_last(cnt + 9, 12, lane)) { if (lane == 0) __hip_atomic_store((gu64*)(ws + WS_F3) + tt * 8 + sl, tag, RLX); STAMPL(44); }
    STAMP(14);

    {
      const int c = lane >> 2, pc = lane & 3;
      *(LAS v4u*)(lds + G_H + wv * 1024 + c * 64 + ((((pc >> 1) ^ ((c >> 3) & 1)) << 1 | (pc & 1)) << 4)) = hpiece;
    }
    if (wv == 0) {
      int t = tt - 1 + (lane < 18 ? lane / 6 : 1);
      t = t < 0 ? 0 : (t > 31 ? 31 : t);
      poll_tag((gu64*)(ws + WS_F3) + t * 8 + (lane < 18 ? lane % 6 : 0), tag);
    }
    __syncthreads();
    STAMP(15);
    {
#pragma unroll
      for (int i = 0; i < 6; ++i) {
        const int p = tid + NT * i, r = p / 72, c = (p % 72) ^ ((r >> 1) & 7);
        const int cg = c / 18;
        const int sh = cg == 0 ? -1 : (cg == 1 ? 1 : (cg == 2 ? -2 : 2));
        const int t = tt * 64 + r + sh;
        dma16(rs, lds + G_A + (wv * 64 + NT * i) * 16, (t >= 0 && t < L) ? WS_H1 + (t * D + c * 8) * 2 : WS_END);
      }
    }
    __syncthreads();
    STAMP(16);
#pragma unroll
    for (int r = 0; r < 4; ++r) acc[r] = f32x4{0, 0, 0, 0};
    {
      const int sw = (tok >> 1) & 7;
      LAS unsigned char* pa = lds + G_A + tok * ROWB;
      v4u af[2][4];
#pragma unroll
      for (int r = 0; r < 4; ++r) af[0][r] = *(LAS v4u*)(pa + r * 16 * ROWB + (((4 * 9 * kh + q) ^ sw) << 4));
#pragma unroll
      for (int ks = 0; ks < 9; ++ks) {
        if (ks < 8) {
#pragma unroll
          for (int r = 0; r < 4; ++r) af[(ks + 1) & 1][r] = *(LAS v4u*)(pa + r * 16 * ROWB + (((4 * (9 * kh + ks + 1) + q) ^ sw) << 4));
        }
        __builtin_amdgcn_sched_barrier(0);
#pragma unroll
        for (int r = 0; r < 4; ++r) acc[r] = mfma_bf(bw2[ks], af[ks & 1][r], acc[r]);
        __builtin_amdgcn_sched_barrier(0);
      }
    }
    v2u hh[2];
    {
      LAS unsigned char* xo = lds + G_X + wv * 2048 + lane * 16;
#pragma unroll
      for (int r = 0; r < 2; ++r) {
        f32x4 lo = acc[r], hi = acc[2 + r];
        asm volatile("" : "+v"(lo), "+v"(hi));
        *(LAS f32x4*)(xo + r * 1024) = kh ? lo : hi;
        acc[r] = kh ? hi : lo;
      }
#pragma unroll
      for (int r = 0; r < 2; ++r) {
        const int c = 4 * q + ((lane & 15) >> 2);
        hh[r] = tr_read(lds + G_H + wv * 1024 + c * 64 + ((r ^ ((c >> 3) & 1)) << 5) + (lane & 3) * 8);
        const float m1 = st1[r][0], r1 = st1[r][1];
        const f32x4 hv = {bflo(hh[r].x), bfhi(hh[r].x), bflo(hh[r].y), bfhi(hh[r].y)};
        acc[r] += (hv - m1) * r1;
        *(LAS v2u*)(lds + G_O + 64 * G_OROW + ((2 * kh + r) * 16 + tok) * G_OROW + cl * 32 + q * 8) = hh[r];
      }
    }
    __syncthreads();
    STAMP(17);
    {
      LAS unsigned char* xi = lds + G_X + (kh ? wv - 6 : wv + 6) * 2048 + lane * 16;
      f32x4 o[2];
#pragma unroll
      for (int r = 0; r < 2; ++r) o[r] = *(LAS f32x4*)(xi + r * 1024);
#pragma unroll
      for (int r = 0; r < 2; ++r) {
        const f32x4 v = acc[r] + o[r];
        *(LAS v2u*)(lds + G_O + ((2 * kh + r) * 16 + tok) * G_OROW + cl * 32 + q * 8) = v2u{cvtpk(v[0], v[1]), cvtpk(v[2], v[3])};
      }
    }
    __syncthreads();
    {
      const int off = ((tt * 64 + orow) * D + sl * 96 + ocol * 8) * 2;
      st16(rs, *(LAS v4u*)(lds + G_O + orow * G_OROW + ocol * 16), WS_X1 + off);
      st16(rs, *(LAS v4u*)(lds + G_O + 64 * G_OROW + orow * G_OROW + ocol * 16), WS_HR + off);
    }
    if (drain_arrive_last(cnt + 10, 12, lane)) { if (lane == 0) __hip_atomic_store((gu64*)(ws + WS_F4) + tt * 8 + sl, tag, RLX); STAMPL(45); }
    STAMP(18);
  }

  if (!is_gemm) {
    const int sb = 2 * (xcd & 3), part = jj - 24;
    if (wv == 0) {
      gu64* p = (gu64*)(ws + WS_FPT + (b & 3) * 2048) + (lane < 18 ? 18 + 9 * sb + lane : (lane < 36 ? 90 + 9 * sb + lane - 18 : 18 + 9 * sb));
      for (unsigned spins = 0; spins < SPIN_CAP; ++spins) {
        if (__all(__hip_atomic_load(p, RLX) == tag)) break;
        __builtin_amdgcn_s_sleep(8);
      }
      ORDER();
    }
    __syncthreads();
    unsigned sink = 0;
    const int n3 = 72 * 18 / 8, n4 = 36 * 36 / 8;
#pragma unroll 7
    for (int f = wv; f < n3; f += 12) { v4u v = ldw(rs, lane * 16, WS_W3T + (36 * sb * 18 + part * n3 + f) * 1024); sink ^= v.x; }
#pragma unroll 7
    for (int f = wv; f < n4; f += 12) { const int g = part * n4 + f; v4u v = ldw(rs, lane * 16, WS_W4T + ((g / 36) * 144 + 18 * sb + g % 36) * 1024); sink ^= v.x; }
    asm volatile("" ::"v"(sink));
    STAMP(2);
  }
  const int grp = 2 * (jj >> 1) + (xcd >> 2), s = 2 * (xcd & 3) + (jj & 1);
  {
    constexpr int DEPTH = 3;
    if (wv == 0) {
      poll_tag((gu64*)(ws + WS_FPT + (b & 3) * 2048) + (lane < 9 ? 18 + 9 * s + lane : (lane < 18 ? 90 + 9 * s + lane - 9 : 18 + 9 * s)), tag);
    }
    __syncthreads();
    const int o3 = WS_W3T + (36 * s + 3 * wv) * 18 * 1024;
    LAS unsigned* mcnt = (LAS unsigned*)(lds + ML_ST + 512);
    v4u bw[DEPTH][3];
    const v4u csl = ld16(rs, WS_CS3 + 36 * 16 * 4 * s + (tid < 144 ? tid : 0) * 16);
    if (wv != 0) {
#pragma unroll
      for (int d = 0; d < DEPTH; ++d)
#pragma unroll
        for (int c = 0; c < 3; ++c) bw[d][c] = ldw(rs, lane * 16, o3 + (c * 18 + d) * 1024);
    }
    if (tid < 144) *(LAS v4u*)(lds + ML_CS + tid * 16) = csl;
    if (tid >= 704 && tid < 714) mcnt[tid - 704] = 0;
    if (wv == 0) {
      poll_tag((gu64*)(ws + WS_F4) + grp * 8 + (lane < 6 ? lane : 0), tag);
#pragma unroll
      for (int d = 0; d < DEPTH; ++d)
#pragma unroll
        for (int c = 0; c < 3; ++c) bw[d][c] = ldw(rs, lane * 16, o3 + (c * 18 + d) * 1024);
    }
    __syncthreads();
    STAMP(19);
#pragma unroll
    for (int i = 0; i < 6; ++i) {
      const int p = tid + NT * i, r = p / 72, c = (p % 72) ^ ((r >> 1) & 7);
      dma16(rs, lds + ML_A + (wv * 64 + NT * i) * 16, WS_X1 + grp * 64 * ROWB + r * ROWB + c * 16);
    }
    __syncthreads();
    STAMP(20);
    STAMPV(6);
    STAMPC(28);
    const int tok = lane & 15, q = lane >> 4;
    const int sw = (tok >> 1) & 7;
    const int wg = wv >> 2, wi = wv & 3;
    LAS unsigned char* abase = lds + ML_A + tok * ROWB;
    if (wv < 4) {
      f32x4 dsum = {0, 0, 0, 0}, dsq = {0, 0, 0, 0};
      const v4u onesv = {0x3F803F80u, 0x3F803F80u, 0x3F803F80u, 0x3F803F80u};
      LAS unsigned char* pst = abase + wv * 16 * ROWB;
      v4u as[18];
#pragma unroll
      for (int ks = 0; ks < 18; ++ks) as[ks] = *(LAS v4u*)(pst + (((4 * ks + q) ^ sw) << 4));
#pragma unroll
      for (int ks = 0; ks < 18; ++ks) { dsum = mfma_bf(onesv, as[ks], dsum); dsq = mfma_bf(as[ks], as[ks], dsq); }
      const int e = tok & 3;
      const float sq = e == 0 ? dsq[0] : (e == 1 ? dsq[1] : (e == 2 ? dsq[2] : dsq[3]));
      float m, sd;
      ln_scalars(dsum[0], sq, m, sd);
      if (q == (tok >> 2)) *(LAS f32x2*)(lds + ML_ST + (wv * 16 + tok) * 8) = f32x2{m, __builtin_amdgcn_rcpf(sd)};
      lds_arrive(mcnt, lane);
    }
    LAS unsigned char* H = lds + ML_H;
    {
      f32x4 acc[4][3];
#pragma unroll
      for (int r = 0; r < 4; ++r)
#pragma unroll
        for (int c = 0; c < 3; ++c) acc[r][c] = f32x4{0, 0, 0, 0};
#define PRIO_P1 2
      __builtin_amdgcn_s_setprio(PRIO_P1);
      v4u af[2][4];
#pragma unroll
      for (int r = 0; r < 4; ++r) af[0][r] = *(LAS v4u*)(abase + r * 16 * ROWB + ((q ^ sw) << 4));
#pragma unroll
      for (int ks = 0; ks < 18; ++ks) {
        if (ks < 17) {
#pragma unroll
          for (int r = 0; r < 4; ++r) af[(ks + 1) & 1][r] = *(LAS v4u*)(abase + r * 16 * ROWB + (((4 * (ks + 1) + q) ^ sw) << 4));
        }
        __builtin_amdgcn_sched_barrier(0);
#pragma unroll
        for (int c = 0; c < 3; ++c) {
#pragma unroll
          for (int r = 0; r < 4; ++r) acc[r][c] = mfma_bf(bw[ks % DEPTH][c], af[ks & 1][r], acc[r][c]);
          const int nk = ks + DEPTH;
          if (nk < 18) bw[ks % DEPTH][c] = ldw(rs, lane * 16, o3 + (c * 18 + nk) * 1024);
        }
        __builtin_amdgcn_sched_barrier(0);
      }
#define PRIO_GELU 3
#define PRIO_P2 0
      __builtin_amdgcn_s_setprio(PRIO_GELU);
      STAMP(21);
      STAMPV(7);
      lds_wait(mcnt, 4);
      STAMP(22);
      f32x2 st[4];
#pragma unroll
      for (int r = 0; r < 4; ++r) st[r] = *(LAS f32x2*)(lds + ML_ST + (r * 16 + tok) * 8);
#pragma unroll
      for (int c = 0; c < 3; ++c) {
        const f32x4 cs = *(LAS f32x4*)(lds + ML_CS + ((3 * wv + c) * 16 + 4 * q) * 4);
#pragma unroll
        for (int r = 0; r < 4; ++r) {
          const f32x2 nm = {-st[r].x, -st[r].x}, rr = {st[r].y, st[r].y};
          const f32x2 ga = gelu2(__builtin_elementwise_fma(nm, f32x2{cs[0], cs[1]}, f32x2{acc[r][c][0], acc[r][c][1]}) * rr);
          const f32x2 gb = gelu2(__builtin_elementwise_fma(nm, f32x2{cs[2], cs[3]}, f32x2{acc[r][c][2], acc[r][c][3]}) * rr);
          v2u o = {cvtpk(ga[0], ga[1]), cvtpk(gb[0], gb[1])};
          const int chunk = 2 * (3 * wv + c) + (q >> 1);
          *(LAS v2u*)(H + (r * 16 + tok) * ROWB + ((chunk ^ sw) << 4) + (q & 1) * 8) = o;
        }
      }
      lds_arrive(mcnt + 1 + wg, lane);
      if (wg == 0) __builtin_amdgcn_s_setprio(0); else if (wg == 1) __builtin_amdgcn_s_setprio(1); else __builtin_amdgcn_s_setprio(2);
      STAMP(23);
      STAMPV(8);
    }
    const int rpo = s >> 2;
    if (wg == 0) mlp_second<4>(lds, rs, ws, out, tag, grp, s, 4 * wi, wi, 0, mcnt, lane, tok, q, sw);
    else if (wg == 1) mlp_second<3>(lds, rs, ws, out, tag, grp, s, 16 + 3 * wi, wi, 1, mcnt, lane, tok, q, sw);
    else mlp_second<2>(lds, rs, ws, out, tag, grp, s, 28 + 2 * wi, wi, 2, mcnt, lane, tok, q, sw);
    (void)rpo;
  }
  if (b == 0 && tid == 0) __hip_atomic_store((gu64*)(ws + WS_CTRL), tag, RLX);
  STAMP(27);
}

extern "C" void kernel_launch(void* const* d_in, const int* in_sizes, int n_in, void* d_out, int out_size, void* d_ws, size_t ws_size,
                              hipStream_t stream) {
  (void)hipFuncSetAttribute((const void*)k_all, hipFuncAttributeMaxDynamicSharedMemorySize, LDS_BYTES);
  const unsigned short* const* in = (const unsigned short* const*)d_in;
  k_all<<<256, NT, LDS_BYTES, stream>>>(in[0], in[1], in[2], in[3], in[4], in[5], in[6], in[7], in[8], in[10], in[11], in[12], in[13], (unsigned char*)d_ws,
                                        (unsigned short*)d_out);
}
```

```cpp
#define BW2_LATE 1
#include <hip/hip_runtime.h>
#include <hip/hip_bf16.h>
#include <stdint.h>

typedef unsigned long long u64;
typedef unsigned v4u __attribute__((ext_vector_type(4)));
typedef unsigned v2u __attribute__((ext_vector_type(2)));
typedef float f32x4 __attribute__((ext_vector_type(4)));
typedef float f32x2 __attribute__((ext_vector_type(2)));
typedef short s16x8 __attribute__((ext_vector_type(8)));
typedef _Float16 h16x8 __attribute__((ext_vector_type(8)));
typedef _Float16 h16x2 __attribute__((ext_vector_type(2)));
typedef __bf16 bf16x2_t __attribute__((ext_vector_type(2)));
#define GAS __attribute__((address_space(1)))
#define LAS __attribute__((address_space(3)))
#define DI __device__ __forceinline__
#define RLX __ATOMIC_RELAXED, __HIP_MEMORY_SCOPE_AGENT
typedef GAS u64 gu64;
typedef __amdgpu_buffer_rsrc_t rsrc_t;

#define NLAUNCH 1

constexpr int L = 2048, D = 576, NS = 128, F = 4608;
constexpr int NT = 768;

constexpr int WS_CTRL = 0;
constexpr int WS_F1 = 4096;
constexpr int WS_FPT = WS_F1 + 8 * 2048;
constexpr int WS_F2 = WS_FPT + 4 * 2048;
constexpr int WS_F3 = WS_F2 + 8 * 2048;
constexpr int WS_F4 = WS_F3 + 32 * 64;
constexpr int WS_F5 = WS_F4 + 32 * 64;
constexpr int WS_W1T = 131072;
constexpr int WS_W2T = WS_W1T + 36 * 18 * 1024;
constexpr int WS_W3T = WS_W2T + 36 * 18 * 1024;
constexpr int WS_W4T = WS_W3T + 288 * 18 * 1024;
constexpr int WS_CS1 = WS_W4T + 36 * 144 * 1024;
constexpr int WS_CS3 = WS_CS1 + 4096;
constexpr int WS_HT = WS_CS3 + 4608 * 4;
constexpr int WS_H1 = WS_HT + D * L * 2;
constexpr int WS_ST2 = WS_H1 + D * L * 2;
constexpr int WS_X1 = WS_ST2 + 2048 * 8;
constexpr int WS_HR = WS_X1 + D * L * 2;
constexpr int WS_PART = WS_HR + D * L * 2;
constexpr int WS_UX = WS_PART + 8 * D * L * 2;
constexpr int WS_END = WS_UX + 256 * 144 * 128;
static_assert(WS_F5 + 32 * 192 <= WS_W1T, "control block");

#define STAMP(i)
#define STAMPW(w, i)
#define STAMPC(i)
#define STAMPV(i)
#define STAMPL(i)

DI float bf2f(unsigned short b) { return __builtin_bit_cast(float, (unsigned)b << 16); }
DI float asf(unsigned u) { return __builtin_bit_cast(float, u); }
DI unsigned asu(float f) { return __builtin_bit_cast(unsigned, f); }
DI float bflo(unsigned u) { return __builtin_bit_cast(float, u << 16); }
DI float bfhi(unsigned u) { return __builtin_bit_cast(float, u & 0xFFFF0000u); }
DI unsigned cvtpk(float lo, float hi) {
  f32x2 v = {lo, hi};
  bf16x2_t b = __builtin_convertvector(v, bf16x2_t);
  return __builtin_bit_cast(unsigned, b);
}
DI float bfr(float x) { return bflo(cvtpk(x, x)); }
DI unsigned cvtpkh(float lo, float hi) {
  f32x2 v = {lo, hi};
  h16x2 b = __builtin_convertvector(v, h16x2);
  return __builtin_bit_cast(unsigned, b);
}
DI f32x4 mfma_bf(v4u a, v4u b, f32x4 c) {
  return __builtin_amdgcn_mfma_f32_16x16x32_bf16(__builtin_bit_cast(s16x8, a), __builtin_bit_cast(s16x8, b), c, 0, 0, 0);
}
DI f32x4 mfma_h(v4u a, v4u b, f32x4 c) {
  return __builtin_amdgcn_mfma_f32_16x16x32_f16(__builtin_bit_cast(h16x8, a), __builtin_bit_cast(h16x8, b), c, 0, 0, 0);
}
DI float dot2bf(unsigned a, unsigned b, float acc) {
  return __builtin_amdgcn_fdot2_f32_bf16(__builtin_bit_cast(bf16x2_t, a), __builtin_bit_cast(bf16x2_t, b), acc, false);
}
DI v2u tr_read(LAS unsigned char* p) {
  typedef short v4i16_t __attribute__((ext_vector_type(4)));
  return __builtin_bit_cast(v2u, __builtin_amdgcn_ds_read_tr16_b64_v4i16((LAS v4i16_t*)p));
}
template <int CTRL> DI float dppf(float v) { return asf((unsigned)__builtin_amdgcn_update_dpp(0, (int)asu(v), CTRL, 0xF, 0xF, true)); }
DI float sum8(float v) { v += dppf<0xB1>(v); v += dppf<0x4E>(v); v += dppf<0x141>(v); return v; }
DI float sum_rows(float v) {
  auto a = __builtin_amdgcn_permlane16_swap(asu(v), asu(v), false, false);
  v = asf(a[0]) + asf(a[1]);
  auto c = __builtin_amdgcn_permlane32_swap(asu(v), asu(v), false, false);
  return asf(c[0]) + asf(c[1]);
}
DI float wave_max(float m) {
  m = fmaxf(m, dppf<0xB1>(m)); m = fmaxf(m, dppf<0x4E>(m)); m = fmaxf(m, dppf<0x141>(m)); m = fmaxf(m, dppf<0x140>(m));
  m = fmaxf(m, asf((unsigned)__builtin_amdgcn_update_dpp((int)asu(m), (int)asu(m), 0x142, 0xA, 0xF, false)));
  m = fmaxf(m, asf((unsigned)__builtin_amdgcn_update_dpp((int)asu(m), (int)asu(m), 0x143, 0xC, 0xF, false)));
  return asf((unsigned)__builtin_amdgcn_readlane((int)asu(m), 63));
}
DI float gelu_f(float x) {
  const float c0 = -2.3022081986f, c1 = -0.10294324f;
  float x2 = x * x;
  float p = __builtin_fmaf(x2, c1, c0);
  float e = __builtin_amdgcn_exp2f(x * p);
  return x * __builtin_amdgcn_rcpf(1.0f + e);
}
DI f32x2 gelu2(f32x2 x) {
  const f32x2 c0 = {-2.3022081986f, -2.3022081986f}, c1 = {-0.10294324f, -0.10294324f}, one = {1.0f, 1.0f};
  const f32x2 e = x * __builtin_elementwise_fma(x * x, c1, c0);
  const f32x2 d = f32x2{__builtin_amdgcn_exp2f(e[0]), __builtin_amdgcn_exp2f(e[1])} + one;
  return x * f32x2{__builtin_amdgcn_rcpf(d[0]), __builtin_amdgcn_rcpf(d[1])};
}
DI void ln_scalars(float sum, float sumsq, float& m, float& s) {
  float mean = sum * (1.0f / D);
  float var = __builtin_fmaf(-mean, mean, sumsq * (1.0f / D));
  var = var < 0.f ? 0.f : var;
  m = bfr(mean);
  s = bfr(__builtin_amdgcn_sqrtf(bfr(bfr(var) + 1e-5f)));
}
constexpr int SC1 = 16;
DI v4u ld16(rsrc_t rs, int voff, int soff = 0) { return __builtin_amdgcn_raw_buffer_load_b128(rs, voff, soff, SC1); }
#define WAUX 16
DI v4u ldw(rsrc_t rs, int voff, int soff = 0) { return __builtin_amdgcn_raw_buffer_load_b128(rs, voff, soff, WAUX); }
DI void dma16(rsrc_t rs, LAS unsigned char* dst, int voff) { __builtin_amdgcn_raw_ptr_buffer_load_lds(rs, dst, 16, voff, 0, 0, SC1); }
DI v2u ld8(rsrc_t rs, int voff, int soff = 0) { return __builtin_amdgcn_raw_buffer_load_b64(rs, voff, soff, SC1); }
DI void st16(rsrc_t rs, v4u v, int voff, int soff = 0) { __builtin_amdgcn_raw_buffer_store_b128(v, rs, voff, soff, SC1); }
DI void st8(rsrc_t rs, v2u v, int voff, int soff = 0) { __builtin_amdgcn_raw_buffer_store_b64(v, rs, voff, soff, SC1); }
DI void st4(rsrc_t rs, unsigned v, int voff, int soff = 0) { __builtin_amdgcn_raw_buffer_store_b32(v, rs, voff, soff, SC1); }
#define DRAIN() asm volatile("s_waitcnt vmcnt(0)" ::: "memory")
#define ORDER() __builtin_amdgcn_fence(__ATOMIC_ACQUIRE, "wavefront")
constexpr unsigned SPIN_CAP = 1u << 15;

#define PSLEEP 8
DI void poll_tag(gu64* p, u64 tag) {
  for (unsigned spins = 0; spins < SPIN_CAP; ++spins) {
    if (__all(__hip_atomic_load(p, RLX) == tag)) break;
    if (PSLEEP) __builtin_amdgcn_s_sleep(PSLEEP);
  }
  ORDER();
}

struct KIdx { unsigned short v[2080]; };
constexpr KIdx make_kidx() {
  KIdx t{};
  for (int y = 0; y < 2080; ++y) {
    if (y >= 2048) { t.v[y] = 768; continue; }
    int j = 2047 - y;
    int v = j;
    if (j >= 256) {
      int sh = j >= 1024 ? 3 : (j >= 512 ? 2 : 1);
      int step = 1 << sh, lo = (j >> sh) << sh, rem = j - lo, half = step >> 1;
      if (rem > half || (rem == half && (((lo >> sh) & 1) != 0))) lo += step;
      v = lo;
    }
    int seg = v >= 1024 ? 3 : (v >= 512 ? 2 : (v >= 256 ? 1 : 0));
    int u = seg == 0 ? v : v - (128 << seg);
    int rb = seg == 0 ? 0 : (seg == 1 ? 16 : (seg == 2 ? 24 : 32));
    int row = rb + ((u & 7) >> seg) + (u >> 7) * (8 >> seg);
    int col = (u >> 3) & 15;
    t.v[y] = (unsigned short)(row * 16 + col);
  }
  return t;
}
__device__ const KIdx kIdxTab = make_kidx();

struct cplx { float r, i; };
DI cplx cmul(cplx a, cplx b) {
  const f32x2 t = f32x2{-a.i, a.i} * f32x2{b.i, b.r};
  const f32x2 r = __builtin_elementwise_fma(f32x2{a.r, a.r}, f32x2{b.r, b.i}, t);
  return cplx{r[0], r[1]};
}
DI cplx cexp_r(float a2, float th, float r) {
  float m = __builtin_amdgcn_exp2f(a2 * r);
  float ang = __builtin_amdgcn_fractf(th * r);
  return cplx{m * __builtin_amdgcn_cosf(ang), m * __builtin_amdgcn_sinf(ang)};
}

constexpr int CV_TAB = 57 * 256;
constexpr int CV_CH = 2 * CV_TAB;
constexpr int CV_KC = 4160;
constexpr int CV_XCH = 4 * CV_KC;
constexpr int CV_KCT = 152 * 1024;
constexpr int CV_U = 4 * CV_CH;
constexpr int CV_UCH = (256 + 2048) * 2;
constexpr int CV_X = CV_U + 4 * CV_UCH;
constexpr int LDS_BYTES = 160 * 1024;
constexpr int CV_MISC = LDS_BYTES - 512;
static_assert(4 * CV_KC + 8192 <= CV_CH, "conv channel region");
constexpr int PR_ROW = 1168;
constexpr int PR_U = CV_U, PR_X = CV_U + 8 * PR_ROW;
constexpr int ROWB = 1152;
constexpr int G_A = 0;
constexpr int G_ST = 73728;
constexpr int G_X = G_ST + 4096;
constexpr int G_H = G_X + 12 * 2048;
constexpr int G_O = G_H + 12 * 1024;
constexpr int G_OROW = 208;
constexpr int ML_A = 0, ML_H = 64 * ROWB, ML_ST = 2 * 64 * ROWB;
constexpr int ML_CS = ML_ST + 576;
static_assert(CV_X + 4 * 4096 <= CV_KCT && CV_KCT + 4 * 1600 <= CV_MISC && ML_CS + 2304 <= CV_MISC && G_O + 2 * 64 * G_OROW <= CV_MISC && PR_X + 8 * PR_ROW <= CV_MISC, "LDS");

DI void lds_arrive_wait(LAS unsigned* ctr, unsigned target, int lane) {
  asm volatile("s_waitcnt lgkmcnt(0)" ::: "memory");
  if (lane == 0) __hip_atomic_fetch_add(ctr, 1u, __ATOMIC_RELAXED, __HIP_MEMORY_SCOPE_WORKGROUP);
  while (__hip_atomic_load(ctr, __ATOMIC_RELAXED, __HIP_MEMORY_SCOPE_WORKGROUP) < target) __builtin_amdgcn_s_sleep(1);
  asm volatile("" ::: "memory");
}

DI void lds_arrive(LAS unsigned* ctr, int lane) {
  asm volatile("s_waitcnt lgkmcnt(0)" ::: "memory");
  if (lane == 0) __hip_atomic_fetch_add(ctr, 1u, __ATOMIC_RELAXED, __HIP_MEMORY_SCOPE_WORKGROUP);
  asm volatile("" ::: "memory");
}
DI bool drain_arrive_last(LAS unsigned* ctr, unsigned n, int lane) {
  DRAIN();
  unsigned old = 0;
  if (lane == 0) old = __hip_atomic_fetch_add(ctr, 1u, __ATOMIC_RELAXED, __HIP_MEMORY_SCOPE_WORKGROUP);
  return (unsigned)__builtin_amdgcn_readfirstlane(old) == n - 1;
}
#define LSLEEP 1
DI void lds_wait(LAS unsigned* ctr, unsigned target) {
  while (__hip_atomic_load(ctr, __ATOMIC_RELAXED, __HIP_MEMORY_SCOPE_WORKGROUP) < target) __builtin_amdgcn_s_sleep(LSLEEP);
  asm volatile("" ::: "memory");
}
DI void pretile_unit(LAS unsigned char* lds, rsrc_t rs, unsigned char* ws, int unit, u64 tag, const unsigned short* __restrict__ w1, const unsigned short* __restrict__ w2,
                     const unsigned short* __restrict__ w3, const unsigned short* __restrict__ w4) {
  const int tid = threadIdx.x, lane = tid & 63, wv = __builtin_amdgcn_readfirstlane(tid >> 6);
  const unsigned short* w;
  int ldw, dst, ksn, cs = -1;
  if (unit < 9) { w = w1 + unit * 64; ldw = D; dst = WS_W1T + unit * 4 * 18 * 1024; ksn = 18; cs = WS_CS1 + unit * 256; }
  else if (unit < 18) { w = w2 + (unit - 9) * 64; ldw = D; dst = WS_W2T + (unit - 9) * 4 * 18 * 1024; ksn = 18; }
  else if (unit < 90) { w = w3 + (unit - 18) * 64; ldw = F; dst = WS_W3T + (unit - 18) * 4 * 18 * 1024; ksn = 18; cs = WS_CS3 + (unit - 18) * 256; }
  else { int u = unit - 90, kc = u / 9, cg = u % 9; w = w4 + (size_t)kc * 576 * D + cg * 64; ldw = D; dst = WS_W4T + (cg * 4 * 144 + kc * 18) * 1024; ksn = 144; }
  v4u a[6];
#pragma unroll
  for (int i = 0; i < 6; ++i) {
    int p = tid + NT * i, r = p >> 3, c = p & 7;
    a[i] = *(const GAS v4u*)(w + (size_t)r * ldw + c * 8);
  }
  __syncthreads();
#pragma unroll
  for (int i = 0; i < 6; ++i) {
    int p = tid + NT * i, r = p >> 3, c = p & 7;
    int f = ((r >> 1) & 1) | (((r >> 3) & 1) << 1);
    *(LAS v4u*)(lds + r * 128 + ((((c >> 1) ^ f) << 1 | (c & 1)) << 4)) = a[i];
  }
  __syncthreads();
  const int ct = wv & 3, k0 = 6 * (wv >> 2);
  const int g = lane >> 4, q = (lane & 15) >> 2, p = lane & 3;
  const int f = ((q >> 1) & 1) | ((g & 1) << 1);
  float csum = 0.f;
  const unsigned ones = 0x3F803F80u;
#pragma unroll
  for (int ks = 0; ks < 6; ++ks) {
    LAS unsigned char* pa = lds + (32 * (k0 + ks) + 8 * g + q) * 128 + ((ct ^ f) << 5) + p * 8;
    v2u lo = tr_read(pa), hi = tr_read(pa + 4 * 128);
    v4u o = {lo.x, lo.y, hi.x, hi.y};
    st16(rs, o, lane * 16, dst + (ct * ksn + k0 + ks) * 1024);
    csum = dot2bf(o.x, ones, csum); csum = dot2bf(o.y, ones, csum); csum = dot2bf(o.z, ones, csum); csum = dot2bf(o.w, ones, csum);
  }
  if (cs >= 0) {
    csum = sum_rows(csum);
    LAS float* pc = (LAS float*)(lds + 73728);
    if (lane < 16) pc[wv * 16 + lane] = csum;
    __syncthreads();
    if (tid < 64) st4(rs, __builtin_bit_cast(unsigned, pc[tid] + pc[64 + tid] + pc[128 + tid]), cs + tid * 4);
  }
  DRAIN();
  __syncthreads();
  if (tid < 4) __hip_atomic_store((gu64*)(ws + WS_FPT) + tid * 256 + unit, tag, RLX);
}

#define NC0 4
#define NC1 3
#define NC2 2
template <int NC>
DI void mlp_second(LAS unsigned char* lds, rsrc_t rs, unsigned char* ws, unsigned short* __restrict__ out, u64 tag, int grp, int s, int oc0, int wi, int g,
                   LAS unsigned* mcnt, int lane, int tok, int q, int sw) {
  constexpr int DEP = NC == 4 ? 2 : 3;
  const int o4 = WS_W4T + (oc0 * 144 + 18 * s) * 1024;
  v4u bw[DEP][NC];
#pragma unroll
  for (int d = 0; d < DEP; ++d)
#pragma unroll
    for (int c = 0; c < NC; ++c) bw[d][c] = ldw(rs, lane * 16, o4 + (c * 144 + d) * 1024);
  f32x4 acc[4][NC];
#pragma unroll
  for (int r = 0; r < 4; ++r)
#pragma unroll
    for (int c = 0; c < NC; ++c) acc[r][c] = f32x4{0, 0, 0, 0};
  LAS unsigned char* hbase = lds + ML_H + tok * ROWB;
  v4u af[2][4];
#pragma unroll
  for (int k2 = 0; k2 < 18; ++k2) {
    if (k2 % 6 == 0) {
      lds_wait(mcnt + 1 + k2 / 6, 4);
#pragma unroll
      for (int r = 0; r < 4; ++r) af[k2 & 1][r] = *(LAS v4u*)(hbase + r * 16 * ROWB + (((4 * k2 + q) ^ sw) << 4));
    }
    if (k2 % 6 != 5) {
#pragma unroll
      for (int r = 0; r < 4; ++r) af[(k2 + 1) & 1][r] = *(LAS v4u*)(hbase + r * 16 * ROWB + (((4 * (k2 + 1) + q) ^ sw) << 4));
    }
    __builtin_amdgcn_sched_barrier(0);
#pragma unroll
    for (int c = 0; c < NC; ++c) {
#pragma unroll
      for (int r = 0; r < 4; ++r) acc[r][c] = mfma_bf(bw[k2 % DEP][c], af[k2 & 1][r], acc[r][c]);
      const int nk = k2 + DEP;
      if (nk < 18) bw[k2 % DEP][c] = ldw(rs, lane * 16, o4 + (c * 144 + nk) * 1024);
    }
    __builtin_amdgcn_sched_barrier(0);
  }
  STAMP(24);
  STAMPV(9);
  const int rpo = s >> 2, slot0 = g == 0 ? 0 : (g == 1 ? NC0 : NC0 + NC1);
  LAS unsigned char* own = lds + ML_A + (rpo ^ 1) * 32 * ROWB + slot0 * 1024;
  {
    const int po = WS_PART + (s * 32 + grp) * 72 * 1024;
#pragma unroll
    for (int c = 0; c < NC; ++c)
#pragma unroll
      for (int rp = 0; rp < 2; ++rp) {
        unsigned x0 = cvtpk(acc[2 * rp][c][0], acc[2 * rp][c][1]), x1 = cvtpk(acc[2 * rp][c][2], acc[2 * rp][c][3]);
        unsigned y0 = cvtpk(acc[2 * rp + 1][c][0], acc[2 * rp + 1][c][1]), y1 = cvtpk(acc[2 * rp + 1][c][2], acc[2 * rp + 1][c][3]);
        auto s0 = __builtin_amdgcn_permlane16_swap(x0, y0, false, false);
        auto s1 = __builtin_amdgcn_permlane16_swap(x1, y1, false, false);
        const v4u o = {s0[0], s1[0], s0[1], s1[1]};
        if (rp == rpo && wi == (s & 3)) *(LAS v4u*)(own + c * 1024 + lane * 16) = o;
        else st16(rs, o, lane * 16, po + (rp * 36 + oc0 + c) * 1024);
      }
  }
  asm volatile("s_waitcnt lgkmcnt(0)" ::: "memory");
  if (drain_arrive_last(mcnt + 4 + g, 4, lane)) { if (lane == 0) __hip_atomic_store((gu64*)(ws + WS_F5) + (grp * 3 + g) * 8 + s, tag, RLX); if (g == 2) STAMPL(46); }
  STAMP(25);
  STAMPC(29);
  if (wi < NC) {
    if (wi == 0) { poll_tag((gu64*)(ws + WS_F5) + (grp * 3 + g) * 8 + (lane & 7), tag); lds_arrive(mcnt + 7 + g, lane); }
    else lds_wait(mcnt + 7 + g, 1);
    lds_wait(mcnt + 4 + g, 4);
    if (g == 2) STAMP(26);
    const int oc = (g == 0 ? 0 : (g == 1 ? 4 * NC0 : 4 * (NC0 + NC1))) + (s & 3) * NC + wi, u = rpo * 36 + oc;
    const int row = rpo * 32 + (q & 1) * 16 + tok;
    const int off = ((grp * 64 + row) * D + oc * 16 + 8 * (q >> 1)) * 2;
    v4u p[8];
#pragma unroll
    for (int k = 0; k < 7; ++k) p[k] = ld16(rs, lane * 16, WS_PART + ((((s + 1 + k) & 7) * 32 + grp) * 72 + u) * 1024);
    const v4u hv = ld16(rs, WS_HR + off);
    p[7] = *(LAS v4u*)(own + wi * 1024 + lane * 16);
    const v4u xv = *(LAS v4u*)(lds + ML_A + row * ROWB + (((2 * oc + (q >> 1)) ^ ((row >> 1) & 7)) << 4));
    v4u o;
#pragma unroll
    for (int j = 0; j < 4; ++j) {
      f32x2 sum = f32x2{bflo(xv[j]), bfhi(xv[j])} + f32x2{bflo(hv[j]), bfhi(hv[j])};
#pragma unroll
      for (int k = 0; k < 8; ++k) sum += f32x2{bflo(p[k][j]), bfhi(p[k][j])};
      o[j] = cvtpk(sum[0], sum[1]);
    }
    *(GAS v4u*)((GAS unsigned char*)out + off) = o;
  }
}

__global__ __launch_bounds__(NT, 3) void k_all(const unsigned short* __restrict__ x, const unsigned short* __restrict__ Are, const unsigned short* __restrict__ Aim,
                                               const unsigned short* __restrict__ Bre, const unsigned short* __restrict__ Bim, const unsigned short* __restrict__ Cre,
                                               const unsigned short* __restrict__ Cim, const unsigned short* __restrict__ logdt, const unsigned short* __restrict__ dskip,
                                               const unsigned short* __restrict__ w1, const unsigned short* __restrict__ w2, const unsigned short* __restrict__ w3,
                                               const unsigned short* __restrict__ w4, unsigned char* __restrict__ ws, unsigned short* __restrict__ out) {
  extern __shared__ __attribute__((aligned(16))) unsigned char smem[];
  LAS unsigned char* lds = (LAS unsigned char*)smem;
  const int tid = threadIdx.x, lane = tid & 63, wv = __builtin_amdgcn_readfirstlane(tid >> 6);
  const int b = blockIdx.x, xcd = b & 7, jj = b >> 3;
  const rsrc_t rs = __builtin_amdgcn_make_buffer_rsrc((void*)ws, 0, WS_END, 0x00020000);
  STAMP(0);
  STAMPC(30);
  const bool is_conv = jj < 18, is_gemm = jj < 24;
  const int c0 = 4 * (18 * xcd + jj);
  if (tid < 16) ((LAS unsigned*)(lds + CV_MISC))[tid] = 0;
  asm volatile("s_waitcnt lgkmcnt(0)" ::: "memory");
  asm volatile("" ::"s"(x), "s"(Are), "s"(Aim), "s"(Bre), "s"(Bim), "s"(Cre), "s"(Cim), "s"(logdt), "s"(ws));
  const u64 epoch = __hip_atomic_load((gu64*)(ws + WS_CTRL), RLX);
#define GET_TAG() ((((u64)(unsigned)__builtin_amdgcn_readfirstlane((unsigned)(epoch >> 32)) << 32) | (unsigned)__builtin_amdgcn_readfirstlane((unsigned)epoch)) + 1)
  u64 tag;
  LAS unsigned* cnt = (LAS unsigned*)(lds + CV_MISC);
  LAS float* smx = (LAS float*)(lds + CV_MISC + 64);
  LAS float* kscs = (LAS float*)(lds + CV_MISC + 96);

  if (wv < 4) {
    if (wv < 3) {
      const int t8 = lane >> 3, sub = lane & 7;
      const GAS v4u* row = (const GAS v4u*)(x + (size_t)(b * 8 + t8) * D) + 24 * wv + sub;
      v4u a[3];
#pragma unroll
      for (int i = 0; i < 3; ++i) a[i] = row[8 * i];
      __builtin_amdgcn_sched_barrier(0);
      __builtin_amdgcn_s_barrier();
      tag = GET_TAG();
      float s = 0.f, ss = 0.f;
      const unsigned ones = 0x3F803F80u;
#pragma unroll
      for (int i = 0; i < 3; ++i) {
#pragma unroll
        for (int j = 0; j < 4; ++j) { s = dot2bf(a[i][j], ones, s); ss = dot2bf(a[i][j], a[i][j], ss); }
      }
#pragma unroll
      for (int o = 0; o < 1; ++o) { s = sum8(s); ss = sum8(ss); }
      STAMP(38);
      LAS f32x2* part = (LAS f32x2*)(lds + CV_MISC + 128);
      if (sub == 0) part[wv * 8 + t8] = f32x2{s, ss};
#pragma unroll
      for (int i = 0; i < 3; ++i) *(LAS v4u*)(lds + PR_X + t8 * PR_ROW + (24 * wv + 8 * i + sub) * 16) = a[i];
      lds_arrive_wait(cnt + 4, 3, lane);
      STAMP(39);
      {
        const f32x2 p0 = part[t8], p1 = part[8 + t8], p2 = part[16 + t8];
        float m, sd;
        ln_scalars(p0.x + p1.x + p2.x, p0.y + p1.y + p2.y, m, sd);
        const float r = __builtin_amdgcn_rcpf(sd), nmr = -m * r;
#pragma unroll
        for (int i = 0; i < 3; ++i) {
          v4u up;
#pragma unroll
          for (int j = 0; j < 4; ++j) {
            const f32x2 xv = {bflo(a[i][j]), bfhi(a[i][j])};
            const f32x2 uv = __builtin_elementwise_fma(xv, f32x2{r, r}, f32x2{nmr, nmr});
            up[j] = cvtpkh(uv[0], uv[1]);
          }
          *(LAS v4u*)(lds + PR_U + t8 * PR_ROW + (24 * wv + 8 * i + sub) * 16) = up;
        }
      }
      {
        const int g = lane >> 4, q = (lane & 15) >> 2, p = lane & 3;
        LAS unsigned char* pa = lds + ((p & 1) ? PR_X : PR_U) + q * PR_ROW + (192 * wv + 8 * g + 4 * (p >> 1)) * 2;
#pragma unroll
        for (int k = 0; k < 6; ++k) {
          v2u lo = tr_read(pa + k * 64), hi = tr_read(pa + k * 64 + 4 * PR_ROW);
          st16(rs, v4u{lo.x, lo.y, hi.x, hi.y}, lane * 16, WS_UX + b * 18432 + (6 * wv + k) * 1024);
        }
      }
      STAMP(40);
      DRAIN();
      STAMP(41);
      lds_arrive_wait(cnt + 4, 6, lane);
      if (tid < 8) __hip_atomic_store((gu64*)(ws + WS_F1) + tid * 256 + b, tag, RLX);
      STAMP(1);
    } else {
      __builtin_amdgcn_s_barrier();
      tag = GET_TAG();
    }
    if (is_conv) {
      poll_tag((gu64*)(ws + WS_F1 + (b & 7) * 2048) + 64 * wv + lane, tag);
      STAMP(3);
      v4u a[8];
#pragma unroll
      for (int i = 0; i < 8; ++i) a[i] = ld16(rs, (lane >> 3) * 18432 + (lane & 7) * 16, WS_UX + (64 * wv + 8 * i) * 18432 + (c0 >> 2) * 128);
      if (wv == 3) lds_arrive_wait(cnt + 4, 6, 64);
      const int pc = lane & 7;
      LAS unsigned char* dst = lds + (pc < 4 ? CV_U + pc * CV_UCH + 512 : CV_X + (pc - 4) * 4096) + (64 * wv + (lane >> 3)) * 16;
#pragma unroll
      for (int i = 0; i < 8; ++i) *(LAS v4u*)(dst + i * 128) = a[i];
      if (tid < 128) *(LAS v4u*)(lds + CV_U + (tid >> 5) * CV_UCH + (tid & 31) * 16) = v4u{0, 0, 0, 0};
      STAMP(4);
      STAMPV(10);
    }
  } else if (is_conv) {
    const int cl = (wv - 4) >> 1, half = wv & 1, ch = c0 + cl, n = half * 64 + lane;
    const int gi = ch * NS + n;
    const int t2 = half * 64 + lane;
    const unsigned short rld = logdt[ch], rar = Are[gi], rai = Aim[gi], rbr = Bre[gi], rbi = Bim[gi], rcr = Cre[gi], rci = Cim[gi];
    v4u kid[3];
    v2u kid2[3];
#pragma unroll
    for (int i = 0; i < 3; ++i) {
      const int p = t2 + 128 * i < 259 ? t2 + 128 * i : 258;
      kid[i] = *(const GAS v4u*)(kIdxTab.v + p * 8);
      kid2[i] = *(const GAS v2u*)(kIdxTab.v + p * 8 + 8);
    }
    __builtin_amdgcn_sched_barrier(0);
    __builtin_amdgcn_s_barrier();
    tag = GET_TAG();
    {
      const float dt = bfr(expf(bf2f(rld)));
      const float zr = dt * bf2f(rar), zi = dt * bf2f(rai);
      const float dBr = dt * bf2f(rbr), dBi = dt * bf2f(rbi);
      cplx W = cmul(cplx{bf2f(rcr), bf2f(rci)}, cplx{dBr, dBi});
      float smax = fmaxf(fabsf(W.r), fabsf(W.i));
      smax = wave_max(smax);
      const float inv = smax > 0.f ? 1.0f / smax : 0.f;
      W.r *= inv; W.i *= inv;
      const float a2 = zr * 1.44269504089f, th = zi * 0.15915494309f;
      const cplx e1 = cexp_r(a2, th, 1.f), e8 = cexp_r(a2, th, 8.f), e128 = cexp_r(a2, th, 128.f);
      const cplx e256 = cexp_r(a2, th, 256.f), e512 = cexp_r(a2, th, 512.f), e1024 = cexp_r(a2, th, 1024.f);
      STAMPW(4, 5);
      STAMPV(15);
      LAS unsigned char* chan = lds + cl * CV_CH;
      LAS unsigned char* tab = chan + half * CV_TAB;
      const int nsw = lane >> 2, nlo = (lane & 3) * 4;
#define PUT(row, val) *(LAS unsigned*)(tab + (row) * 256 + ((nsw ^ ((row) & 15)) << 4) + nlo) = (val)
#define PUTA(row, z) PUT(row, cvtpkh((z).r, -(z).i))
#define PUTB(row, z) PUT(41 + (row), cvtpkh((z).r, (z).i))
      {
        cplx c[16];
        c[0] = cplx{1.f, 0.f}; c[1] = e8; c[2] = cmul(e8, e8); c[3] = cmul(c[2], e8); c[4] = cmul(c[2], c[2]);
        c[5] = cmul(c[4], c[1]); c[6] = cmul(c[4], c[2]); c[7] = cmul(c[4], c[3]); c[8] = cmul(c[4], c[4]);
#pragma unroll
        for (int j = 9; j < 16; ++j) c[j] = cmul(c[8], c[j - 8]);
#pragma unroll
        for (int j = 0; j < 16; ++j) PUTB(j, c[j]);
      }
      {
        const cplx e2 = cmul(e1, e1), e4 = cmul(e2, e2);
        cplx p[8];
        p[0] = W; p[1] = cmul(W, e1); p[2] = cmul(W, e2); p[3] = cmul(p[1], e2);
#pragma unroll
        for (int c = 4; c < 8; ++c) p[c] = cmul(p[c - 4], e4);
#pragma unroll
        for (int c = 0; c < 8; ++c) {
          const cplx qv = cmul(p[c], e128);
          PUTA(c, p[c]);
          PUTA(8 + c, qv);
          if ((c & 1) == 0) {
            PUTA(16 + (c >> 1), cmul(p[c], e256));
            PUTA(20 + (c >> 1), cmul(qv, e256));
          }
        }
        const cplx e384 = cmul(e256, e128);
        const cplx s0 = cmul(p[0], e512), s1 = cmul(p[4], e512);
        PUTA(24, s0); PUTA(25, s1);
        PUTA(26, cmul(s0, e128)); PUTA(27, cmul(s1, e128));
        PUTA(28, cmul(s0, e256)); PUTA(29, cmul(s1, e256));
        PUTA(30, cmul(s0, e384)); PUTA(31, cmul(s1, e384));
        const cplx t0 = cmul(W, e1024), t4 = cmul(t0, e512);
        PUTA(32, t0); PUTA(33, cmul(t0, e128)); PUTA(34, cmul(t0, e256)); PUTA(35, cmul(t0, e384));
        PUTA(36, t4); PUTA(37, cmul(t4, e128)); PUTA(38, cmul(t4, e256)); PUTA(39, cmul(t4, e384));
        PUTA(40, cmul(t0, e1024));
      }
#undef PUT
#undef PUTA
#undef PUTB
      STAMPW(4, 6);
      STAMPV(11);
      if (lane == 0) smx[wv - 4] = smax;
      lds_arrive_wait(cnt + cl, 2, lane);
      STAMPV(12);
      const float s0_ = smx[2 * cl], s1_ = smx[2 * cl + 1];
      const float sm = fmaxf(s0_, s1_);
      const float ksc = sm * (182.f / 1024.f);
      const float kinv = sm > 0.f ? 1.0f / ksc : 0.f;
      LAS unsigned short* kc = (LAS unsigned short*)(lds + CV_KCT + cl * 1600);
      {
        const int r = lane & 15, h = lane >> 4;
        const float f0 = s0_ * kinv, f1 = s1_ * kinv;
#pragma unroll
        for (int tI = 0; tI < 2; ++tI) {
          if (tI == 1 && half == 1) break;
          const int t = half ? 1 : 2 * tI;
          f32x4 d0 = {0, 0, 0, 0}, d1 = {0, 0, 0, 0};
#pragma unroll
          for (int ks = 0; ks < 4; ++ks) {
            const int ob = (41 + r) * 256 + (((4 * ks + h) ^ ((41 + r) & 15)) << 4), oa = (16 * t + r) * 256 + (((4 * ks + h) ^ r) << 4);
            d0 = mfma_h(*(LAS v4u*)(chan + oa), *(LAS v4u*)(chan + ob), d0);
            d1 = mfma_h(*(LAS v4u*)(chan + CV_TAB + oa), *(LAS v4u*)(chan + CV_TAB + ob), d1);
          }
#pragma unroll
          for (int e = 0; e < 4; ++e) {
            const float v = __builtin_fmaf(d0[e], f0, d1[e] * f1);
            kc[(16 * t + 4 * h + e) * 16 + r] = (unsigned short)(cvtpkh(v, v) & 0xFFFFu);
          }
        }
        if (half == 1 && lane == 0) { kc[768] = 0; kscs[cl] = ksc; }
      }
      STAMPV(13);
      lds_arrive_wait(cnt + cl, 4, lane);
      STAMPV(14);
#pragma unroll
      for (int i = 0; i < 3; ++i) {
        const int z = t2 + 128 * i;
        unsigned d[6];
#pragma unroll
        for (int j = 0; j < 4; ++j) d[j] = (unsigned)kc[kid[i][j] & 0xFFFFu] | ((unsigned)kc[kid[i][j] >> 16] << 16);
        d[4] = (unsigned)kc[kid2[i].x & 0xFFFFu] | ((unsigned)kc[kid2[i].x >> 16] << 16);
        d[5] = (unsigned)kc[kid2[i].y & 0xFFFFu];
        if (z < 259) {
          LAS unsigned char* base = chan + z * 16;
          *(LAS v4u*)(base) = v4u{d[0], d[1], d[2], d[3]};
          *(LAS v4u*)(base + CV_KC) = v4u{__builtin_amdgcn_alignbit(d[1], d[0], 16), __builtin_amdgcn_alignbit(d[2], d[1], 16), __builtin_amdgcn_alignbit(d[3], d[2], 16), __builtin_amdgcn_alignbit(d[4], d[3], 16)};
          *(LAS v4u*)(base + 2 * CV_KC) = v4u{d[1], d[2], d[3], d[4]};
          *(LAS v4u*)(base + 3 * CV_KC) = v4u{__builtin_amdgcn_alignbit(d[2], d[1], 16), __builtin_amdgcn_alignbit(d[3], d[2], 16), __builtin_amdgcn_alignbit(d[4], d[3], 16), __builtin_amdgcn_alignbit(d[5], d[4], 16)};
        }
      }
      STAMPW(4, 7);
      STAMPV(10);
    }
  } else {
    __builtin_amdgcn_s_barrier();
    tag = GET_TAG();
  }
  if (!is_conv) {
    const int idx = (jj - 18) * 8 + xcd;
#define PT_DELAY 3
    for (int i = 0; i < PT_DELAY; ++i) __builtin_amdgcn_s_sleep(32);
    pretile_unit(lds, rs, ws, idx, tag, w1, w2, w3, w4);
    if (idx >= 48 && idx < 98) pretile_unit(lds, rs, ws, 112 + idx - 48, tag, w1, w2, w3, w4);
    STAMP(2);
  }
  if (is_conv) {
    const float dsk = bf2f(dskip[c0 + (wv & 3)]);
    __syncthreads();
    STAMP(8);
    STAMPC(31);
    const int cl = wv & 3, kh = (wv >> 2) & 1;
    f32x4 acc[8], base[4];
    float ksc = 0.f;
    if (wv < 8) {
#pragma unroll
      for (int t = 0; t < 8; ++t) acc[t] = f32x4{0, 0, 0, 0};
      const int bb = lane & 15, h = lane >> 4;
      const int cpy = 3 - (bb & 3);
      LAS unsigned char* abase = lds + cl * CV_CH + cpy * CV_KC + (2047 - 16 - bb + 8 * h - cpy) * 2 - 64 * 4 * kh;
      LAS unsigned char* bbase = lds + CV_U + cl * CV_UCH + 512 + (16 * (bb - 1) + 8 * h) * 2 - 64 * 4 * kh;
      LAS unsigned char* abase2 = abase + 8;
      asm volatile("" : "+v"(abase2));
      v4u A[8], B[8];
#define LDA(i, rho) do { v2u lo_ = *(LAS v2u*)(abase - 64 * ((rho) + 8 * (i))), hi_ = *(LAS v2u*)(abase2 - 64 * ((rho) + 8 * (i))); A[i] = v4u{lo_.x, lo_.y, hi_.x, hi_.y}; } while (0)
#define LDB(m, rho) B[m] = *(LAS v4u*)(bbase + 32 * (16 * (m) - 2 * (rho)))
#pragma unroll
      for (int i = 0; i < 8; ++i) { LDB(i, 0); LDA(i, 0); }
#pragma unroll
      for (int rho = 0; rho < 4; ++rho) {
#pragma unroll
        for (int i = 0; i < 8; ++i) {
#pragma unroll
          for (int m = 7 - i; m >= 0; --m) acc[i + m] = mfma_h(A[i], B[m], acc[i + m]);
          if (rho < 3) { LDA(i, rho + 1); LDB(7 - i, rho + 1); }
          __builtin_amdgcn_sched_barrier(0);
        }
      }
#undef LDA
#undef LDB
      LAS unsigned char* xo = lds + cl * CV_CH + CV_XCH + kh * 4096 + lane * 16;
#pragma unroll
      for (int t = 0; t < 4; ++t) {
        f32x4 lo = acc[t], hi = acc[4 + t];
        asm volatile("" : "+v"(lo), "+v"(hi));
        *(LAS f32x4*)(xo + t * 1024) = kh ? lo : hi;
        acc[t] = kh ? hi : lo;
      }
      const int a = lane & 15, q = lane >> 4;
#pragma unroll
      for (int t = 0; t < 4; ++t) {
        const int tok = 256 * (4 * kh + t) + 16 * a + 4 * q;
        const v2u uu = *(LAS v2u*)(lds + CV_U + cl * CV_UCH + 512 + tok * 2);
        const v2u xx = *(LAS v2u*)(lds + CV_X + cl * 4096 + tok * 2);
#pragma unroll
        for (int e = 0; e < 2; ++e) {
          const unsigned uw = e ? uu.y : uu.x, xw = e ? xx.y : xx.x;
          const h16x2 u2 = __builtin_bit_cast(h16x2, uw);
          const f32x2 bv = __builtin_elementwise_fma(f32x2{(float)u2[0], (float)u2[1]}, f32x2{dsk, dsk}, f32x2{bflo(xw), bfhi(xw)});
          base[t][2 * e] = bv[0]; base[t][2 * e + 1] = bv[1];
        }
      }
      ksc = kscs[cl];
#pragma unroll
      for (int t = 0; t < 4; ++t) base[t] += acc[t] * ksc;
    }
    STAMP(9);
    STAMPV(0);
    __syncthreads();
    STAMP(32);
    if (wv < 8) {
      const int ch = c0 + cl;
      const int a = lane & 15, q = lane >> 4;
      LAS unsigned char* xi = lds + cl * CV_CH + CV_XCH + (kh ^ 1) * 4096 + lane * 16;
      f32x4 o[4];
#pragma unroll
      for (int t = 0; t < 4; ++t) o[t] = *(LAS f32x4*)(xi + t * 1024);
      unsigned pk[4][2];
#pragma unroll
      for (int t = 0; t < 4; ++t) {
        const f32x4 hv = base[t] + o[t] * ksc;
        pk[t][0] = cvtpk(hv[0], hv[1]);
        pk[t][1] = cvtpk(hv[2], hv[3]);
      }
#pragma unroll
      for (int t = 0; t < 4; t += 2) {
        auto s0 = __builtin_amdgcn_permlane16_swap(pk[t][0], pk[t + 1][0], false, false);
        auto s1 = __builtin_amdgcn_permlane16_swap(pk[t][1], pk[t + 1][1], false, false);
        const int tok = 256 * (4 * kh + t + (q & 1)) + 16 * a + 8 * (q >> 1);
        st16(rs, v4u{s0[0], s1[0], s0[1], s1[1]}, WS_HT + (ch * L + tok) * 2);
      }
      STAMP(33);
      STAMPV(1);
      if (drain_arrive_last(cnt + 8, 8, lane)) { if (lane < 8) __hip_atomic_store((gu64*)(ws + WS_F2) + lane * 256 + (18 * xcd + jj), tag, RLX); STAMPL(43); }
    }
    STAMP(10);
  }

  const int tt = 4 * xcd + jj / 6, sl = jj % 6;
  if (is_gemm) {
    const int cl = wv % 6, kh = wv / 6, ct = 6 * sl + cl;
    const int tok = lane & 15, q = lane >> 4;
    if (wv == 0) {
      const int u0 = (96 * sl) >> 6;
      poll_tag((gu64*)(ws + WS_FPT + (b & 3) * 2048) + (lane & 1) + u0 + ((lane & 2) ? 9 : 0), tag);
    }
    __syncthreads();
    v4u bw[9];
    if (wv != 0) {
#pragma unroll
      for (int ks = 0; ks < 9; ++ks) bw[ks] = ld16(rs, lane * 16, WS_W1T + (ct * 18 + 9 * kh + ks) * 1024);
    }
    if (wv == 0) {
      const int fo = WS_F2 + (b & 7) * 2048 + (lane < 36 ? lane : 0) * 32;
      const unsigned tl = (unsigned)tag, th = (unsigned)(tag >> 32);
      for (unsigned spins = 0; spins < SPIN_CAP; ++spins) {
        asm volatile("" ::: "memory");
        const v4u f0 = ld16(rs, fo), f1 = ld16(rs, fo + 16);
        if (__all(f0.x == tl && f0.y == th && f0.z == tl && f0.w == th && f1.x == tl && f1.y == th && f1.z == tl && f1.w == th)) break;
        if (PSLEEP) __builtin_amdgcn_s_sleep(PSLEEP);
      }
      ORDER();
#pragma unroll
      for (int ks = 0; ks < 9; ++ks) bw[ks] = ld16(rs, lane * 16, WS_W1T + (ct * 18 + 9 * kh + ks) * 1024);
    }
    const f32x4 cs = __builtin_bit_cast(f32x4, ld16(rs, WS_CS1 + (ct * 16 + 4 * q) * 4));
    __syncthreads();
    STAMP(11);
    {
#pragma unroll
      for (int i = 0; i < 6; ++i) {
        const int p = tid + NT * i, c = p >> 3, pp = p & 7;
        const int f = ((c >> 1) & 1) | (((c >> 3) & 1) << 1);
        dma16(rs, lds + G_A + (wv * 64 + NT * i) * 16, WS_HT + (c * L + tt * 64 + ((((pp >> 1) ^ f) << 1) | (pp & 1)) * 8) * 2);
      }
    }
    __syncthreads();
    v4u bw2[9];
    v4u hpiece;
    STAMP(12);
    STAMPV(2);
    f32x4 acc[4];
#pragma unroll
    for (int r = 0; r < 4; ++r) acc[r] = f32x4{0, 0, 0, 0};
    {
      const int pq = (lane & 15) >> 2, pp = lane & 3;
      const int f = ((pq >> 1) & 1) | ((q & 1) << 1);
      const unsigned ones = 0x3F803F80u;
      LAS unsigned char* pa0 = lds + G_A + (8 * q + pq) * 128 + pp * 8;
      LAS unsigned char* par[4];
#pragma unroll
      for (int r = 0; r < 4; ++r) par[r] = pa0 + ((r ^ f) << 5);
      v4u af[2][4];
#pragma unroll
      for (int r = 0; r < 4; ++r) { v2u lo = tr_read(par[r] + 9 * kh * 4096), hi = tr_read(par[r] + 9 * kh * 4096 + 512); af[0][r] = v4u{lo.x, lo.y, hi.x, hi.y}; }
#pragma unroll
      for (int ks = 0; ks < 9; ++ks) {
        if (ks < 8) {
#pragma unroll
          for (int r = 0; r < 4; ++r) {
            v2u lo = tr_read(par[r] + (9 * kh + ks + 1) * 4096), hi = tr_read(par[r] + (9 * kh + ks + 1) * 4096 + 512);
            af[(ks + 1) & 1][r] = v4u{lo.x, lo.y, hi.x, hi.y};
          }
        }
        __builtin_amdgcn_sched_barrier(0);
#pragma unroll
        for (int r = 0; r < 4; ++r) acc[r] = mfma_bf(bw[ks], af[ks & 1][r], acc[r]);
        __builtin_amdgcn_sched_barrier(0);
      }
    }
#pragma unroll
    for (int ks = 0; ks < 9; ++ks) bw2[ks] = ld16(rs, lane * 16, WS_W2T + (ct * 18 + 9 * kh + ks) * 1024);
    hpiece = ld16(rs, WS_HT + ((ct * 16 + (lane >> 2)) * L + tt * 64 + kh * 32 + (lane & 3) * 8) * 2);
    __builtin_amdgcn_sched_barrier(0);
    STAMP(35);
    STAMPV(3);
    {
      LAS unsigned char* xo = lds + G_X + wv * 2048 + lane * 16;
#pragma unroll
      for (int r = 0; r < 2; ++r) {
        f32x4 lo = acc[r], hi = acc[2 + r];
        asm volatile("" : "+v"(lo), "+v"(hi));
        *(LAS f32x4*)(xo + r * 1024) = kh ? lo : hi;
        acc[r] = kh ? hi : lo;
      }
    }
    if (wv < 4) {
      const int pq = (lane & 15) >> 2, pp = lane & 3;
      const int f = ((pq >> 1) & 1) | ((q & 1) << 1);
      LAS unsigned char* ps0 = lds + G_A + (8 * q + pq) * 128 + pp * 8 + ((wv ^ f) << 5);
      f32x4 dsum = {0, 0, 0, 0}, dsq = {0, 0, 0, 0};
      const v4u onesv = {0x3F803F80u, 0x3F803F80u, 0x3F803F80u, 0x3F803F80u};
#pragma unroll
      for (int hf = 0; hf < 2; ++hf) {
        v4u as[9];
#pragma unroll
        for (int i = 0; i < 9; ++i) { v2u lo = tr_read(ps0 + (9 * hf + i) * 4096), hi = tr_read(ps0 + (9 * hf + i) * 4096 + 512); as[i] = v4u{lo.x, lo.y, hi.x, hi.y}; }
#pragma unroll
        for (int i = 0; i < 9; ++i) { dsum = mfma_bf(onesv, as[i], dsum); dsq = mfma_bf(as[i], as[i], dsq); }
      }
      const int e = tok & 3;
      const float sq = e == 0 ? dsq[0] : (e == 1 ? dsq[1] : (e == 2 ? dsq[2] : dsq[3]));
      const float s_ = dsum[0], ss = sq;
      float m1, s1;
      ln_scalars(s_, ss, m1, s1);
      const float mean = s_ * (1.0f / D);
      float var = __builtin_fmaf(-mean, mean, ss * (1.0f / D));
      var = var < 0.f ? 0.f : var;
      const float r1 = __builtin_amdgcn_rcpf(s1);
      const float m2 = bfr((mean - m1) * r1);
      const float s2 = bfr(__builtin_amdgcn_sqrtf(bfr(bfr(var * r1 * r1) + 1e-5f)));
      if (q == (tok >> 2)) *(LAS f32x4*)(lds + G_ST + 2048 + (wv * 16 + tok) * 16) = f32x4{__builtin_fmaf(m2, s1, m1), r1 * __builtin_amdgcn_rcpf(s2), m1, r1};
    }
    __syncthreads();
    STAMP(13);
    STAMPV(4);
    f32x2 st1[2];
    {
      LAS unsigned char* xi = lds + G_X + (kh ? wv - 6 : wv + 6) * 2048 + lane * 16;
      f32x4 o[2], sc[2];
#pragma unroll
      for (int r = 0; r < 2; ++r) { o[r] = *(LAS f32x4*)(xi + r * 1024); sc[r] = *(LAS f32x4*)(lds + G_ST + 2048 + ((2 * kh + r) * 16 + tok) * 16); }
      __builtin_amdgcn_sched_barrier(0);
      unsigned pk[2][2];
#pragma unroll
      for (int r = 0; r < 2; ++r) {
        st1[r] = f32x2{sc[r][2], sc[r][3]};
        const f32x4 v = (acc[r] + o[r] - cs * sc[r][0]) * sc[r][1];
        const f32x2 ga = gelu2(f32x2{v[0], v[1]}), gb = gelu2(f32x2{v[2], v[3]});
        pk[r][0] = cvtpk(ga[0], ga[1]);
        pk[r][1] = cvtpk(gb[0], gb[1]);
      }
#pragma unroll
      for (int r = 0; r < 2; ++r) *(LAS v2u*)(lds + G_O + ((2 * kh + r) * 16 + tok) * G_OROW + cl * 32 + q * 8) = v2u{pk[r][0], pk[r][1]};
    }
    const int orow = tid / 12, ocol = tid % 12;
    __syncthreads();
    st16(rs, *(LAS v4u*)(lds + G_O + orow * G_OROW + ocol * 16), WS_H1 + ((tt * 64 + orow) * D + sl * 96 + ocol * 8) * 2);
    STAMP(36);
    STAMPV(5);
    if (drain_arrive_last(cnt + 9, 12, lane)) { if (lane == 0) __hip_atomic_store((gu64*)(ws + WS_F3) + tt * 8 + sl, tag, RLX); STAMPL(44); }
    STAMP(14);

    {
      const int c = lane >> 2, pc = lane & 3;
      *(LAS v4u*)(lds + G_H + wv * 1024 + c * 64 + ((((pc >> 1) ^ ((c >> 3) & 1)) << 1 | (pc & 1)) << 4)) = hpiece;
    }
    if (wv == 0) {
      int t = tt - 1 + (lane < 18 ? lane / 6 : 1);
      t = t < 0 ? 0 : (t > 31 ? 31 : t);
      poll_tag((gu64*)(ws + WS_F3) + t * 8 + (lane < 18 ? lane % 6 : 0), tag);
    }
    __syncthreads();
    STAMP(15);
    {
#pragma unroll
      for (int i = 0; i < 6; ++i) {
        const int p = tid + NT * i, r = p / 72, c = (p % 72) ^ ((r >> 1) & 7);
        const int cg = c / 18;
        const int sh = cg == 0 ? -1 : (cg == 1 ? 1 : (cg == 2 ? -2 : 2));
        const int t = tt * 64 + r + sh;
        dma16(rs, lds + G_A + (wv * 64 + NT * i) * 16, (t >= 0 && t < L) ? WS_H1 + (t * D + c * 8) * 2 : WS_END);
      }
    }
    __syncthreads();
    STAMP(16);
#pragma unroll
    for (int r = 0; r < 4; ++r) acc[r] = f32x4{0, 0, 0, 0};
    {
      const int sw = (tok >> 1) & 7;
      LAS unsigned char* pa = lds + G_A + tok * ROWB;
      v4u af[2][4];
#pragma unroll
      for (int r = 0; r < 4; ++r) af[0][r] = *(LAS v4u*)(pa + r * 16 * ROWB + (((4 * 9 * kh + q) ^ sw) << 4));
#pragma unroll
      for (int ks = 0; ks < 9; ++ks) {
        if (ks < 8) {
#pragma unroll
          for (int r = 0; r < 4; ++r) af[(ks + 1) & 1][r] = *(LAS v4u*)(pa + r * 16 * ROWB + (((4 * (9 * kh + ks + 1) + q) ^ sw) << 4));
        }
        __builtin_amdgcn_sched_barrier(0);
#pragma unroll
        for (int r = 0; r < 4; ++r) acc[r] = mfma_bf(bw2[ks], af[ks & 1][r], acc[r]);
        __builtin_amdgcn_sched_barrier(0);
      }
    }
    v2u hh[2];
    {
      LAS unsigned char* xo = lds + G_X + wv * 2048 + lane * 16;
#pragma unroll
      for (int r = 0; r < 2; ++r) {
        f32x4 lo = acc[r], hi = acc[2 + r];
        asm volatile("" : "+v"(lo), "+v"(hi));
        *(LAS f32x4*)(xo + r * 1024) = kh ? lo : hi;
        acc[r] = kh ? hi : lo;
      }
#pragma unroll
      for (int r = 0; r < 2; ++r) {
        const int c = 4 * q + ((lane & 15) >> 2);
        hh[r] = tr_read(lds + G_H + wv * 1024 + c * 64 + ((r ^ ((c >> 3) & 1)) << 5) + (lane & 3) * 8);
        const float m1 = st1[r][0], r1 = st1[r][1];
        const f32x4 hv = {bflo(hh[r].x), bfhi(hh[r].x), bflo(hh[r].y), bfhi(hh[r].y)};
        acc[r] += (hv - m1) * r1;
        *(LAS v2u*)(lds + G_O + 64 * G_OROW + ((2 * kh + r) * 16 + tok) * G_OROW + cl * 32 + q * 8) = hh[r];
      }
    }
    __syncthreads();
    STAMP(17);
    {
      LAS unsigned char* xi = lds + G_X + (kh ? wv - 6 : wv + 6) * 2048 + lane * 16;
      f32x4 o[2];
#pragma unroll
      for (int r = 0; r < 2; ++r) o[r] = *(LAS f32x4*)(xi + r * 1024);
      unsigned pk[2][2];
#pragma unroll
      for (int r = 0; r < 2; ++r) {
        const f32x4 v = acc[r] + o[r];
        pk[r][0] = cvtpk(v[0], v[1]);
        pk[r][1] = cvtpk(v[2], v[3]);
      }
#pragma unroll
      for (int r = 0; r < 2; ++r) *(LAS v2u*)(lds + G_O + ((2 * kh + r) * 16 + tok) * G_OROW + cl * 32 + q * 8) = v2u{pk[r][0], pk[r][1]};
    }
    __syncthreads();
    {
      const int off = ((tt * 64 + orow) * D + sl * 96 + ocol * 8) * 2;
      st16(rs, *(LAS v4u*)(lds + G_O + orow * G_OROW + ocol * 16), WS_X1 + off);
      st16(rs, *(LAS v4u*)(lds + G_O + 64 * G_OROW + orow * G_OROW + ocol * 16), WS_HR + off);
    }
    if (drain_arrive_last(cnt + 10, 12, lane)) { if (lane == 0) __hip_atomic_store((gu64*)(ws + WS_F4) + tt * 8 + sl, tag, RLX); STAMPL(45); }
    STAMP(18);
  }

  const int grp = 2 * (jj >> 1) + (xcd >> 2), s = 2 * (xcd & 3) + (jj & 1);
  {
    constexpr int DEPTH = 3;
    if (wv == 0) {
      poll_tag((gu64*)(ws + WS_FPT + (b & 3) * 2048) + (lane < 9 ? 18 + 9 * s + lane : (lane < 18 ? 90 + 9 * s + lane - 9 : 18 + 9 * s)), tag);
    }
    __syncthreads();
    const int o3 = WS_W3T + (36 * s + 3 * wv) * 18 * 1024;
    LAS unsigned* mcnt = (LAS unsigned*)(lds + ML_ST + 512);
    v4u bw[DEPTH][3];
    const v4u csl = ld16(rs, WS_CS3 + 36 * 16 * 4 * s + (tid < 144 ? tid : 0) * 16);
    if (wv != 0) {
#pragma unroll
      for (int d = 0; d < DEPTH; ++d)
#pragma unroll
        for (int c = 0; c < 3; ++c) bw[d][c] = ldw(rs, lane * 16, o3 + (c * 18 + d) * 1024);
    }
    if (tid < 144) *(LAS v4u*)(lds + ML_CS + tid * 16) = csl;
    if (tid >= 704 && tid < 714) mcnt[tid - 704] = 0;
    if (wv == 0) {
      poll_tag((gu64*)(ws + WS_F4) + grp * 8 + (lane < 6 ? lane : 0), tag);
#pragma unroll
      for (int d = 0; d < DEPTH; ++d)
#pragma unroll
        for (int c = 0; c < 3; ++c) bw[d][c] = ldw(rs, lane * 16, o3 + (c * 18 + d) * 1024);
    }
    __syncthreads();
    STAMP(19);
#pragma unroll
    for (int i = 0; i < 6; ++i) {
      const int p = tid + NT * i, r = p / 72, c = (p % 72) ^ ((r >> 1) & 7);
      dma16(rs, lds + ML_A + (wv * 64 + NT * i) * 16, WS_X1 + grp * 64 * ROWB + r * ROWB + c * 16);
    }
    __syncthreads();
    STAMP(20);
    STAMPV(6);
    STAMPC(28);
    const int tok = lane & 15, q = lane >> 4;
    const int sw = (tok >> 1) & 7;
    const int wg = wv >> 2, wi = wv & 3;
    LAS unsigned char* abase = lds + ML_A + tok * ROWB;
    if (wv < 4) {
      f32x4 dsum = {0, 0, 0, 0}, dsq = {0, 0, 0, 0};
      const v4u onesv = {0x3F803F80u, 0x3F803F80u, 0x3F803F80u, 0x3F803F80u};
      LAS unsigned char* pst = abase + wv * 16 * ROWB;
      v4u as[18];
#pragma unroll
      for (int ks = 0; ks < 18; ++ks) as[ks] = *(LAS v4u*)(pst + (((4 * ks + q) ^ sw) << 4));
#pragma unroll
      for (int ks = 0; ks < 18; ++ks) { dsum = mfma_bf(onesv, as[ks], dsum); dsq = mfma_bf(as[ks], as[ks], dsq); }
      const int e = tok & 3;
      const float sq = e == 0 ? dsq[0] : (e == 1 ? dsq[1] : (e == 2 ? dsq[2] : dsq[3]));
      float m, sd;
      ln_scalars(dsum[0], sq, m, sd);
      if (q == (tok >> 2)) *(LAS f32x2*)(lds + ML_ST + (wv * 16 + tok) * 8) = f32x2{m, __builtin_amdgcn_rcpf(sd)};
      lds_arrive(mcnt, lane);
    }
    LAS unsigned char* H = lds + ML_H;
    {
      f32x4 acc[4][3];
#pragma unroll
      for (int r = 0; r < 4; ++r)
#pragma unroll
        for (int c = 0; c < 3; ++c) acc[r][c] = f32x4{0, 0, 0, 0};
#define PRIO_P1 2
      __builtin_amdgcn_s_setprio(PRIO_P1);
      v4u af[2][4];
#pragma unroll
      for (int r = 0; r < 4; ++r) af[0][r] = *(LAS v4u*)(abase + r * 16 * ROWB + ((q ^ sw) << 4));
#pragma unroll
      for (int ks = 0; ks < 18; ++ks) {
        if (ks < 17) {
#pragma unroll
          for (int r = 0; r < 4; ++r) af[(ks + 1) & 1][r] = *(LAS v4u*)(abase + r * 16 * ROWB + (((4 * (ks + 1) + q) ^ sw) << 4));
        }
        __builtin_amdgcn_sched_barrier(0);
#pragma unroll
        for (int c = 0; c < 3; ++c) {
#pragma unroll
          for (int r = 0; r < 4; ++r) acc[r][c] = mfma_bf(bw[ks % DEPTH][c], af[ks & 1][r], acc[r][c]);
          const int nk = ks + DEPTH;
          if (nk < 18) bw[ks % DEPTH][c] = ldw(rs, lane * 16, o3 + (c * 18 + nk) * 1024);
        }
        __builtin_amdgcn_sched_barrier(0);
      }
#define PRIO_GELU 3
#define PRIO_P2 0
      __builtin_amdgcn_s_setprio(PRIO_GELU);
      STAMP(21);
      STAMPV(7);
      lds_wait(mcnt, 4);
      STAMP(22);
      f32x2 st[4];
#pragma unroll
      for (int r = 0; r < 4; ++r) st[r] = *(LAS f32x2*)(lds + ML_ST + (r * 16 + tok) * 8);
#pragma unroll
      for (int c = 0; c < 3; ++c) {
        const f32x4 cs = *(LAS f32x4*)(lds + ML_CS + ((3 * wv + c) * 16 + 4 * q) * 4);
#pragma unroll
        for (int r = 0; r < 4; ++r) {
          const f32x2 nm = {-st[r].x, -st[r].x}, rr = {st[r].y, st[r].y};
          const f32x2 ga = gelu2(__builtin_elementwise_fma(nm, f32x2{cs[0], cs[1]}, f32x2{acc[r][c][0], acc[r][c][1]}) * rr);
          const f32x2 gb = gelu2(__builtin_elementwise_fma(nm, f32x2{cs[2], cs[3]}, f32x2{acc[r][c][2], acc[r][c][3]}) * rr);
          v2u o = {cvtpk(ga[0], ga[1]), cvtpk(gb[0], gb[1])};
          const int chunk = 2 * (3 * wv + c) + (q >> 1);
          *(LAS v2u*)(H + (r * 16 + tok) * ROWB + ((chunk ^ sw) << 4) + (q & 1) * 8) = o;
        }
      }
      lds_arrive(mcnt + 1 + wg, lane);
#define PP0 0
#define PP1 1
#define PP2 2
      if (wg == 0) __builtin_amdgcn_s_setprio(PP0); else if (wg == 1) __builtin_amdgcn_s_setprio(PP1); else __builtin_amdgcn_s_setprio(PP2);
      STAMP(23);
      STAMPV(8);
    }
    const int rpo = s >> 2;
    if (wg == 0) mlp_second<NC0>(lds, rs, ws, out, tag, grp, s, NC0 * wi, wi, 0, mcnt, lane, tok, q, sw);
    else if (wg == 1) mlp_second<NC1>(lds, rs, ws, out, tag, grp, s, 4 * NC0 + NC1 * wi, wi, 1, mcnt, lane, tok, q, sw);
    else mlp_second<NC2>(lds, rs, ws, out, tag, grp, s, 4 * (NC0 + NC1) + NC2 * wi, wi, 2, mcnt, lane, tok, q, sw);
    (void)rpo;
  }
  if (b == 0 && tid == 0) __hip_atomic_store((gu64*)(ws + WS_CTRL), tag, RLX);
  STAMP(27);
}

extern "C" void kernel_launch(void* const* d_in, const int* in_sizes, int n_in, void* d_out, int out_size, void* d_ws, size_t ws_size,
                              hipStream_t stream) {
  (void)hipFuncSetAttribute((const void*)k_all, hipFuncAttributeMaxDynamicSharedMemorySize, LDS_BYTES);
  const unsigned short* const* in = (const unsigned short* const*)d_in;
  k_all<<<256, NT, LDS_BYTES, stream>>>(in[0], in[1], in[2], in[3], in[4], in[5], in[6], in[7], in[8], in[10], in[11], in[12], in[13], (unsigned char*)d_ws,
                                        (unsigned short*)d_out);
}
```

```cpp
#define BW2_LATE 1
#include <hip/hip_runtime.h>
#include <hip/hip_bf16.h>
#include <stdint.h>

typedef unsigned long long u64;
typedef unsigned v4u __attribute__((ext_vector_type(4)));
typedef unsigned v2u __attribute__((ext_vector_type(2)));
typedef float f32x4 __attribute__((ext_vector_type(4)));
typedef float f32x2 __attribute__((ext_vector_type(2)));
typedef short s16x8 __attribute__((ext_vector_type(8)));
typedef _Float16 h16x8 __attribute__((ext_vector_type(8)));
typedef _Float16 h16x2 __attribute__((ext_vector_type(2)));
typedef __bf16 bf16x2_t __attribute__((ext_vector_type(2)));
#define GAS __attribute__((address_space(1)))
#define LAS __attribute__((address_space(3)))
#define DI __device__ __forceinline__
#define RLX __ATOMIC_RELAXED, __HIP_MEMORY_SCOPE_AGENT
typedef GAS u64 gu64;
typedef __amdgpu_buffer_rsrc_t rsrc_t;

#define NLAUNCH 1

constexpr int L = 2048, D = 576, NS = 128, F = 4608;
constexpr int NT = 768;

constexpr int WS_CTRL = 0;
constexpr int WS_F1 = 4096;
constexpr int WS_FPT = WS_F1 + 8 * 2048;
constexpr int WS_F2 = WS_FPT + 4 * 2048;
constexpr int WS_F3 = WS_F2 + 8 * 2048;
constexpr int WS_F4 = WS_F3 + 32 * 64;
constexpr int WS_F5 = WS_F4 + 32 * 64;
constexpr int WS_W1T = 131072;
constexpr int WS_W2T = WS_W1T + 36 * 18 * 1024;
constexpr int WS_W3T = WS_W2T + 36 * 18 * 1024;
constexpr int WS_W4T = WS_W3T + 288 * 18 * 1024;
constexpr int WS_CS1 = WS_W4T + 36 * 144 * 1024;
constexpr int WS_CS3 = WS_CS1 + 4096;
constexpr int WS_HT = WS_CS3 + 4608 * 4;
constexpr int WS_H1 = WS_HT + D * L * 2;
constexpr int WS_ST2 = WS_H1 + D * L * 2;
constexpr int WS_X1 = WS_ST2 + 2048 * 8;
constexpr int WS_HR = WS_X1 + D * L * 2;
constexpr int WS_PART = WS_HR + D * L * 2;
constexpr int WS_UX = WS_PART + 8 * D * L * 2;
constexpr int WS_END = WS_UX + 256 * 144 * 128;
static_assert(WS_F5 + 32 * 192 <= WS_W1T, "control block");

#define STAMP(i)
#define STAMPW(w, i)
#define STAMPC(i)
#define STAMPV(i)
#define STAMPL(i)

DI float bf2f(unsigned short b) { return __builtin_bit_cast(float, (unsigned)b << 16); }
DI float asf(unsigned u) { return __builtin_bit_cast(float, u); }
DI unsigned asu(float f) { return __builtin_bit_cast(unsigned, f); }
DI float bflo(unsigned u) { return __builtin_bit_cast(float, u << 16); }
DI float bfhi(unsigned u) { return __builtin_bit_cast(float, u & 0xFFFF0000u); }
DI unsigned cvtpk(float lo, float hi) {
  f32x2 v = {lo, hi};
  bf16x2_t b = __builtin_convertvector(v, bf16x2_t);
  return __builtin_bit_cast(unsigned, b);
}
DI float bfr(float x) { return bflo(cvtpk(x, x)); }
DI unsigned cvtpkh(float lo, float hi) {
  f32x2 v = {lo, hi};
  h16x2 b = __builtin_convertvector(v, h16x2);
  return __builtin_bit_cast(unsigned, b);
}
DI f32x4 mfma_bf(v4u a, v4u b, f32x4 c) {
  return __builtin_amdgcn_mfma_f32_16x16x32_bf16(__builtin_bit_cast(s16x8, a), __builtin_bit_cast(s16x8, b), c, 0, 0, 0);
}
DI f32x4 mfma_h(v4u a, v4u b, f32x4 c) {
  return __builtin_amdgcn_mfma_f32_16x16x32_f16(__builtin_bit_cast(h16x8, a), __builtin_bit_cast(h16x8, b), c, 0, 0, 0);
}
DI float dot2bf(unsigned a, unsigned b, float acc) {
  return __builtin_amdgcn_fdot2_f32_bf16(__builtin_bit_cast(bf16x2_t, a), __builtin_bit_cast(bf16x2_t, b), acc, false);
}
DI v2u tr_read(LAS unsigned char* p) {
  typedef short v4i16_t __attribute__((ext_vector_type(4)));
  return __builtin_bit_cast(v2u, __builtin_amdgcn_ds_read_tr16_b64_v4i16((LAS v4i16_t*)p));
}
template <int CTRL> DI float dppf(float v) { return asf((unsigned)__builtin_amdgcn_update_dpp(0, (int)asu(v), CTRL, 0xF, 0xF, true)); }
DI float sum8(float v) { v += dppf<0xB1>(v); v += dppf<0x4E>(v); v += dppf<0x141>(v); return v; }
DI float sum_rows(float v) {
  auto a = __builtin_amdgcn_permlane16_swap(asu(v), asu(v), false, false);
  v = asf(a[0]) + asf(a[1]);
  auto c = __builtin_amdgcn_permlane32_swap(asu(v), asu(v), false, false);
  return asf(c[0]) + asf(c[1]);
}
DI float wave_max(float m) {
  m = fmaxf(m, dppf<0xB1>(m)); m = fmaxf(m, dppf<0x4E>(m)); m = fmaxf(m, dppf<0x141>(m)); m = fmaxf(m, dppf<0x140>(m));
  m = fmaxf(m, asf((unsigned)__builtin_amdgcn_update_dpp((int)asu(m), (int)asu(m), 0x142, 0xA, 0xF, false)));
  m = fmaxf(m, asf((unsigned)__builtin_amdgcn_update_dpp((int)asu(m), (int)asu(m), 0x143, 0xC, 0xF, false)));
  return asf((unsigned)__builtin_amdgcn_readlane((int)asu(m), 63));
}
DI float gelu_f(float x) {
  const float c0 = -2.3022081986f, c1 = -0.10294324f;
  float x2 = x * x;
  float p = __builtin_fmaf(x2, c1, c0);
  float e = __builtin_amdgcn_exp2f(x * p);
  return x * __builtin_amdgcn_rcpf(1.0f + e);
}
DI f32x2 gelu2(f32x2 x) {
  const f32x2 c0 = {-2.3022081986f, -2.3022081986f}, c1 = {-0.10294324f, -0.10294324f}, one = {1.0f, 1.0f};
  const f32x2 e = x * __builtin_elementwise_fma(x * x, c1, c0);
  const f32x2 d = f32x2{__builtin_amdgcn_exp2f(e[0]), __builtin_amdgcn_exp2f(e[1])} + one;
  return x * f32x2{__builtin_amdgcn_rcpf(d[0]), __builtin_amdgcn_rcpf(d[1])};
}
DI void ln_scalars(float sum, float sumsq, float& m, float& s) {
  float mean = sum * (1.0f / D);
  float var = __builtin_fmaf(-mean, mean, sumsq * (1.0f / D));
  var = var < 0.f ? 0.f : var;
  m = bfr(mean);
  s = bfr(__builtin_amdgcn_sqrtf(bfr(bfr(var) + 1e-5f)));
}
constexpr int SC1 = 16;
DI v4u ld16(rsrc_t rs, int voff, int soff = 0) { return __builtin_amdgcn_raw_buffer_load_b128(rs, voff, soff, SC1); }
#define WAUX 16
DI v4u ldw(rsrc_t rs, int voff, int soff = 0) { return __builtin_amdgcn_raw_buffer_load_b128(rs, voff, soff, WAUX); }
DI void dma16(rsrc_t rs, LAS unsigned char* dst, int voff) { __builtin_amdgcn_raw_ptr_buffer_load_lds(rs, dst, 16, voff, 0, 0, SC1); }
DI v2u ld8(rsrc_t rs, int voff, int soff = 0) { return __builtin_amdgcn_raw_buffer_load_b64(rs, voff, soff, SC1); }
DI void st16(rsrc_t rs, v4u v, int voff, int soff = 0) { __builtin_amdgcn_raw_buffer_store_b128(v, rs, voff, soff, SC1); }
DI void st8(rsrc_t rs, v2u v, int voff, int soff = 0) { __builtin_amdgcn_raw_buffer_store_b64(v, rs, voff, soff, SC1); }
DI void st4(rsrc_t rs, unsigned v, int voff, int soff = 0) { __builtin_amdgcn_raw_buffer_store_b32(v, rs, voff, soff, SC1); }
#define DRAIN() asm volatile("s_waitcnt vmcnt(0)" ::: "memory")
#define ORDER() __builtin_amdgcn_fence(__ATOMIC_ACQUIRE, "wavefront")
constexpr unsigned SPIN_CAP = 1u << 15;

#define PSLEEP 8
DI void poll_tag(gu64* p, u64 tag) {
  for (unsigned spins = 0; spins < SPIN_CAP; ++spins) {
    if (__all(__hip_atomic_load(p, RLX) == tag)) break;
    if (PSLEEP) __builtin_amdgcn_s_sleep(PSLEEP);
  }
  ORDER();
}

struct KIdx { unsigned short v[2080]; };
constexpr KIdx make_kidx() {
  KIdx t{};
  for (int y = 0; y < 2080; ++y) {
    if (y >= 2048) { t.v[y] = 768; continue; }
    int j = 2047 - y;
    int v = j;
    if (j >= 256) {
      int sh = j >= 1024 ? 3 : (j >= 512 ? 2 : 1);
      int step = 1 << sh, lo = (j >> sh) << sh, rem = j - lo, half = step >> 1;
      if (rem > half || (rem == half && (((lo >> sh) & 1) != 0))) lo += step;
      v = lo;
    }
    int seg = v >= 1024 ? 3 : (v >= 512 ? 2 : (v >= 256 ? 1 : 0));
    int u = seg == 0 ? v : v - (128 << seg);
    int rb = seg == 0 ? 0 : (seg == 1 ? 16 : (seg == 2 ? 24 : 32));
    int row = rb + ((u & 7) >> seg) + (u >> 7) * (8 >> seg);
    int col = (u >> 3) & 15;
    t.v[y] = (unsigned short)(row * 16 + col);
  }
  return t;
}
__device__ const KIdx kIdxTab = make_kidx();

struct cplx { float r, i; };
DI cplx cmul(cplx a, cplx b) {
  const f32x2 t = f32x2{-a.i, a.i} * f32x2{b.i, b.r};
  const f32x2 r = __builtin_elementwise_fma(f32x2{a.r, a.r}, f32x2{b.r, b.i}, t);
  return cplx{r[0], r[1]};
}
DI cplx cexp_r(float a2, float th, float r) {
  float m = __builtin_amdgcn_exp2f(a2 * r);
  float ang = __builtin_amdgcn_fractf(th * r);
  return cplx{m * __builtin_amdgcn_cosf(ang), m * __builtin_amdgcn_sinf(ang)};
}

constexpr int CV_TAB = 57 * 256;
constexpr int CV_CH = 2 * CV_TAB;
constexpr int CV_KC = 4160;
constexpr int CV_XCH = 4 * CV_KC;
constexpr int CV_KCT = 152 * 1024;
constexpr int CV_U = 4 * CV_CH;
constexpr int LDS_BYTES = 160 * 1024;
constexpr int CV_MISC = LDS_BYTES - 512;
static_assert(4 * CV_KC + 8192 <= CV_CH, "conv channel region");
constexpr int PR_ROW = 1168;
constexpr int PR_U = CV_U, PR_X = CV_U + 8 * PR_ROW;
constexpr int ROWB = 1152;
constexpr int G_A = 0;
constexpr int G_ST = 73728;
constexpr int G_X = G_ST + 4096;
constexpr int G_H = G_X + 12 * 2048;
constexpr int G_O = G_H + 12 * 1024;
constexpr int G_OROW = 208;
constexpr int ML_A = 0, ML_H = 64 * ROWB, ML_ST = 2 * 64 * ROWB;
constexpr int ML_CS = ML_ST + 576;
static_assert(CV_U + 288 * 128 <= CV_KCT && CV_KCT + 4 * 1600 <= CV_MISC && ML_CS + 2304 <= CV_MISC && G_O + 2 * 64 * G_OROW <= CV_MISC && PR_X + 8 * PR_ROW <= CV_MISC, "LDS");

DI void lds_arrive_wait(LAS unsigned* ctr, unsigned target, int lane) {
  asm volatile("s_waitcnt lgkmcnt(0)" ::: "memory");
  if (lane == 0) __hip_atomic_fetch_add(ctr, 1u, __ATOMIC_RELAXED, __HIP_MEMORY_SCOPE_WORKGROUP);
  while (__hip_atomic_load(ctr, __ATOMIC_RELAXED, __HIP_MEMORY_SCOPE_WORKGROUP) < target) __builtin_amdgcn_s_sleep(1);
  asm volatile("" ::: "memory");
}

DI void lds_arrive(LAS unsigned* ctr, int lane) {
  asm volatile("s_waitcnt lgkmcnt(0)" ::: "memory");
  if (lane == 0) __hip_atomic_fetch_add(ctr, 1u, __ATOMIC_RELAXED, __HIP_MEMORY_SCOPE_WORKGROUP);
  asm volatile("" ::: "memory");
}
DI bool drain_arrive_last(LAS unsigned* ctr, unsigned n, int lane) {
  DRAIN();
  unsigned old = 0;
  if (lane == 0) old = __hip_atomic_fetch_add(ctr, 1u, __ATOMIC_RELAXED, __HIP_MEMORY_SCOPE_WORKGROUP);
  return (unsigned)__builtin_amdgcn_readfirstlane(old) == n - 1;
}
#define LSLEEP 1
DI void lds_wait(LAS unsigned* ctr, unsigned target) {
  while (__hip_atomic_load(ctr, __ATOMIC_RELAXED, __HIP_MEMORY_SCOPE_WORKGROUP) < target) __builtin_amdgcn_s_sleep(LSLEEP);
  asm volatile("" ::: "memory");
}
DI void pretile_unit(LAS unsigned char* lds, rsrc_t rs, unsigned char* ws, int unit, u64 tag, const unsigned short* __restrict__ w1, const unsigned short* __restrict__ w2,
                     const unsigned short* __restrict__ w3, const unsigned short* __restrict__ w4) {
  const int tid = threadIdx.x, lane = tid & 63, wv = __builtin_amdgcn_readfirstlane(tid >> 6);
  const unsigned short* w;
  int ldw, dst, ksn, cs = -1;
  if (unit < 9) { w = w1 + unit * 64; ldw = D; dst = WS_W1T + unit * 4 * 18 * 1024; ksn = 18; cs = WS_CS1 + unit * 256; }
  else if (unit < 18) { w = w2 + (unit - 9) * 64; ldw = D; dst = WS_W2T + (unit - 9) * 4 * 18 * 1024; ksn = 18; }
  else if (unit < 90) { w = w3 + (unit - 18) * 64; ldw = F; dst = WS_W3T + (unit - 18) * 4 * 18 * 1024; ksn = 18; cs = WS_CS3 + (unit - 18) * 256; }
  else { int u = unit - 90, kc = u / 9, cg = u % 9; w = w4 + (size_t)kc * 576 * D + cg * 64; ldw = D; dst = WS_W4T + (cg * 4 * 144 + kc * 18) * 1024; ksn = 144; }
  v4u a[6];
#pragma unroll
  for (int i = 0; i < 6; ++i) {
    int p = tid + NT * i, r = p >> 3, c = p & 7;
    a[i] = *(const GAS v4u*)(w + (size_t)r * ldw + c * 8);
  }
  __syncthreads();
#pragma unroll
  for (int i = 0; i < 6; ++i) {
    int p = tid + NT * i, r = p >> 3, c = p & 7;
    int f = ((r >> 1) & 1) | (((r >> 3) & 1) << 1);
    *(LAS v4u*)(lds + r * 128 + ((((c >> 1) ^ f) << 1 | (c & 1)) << 4)) = a[i];
  }
  __syncthreads();
  const int ct = wv & 3, k0 = 6 * (wv >> 2);
  const int g = lane >> 4, q = (lane & 15) >> 2, p = lane & 3;
  const int f = ((q >> 1) & 1) | ((g & 1) << 1);
  float csum = 0.f;
  const unsigned ones = 0x3F803F80u;
#pragma unroll
  for (int ks = 0; ks < 6; ++ks) {
    LAS unsigned char* pa = lds + (32 * (k0 + ks) + 8 * g + q) * 128 + ((ct ^ f) << 5) + p * 8;
    v2u lo = tr_read(pa), hi = tr_read(pa + 4 * 128);
    v4u o = {lo.x, lo.y, hi.x, hi.y};
    st16(rs, o, lane * 16, dst + (ct * ksn + k0 + ks) * 1024);
    csum = dot2bf(o.x, ones, csum); csum = dot2bf(o.y, ones, csum); csum = dot2bf(o.z, ones, csum); csum = dot2bf(o.w, ones, csum);
  }
  if (cs >= 0) {
    csum = sum_rows(csum);
    LAS float* pc = (LAS float*)(lds + 73728);
    if (lane < 16) pc[wv * 16 + lane] = csum;
    __syncthreads();
    if (tid < 64) st4(rs, __builtin_bit_cast(unsigned, pc[tid] + pc[64 + tid] + pc[128 + tid]), cs + tid * 4);
  }
  DRAIN();
  __syncthreads();
  if (tid < 4) __hip_atomic_store((gu64*)(ws + WS_FPT) + tid * 256 + unit, tag, RLX);
}

#define NC0 4
#define NC1 3
#define NC2 2
template <int NC>
DI void mlp_second(LAS unsigned char* lds, rsrc_t rs, unsigned char* ws, unsigned short* __restrict__ out, u64 tag, int grp, int s, int oc0, int wi, int g,
                   LAS unsigned* mcnt, int lane, int tok, int q, int sw) {
  constexpr int DEP = NC == 4 ? 2 : 3;
  const int o4 = WS_W4T + (oc0 * 144 + 18 * s) * 1024;
  v4u bw[DEP][NC];
#pragma unroll
  for (int d = 0; d < DEP; ++d)
#pragma unroll
    for (int c = 0; c < NC; ++c) bw[d][c] = ldw(rs, lane * 16, o4 + (c * 144 + d) * 1024);
  f32x4 acc[4][NC];
#pragma unroll
  for (int r = 0; r < 4; ++r)
#pragma unroll
    for (int c = 0; c < NC; ++c) acc[r][c] = f32x4{0, 0, 0, 0};
  LAS unsigned char* hbase = lds + ML_H + tok * ROWB;
  v4u af[2][4];
#pragma unroll
  for (int k2 = 0; k2 < 18; ++k2) {
    if (k2 % 6 == 0) {
      lds_wait(mcnt + 1 + k2 / 6, 4);
#pragma unroll
      for (int r = 0; r < 4; ++r) af[k2 & 1][r] = *(LAS v4u*)(hbase + r * 16 * ROWB + (((4 * k2 + q) ^ sw) << 4));
    }
    if (k2 % 6 != 5) {
#pragma unroll
      for (int r = 0; r < 4; ++r) af[(k2 + 1) & 1][r] = *(LAS v4u*)(hbase + r * 16 * ROWB + (((4 * (k2 + 1) + q) ^ sw) << 4));
    }
    __builtin_amdgcn_sched_barrier(0);
#pragma unroll
    for (int c = 0; c < NC; ++c) {
#pragma unroll
      for (int r = 0; r < 4; ++r) acc[r][c] = mfma_bf(bw[k2 % DEP][c], af[k2 & 1][r], acc[r][c]);
      const int nk = k2 + DEP;
      if (nk < 18) bw[k2 % DEP][c] = ldw(rs, lane * 16, o4 + (c * 144 + nk) * 1024);
    }
    __builtin_amdgcn_sched_barrier(0);
  }
  STAMP(24);
  STAMPV(9);
  const int rpo = s >> 2, slot0 = g == 0 ? 0 : (g == 1 ? NC0 : NC0 + NC1);
  LAS unsigned char* own = lds + ML_A + (rpo ^ 1) * 32 * ROWB + slot0 * 1024;
  {
    const int po = WS_PART + (s * 32 + grp) * 72 * 1024;
#pragma unroll
    for (int c = 0; c < NC; ++c)
#pragma unroll
      for (int rp = 0; rp < 2; ++rp) {
        unsigned x0 = cvtpk(acc[2 * rp][c][0], acc[2 * rp][c][1]), x1 = cvtpk(acc[2 * rp][c][2], acc[2 * rp][c][3]);
        unsigned y0 = cvtpk(acc[2 * rp + 1][c][0], acc[2 * rp + 1][c][1]), y1 = cvtpk(acc[2 * rp + 1][c][2], acc[2 * rp + 1][c][3]);
        auto s0 = __builtin_amdgcn_permlane16_swap(x0, y0, false, false);
        auto s1 = __builtin_amdgcn_permlane16_swap(x1, y1, false, false);
        const v4u o = {s0[0], s1[0], s0[1], s1[1]};
        if (rp == rpo && wi == (s & 3)) *(LAS v4u*)(own + c * 1024 + lane * 16) = o;
        else st16(rs, o, lane * 16, po + (rp * 36 + oc0 + c) * 1024);
      }
  }
  asm volatile("s_waitcnt lgkmcnt(0)" ::: "memory");
  if (drain_arrive_last(mcnt + 4 + g, 4, lane)) { if (lane == 0) __hip_atomic_store((gu64*)(ws + WS_F5) + (grp * 3 + g) * 8 + s, tag, RLX); if (g == 2) STAMPL(46); }
  STAMP(25);
  STAMPC(29);
  if (wi < NC) {
    if (wi == 0) { poll_tag((gu64*)(ws + WS_F5) + (grp * 3 + g) * 8 + (lane & 7), tag); lds_arrive(mcnt + 7 + g, lane); }
    else lds_wait(mcnt + 7 + g, 1);
    lds_wait(mcnt + 4 + g, 4);
    if (g == 2) STAMP(26);
    const int oc = (g == 0 ? 0 : (g == 1 ? 4 * NC0 : 4 * (NC0 + NC1))) + (s & 3) * NC + wi, u = rpo * 36 + oc;
    const int row = rpo * 32 + (q & 1) * 16 + tok;
    const int off = ((grp * 64 + row) * D + oc * 16 + 8 * (q >> 1)) * 2;
    v4u p[8];
#pragma unroll
    for (int k = 0; k < 7; ++k) p[k] = ld16(rs, lane * 16, WS_PART + ((((s + 1 + k) & 7) * 32 + grp) * 72 + u) * 1024);
    const v4u hv = ld16(rs, WS_HR + off);
    p[7] = *(LAS v4u*)(own + wi * 1024 + lane * 16);
    const v4u xv = *(LAS v4u*)(lds + ML_A + row * ROWB + (((2 * oc + (q >> 1)) ^ ((row >> 1) & 7)) << 4));
    v4u o;
#pragma unroll
    for (int j = 0; j < 4; ++j) {
      f32x2 sum = f32x2{bflo(xv[j]), bfhi(xv[j])} + f32x2{bflo(hv[j]), bfhi(hv[j])};
#pragma unroll
      for (int k = 0; k < 8; ++k) sum += f32x2{bflo(p[k][j]), bfhi(p[k][j])};
      o[j] = cvtpk(sum[0], sum[1]);
    }
    *(GAS v4u*)((GAS unsigned char*)out + off) = o;
  }
}

__global__ __launch_bounds__(NT, 3) void k_all(const unsigned short* __restrict__ x, const unsigned short* __restrict__ Are, const unsigned short* __restrict__ Aim,
                                               const unsigned short* __restrict__ Bre, const unsigned short* __restrict__ Bim, const unsigned short* __restrict__ Cre,
                                               const unsigned short* __restrict__ Cim, const unsigned short* __restrict__ logdt, const unsigned short* __restrict__ dskip,
                                               const unsigned short* __restrict__ w1, const unsigned short* __restrict__ w2, const unsigned short* __restrict__ w3,
                                               const unsigned short* __restrict__ w4, unsigned char* __restrict__ ws, unsigned short* __restrict__ out) {
  extern __shared__ __attribute__((aligned(16))) unsigned char smem[];
  LAS unsigned char* lds = (LAS unsigned char*)smem;
  const int tid = threadIdx.x, lane = tid & 63, wv = __builtin_amdgcn_readfirstlane(tid >> 6);
  const int b = blockIdx.x, xcd = b & 7, jj = b >> 3;
  const rsrc_t rs = __builtin_amdgcn_make_buffer_rsrc((void*)ws, 0, WS_END, 0x00020000);
  STAMP(0);
  STAMPC(30);
  const bool is_conv = jj < 18, is_gemm = jj < 24;
  const int c0 = 4 * (18 * xcd + jj);
  if (tid < 16) ((LAS unsigned*)(lds + CV_MISC))[tid] = 0;
  asm volatile("s_waitcnt lgkmcnt(0)" ::: "memory");
  asm volatile("" ::"s"(x), "s"(Are), "s"(Aim), "s"(Bre), "s"(Bim), "s"(Cre), "s"(Cim), "s"(logdt), "s"(ws));
  const u64 epoch = __hip_atomic_load((gu64*)(ws + WS_CTRL), RLX);
#define GET_TAG() ((((u64)(unsigned)__builtin_amdgcn_readfirstlane((unsigned)(epoch >> 32)) << 32) | (unsigned)__builtin_amdgcn_readfirstlane((unsigned)epoch)) + 1)
  u64 tag;
  LAS unsigned* cnt = (LAS unsigned*)(lds + CV_MISC);
  LAS float* smx = (LAS float*)(lds + CV_MISC + 64);
  LAS float* kscs = (LAS float*)(lds + CV_MISC + 96);

  if (wv < 4) {
    if (wv < 3) {
      const int t8 = lane >> 3, sub = lane & 7;
      const GAS v4u* row = (const GAS v4u*)(x + (size_t)(b * 8 + t8) * D) + 24 * wv + sub;
      v4u a[3];
#pragma unroll
      for (int i = 0; i < 3; ++i) a[i] = row[8 * i];
      __builtin_amdgcn_sched_barrier(0);
      __builtin_amdgcn_s_barrier();
      tag = GET_TAG();
      float s = 0.f, ss = 0.f;
      const unsigned ones = 0x3F803F80u;
#pragma unroll
      for (int i = 0; i < 3; ++i) {
#pragma unroll
        for (int j = 0; j < 4; ++j) { s = dot2bf(a[i][j], ones, s); ss = dot2bf(a[i][j], a[i][j], ss); }
      }
#pragma unroll
      for (int o = 0; o < 1; ++o) { s = sum8(s); ss = sum8(ss); }
      STAMP(38);
      LAS f32x2* part = (LAS f32x2*)(lds + CV_MISC + 128);
      if (sub == 0) part[wv * 8 + t8] = f32x2{s, ss};
#pragma unroll
      for (int i = 0; i < 3; ++i) *(LAS v4u*)(lds + PR_X + t8 * PR_ROW + (24 * wv + 8 * i + sub) * 16) = a[i];
      lds_arrive_wait(cnt + 4, 3, lane);
      STAMP(39);
      {
        const f32x2 p0 = part[t8], p1 = part[8 + t8], p2 = part[16 + t8];
        float m, sd;
        ln_scalars(p0.x + p1.x + p2.x, p0.y + p1.y + p2.y, m, sd);
        const float r = __builtin_amdgcn_rcpf(sd), nmr = -m * r;
#pragma unroll
        for (int i = 0; i < 3; ++i) {
          v4u up;
#pragma unroll
          for (int j = 0; j < 4; ++j) {
            const f32x2 xv = {bflo(a[i][j]), bfhi(a[i][j])};
            const f32x2 uv = __builtin_elementwise_fma(xv, f32x2{r, r}, f32x2{nmr, nmr});
            up[j] = cvtpkh(uv[0], uv[1]);
          }
          *(LAS v4u*)(lds + PR_U + t8 * PR_ROW + (24 * wv + 8 * i + sub) * 16) = up;
        }
      }
      {
        const int g = lane >> 4, q = (lane & 15) >> 2, p = lane & 3;
        LAS unsigned char* pa = lds + ((p & 1) ? PR_X : PR_U) + q * PR_ROW + (192 * wv + 8 * g + 4 * (p >> 1)) * 2;
#pragma unroll
        for (int k = 0; k < 6; ++k) {
          v2u lo = tr_read(pa + k * 64), hi = tr_read(pa + k * 64 + 4 * PR_ROW);
          st16(rs, v4u{lo.x, lo.y, hi.x, hi.y}, lane * 16, WS_UX + b * 18432 + (6 * wv + k) * 1024);
        }
      }
      STAMP(40);
      DRAIN();
      STAMP(41);
      lds_arrive_wait(cnt + 4, 6, lane);
      if (tid < 8) __hip_atomic_store((gu64*)(ws + WS_F1) + tid * 256 + b, tag, RLX);
      STAMP(1);
    } else {
      __builtin_amdgcn_s_barrier();
      tag = GET_TAG();
    }
    if (is_conv) {
      poll_tag((gu64*)(ws + WS_F1 + (b & 7) * 2048) + 64 * wv + lane, tag);
      STAMP(3);
      if (wv == 3) lds_arrive_wait(cnt + 4, 6, 64);
#pragma unroll
      for (int i = 0; i < 8; ++i) {
        const int oct = 64 * wv + 8 * i + (lane >> 3);
        dma16(rs, lds + CV_U + (32 + 64 * wv + 8 * i) * 128, WS_UX + oct * 18432 + (c0 >> 2) * 128 + (((lane & 7) ^ ((oct >> 1) & 7)) << 4));
      }
      *(LAS v4u*)(lds + CV_U + tid * 16) = v4u{0, 0, 0, 0};
      STAMP(4);
      STAMPV(10);
    }
  } else if (is_conv) {
    const int cl = (wv - 4) >> 1, half = wv & 1, ch = c0 + cl, n = half * 64 + lane;
    const int gi = ch * NS + n;
    const int t2 = half * 64 + lane;
    const unsigned short rld = logdt[ch], rar = Are[gi], rai = Aim[gi], rbr = Bre[gi], rbi = Bim[gi], rcr = Cre[gi], rci = Cim[gi];
    v4u kid[3];
    unsigned kid2[3];
#pragma unroll
    for (int i = 0; i < 3; ++i) {
      const int p = t2 + 128 * i < 259 ? t2 + 128 * i : 258;
      kid[i] = *(const GAS v4u*)(kIdxTab.v + p * 8);
      kid2[i] = *(const GAS unsigned*)(kIdxTab.v + p * 8 + 8);
    }
    __builtin_amdgcn_sched_barrier(0);
    __builtin_amdgcn_s_barrier();
    tag = GET_TAG();
    {
      const float dt = bfr(expf(bf2f(rld)));
      const float zr = dt * bf2f(rar), zi = dt * bf2f(rai);
      const float dBr = dt * bf2f(rbr), dBi = dt * bf2f(rbi);
      cplx W = cmul(cplx{bf2f(rcr), bf2f(rci)}, cplx{dBr, dBi});
      float smax = fmaxf(fabsf(W.r), fabsf(W.i));
      smax = wave_max(smax);
      const float inv = smax > 0.f ? 1.0f / smax : 0.f;
      W.r *= inv; W.i *= inv;
      const float a2 = zr * 1.44269504089f, th = zi * 0.15915494309f;
      const cplx e1 = cexp_r(a2, th, 1.f), e8 = cexp_r(a2, th, 8.f), e128 = cexp_r(a2, th, 128.f);
      const cplx e256 = cexp_r(a2, th, 256.f), e512 = cexp_r(a2, th, 512.f), e1024 = cexp_r(a2, th, 1024.f);
      STAMPW(4, 5);
      STAMPV(15);
      LAS unsigned char* chan = lds + cl * CV_CH;
      LAS unsigned char* tab = chan + half * CV_TAB;
      const int nsw = lane >> 2, nlo = (lane & 3) * 4;
#define PUT(row, val) *(LAS unsigned*)(tab + (row) * 256 + ((nsw ^ ((row) & 15)) << 4) + nlo) = (val)
#define PUTA(row, z) PUT(row, cvtpkh((z).r, -(z).i))
#define PUTB(row, z) PUT(41 + (row), cvtpkh((z).r, (z).i))
      {
        cplx c[16];
        c[0] = cplx{1.f, 0.f}; c[1] = e8; c[2] = cmul(e8, e8); c[3] = cmul(c[2], e8); c[4] = cmul(c[2], c[2]);
        c[5] = cmul(c[4], c[1]); c[6] = cmul(c[4], c[2]); c[7] = cmul(c[4], c[3]); c[8] = cmul(c[4], c[4]);
#pragma unroll
        for (int j = 9; j < 16; ++j) c[j] = cmul(c[8], c[j - 8]);
#pragma unroll
        for (int j = 0; j < 16; ++j) PUTB(j, c[j]);
      }
      {
        const cplx e2 = cmul(e1, e1), e4 = cmul(e2, e2);
        cplx p[8];
        p[0] = W; p[1] = cmul(W, e1); p[2] = cmul(W, e2); p[3] = cmul(p[1], e2);
#pragma unroll
        for (int c = 4; c < 8; ++c) p[c] = cmul(p[c - 4], e4);
#pragma unroll
        for (int c = 0; c < 8; ++c) {
          const cplx qv = cmul(p[c], e128);
          PUTA(c, p[c]);
          PUTA(8 + c, qv);
          if ((c & 1) == 0) {
            PUTA(16 + (c >> 1), cmul(p[c], e256));
            PUTA(20 + (c >> 1), cmul(qv, e256));
          }
        }
        const cplx e384 = cmul(e256, e128);
        const cplx s0 = cmul(p[0], e512), s1 = cmul(p[4], e512);
        PUTA(24, s0); PUTA(25, s1);
        PUTA(26, cmul(s0, e128)); PUTA(27, cmul(s1, e128));
        PUTA(28, cmul(s0, e256)); PUTA(29, cmul(s1, e256));
        PUTA(30, cmul(s0, e384)); PUTA(31, cmul(s1, e384));
        const cplx t0 = cmul(W, e1024), t4 = cmul(t0, e512);
        PUTA(32, t0); PUTA(33, cmul(t0, e128)); PUTA(34, cmul(t0, e256)); PUTA(35, cmul(t0, e384));
        PUTA(36, t4); PUTA(37, cmul(t4, e128)); PUTA(38, cmul(t4, e256)); PUTA(39, cmul(t4, e384));
        PUTA(40, cmul(t0, e1024));
      }
#undef PUT
#undef PUTA
#undef PUTB
      STAMPW(4, 6);
      STAMPV(11);
      if (lane == 0) smx[wv - 4] = smax;
#define KG_P1 2
      __builtin_amdgcn_s_setprio(KG_P1);
      lds_arrive_wait(cnt + cl, 2, lane);
      STAMPV(12);
      const float s0_ = smx[2 * cl], s1_ = smx[2 * cl + 1];
      const float sm = fmaxf(s0_, s1_);
      const float ksc = sm * (182.f / 1024.f);
      const float kinv = sm > 0.f ? 1.0f / ksc : 0.f;
      LAS unsigned short* kc = (LAS unsigned short*)(lds + CV_KCT + cl * 1600);
      {
        const int r = lane & 15, h = lane >> 4;
        const float f0 = s0_ * kinv, f1 = s1_ * kinv;
#pragma unroll
        for (int tI = 0; tI < 2; ++tI) {
          if (tI == 1 && half == 1) break;
          const int t = half ? 1 : 2 * tI;
          f32x4 d0 = {0, 0, 0, 0}, d1 = {0, 0, 0, 0};
#pragma unroll
          for (int ks = 0; ks < 4; ++ks) {
            const int ob = (41 + r) * 256 + (((4 * ks + h) ^ ((41 + r) & 15)) << 4), oa = (16 * t + r) * 256 + (((4 * ks + h) ^ r) << 4);
            d0 = mfma_h(*(LAS v4u*)(chan + oa), *(LAS v4u*)(chan + ob), d0);
            d1 = mfma_h(*(LAS v4u*)(chan + CV_TAB + oa), *(LAS v4u*)(chan + CV_TAB + ob), d1);
          }
#pragma unroll
          for (int e = 0; e < 4; ++e) {
            const float v = __builtin_fmaf(d0[e], f0, d1[e] * f1);
            kc[(16 * t + 4 * h + e) * 16 + r] = (unsigned short)(cvtpkh(v, v) & 0xFFFFu);
          }
        }
        if (half == 1 && lane == 0) { kc[768] = 0; kscs[cl] = ksc; }
      }
      STAMPV(13);
      __builtin_amdgcn_s_setprio(0);
      lds_arrive_wait(cnt + cl, 4, lane);
      STAMPV(14);
#pragma unroll
      for (int i = 0; i < 3; ++i) {
        const int z = t2 + 128 * i;
        unsigned d[5];
#pragma unroll
        for (int j = 0; j < 4; ++j) d[j] = (unsigned)kc[kid[i][j] & 0xFFFFu] | ((unsigned)kc[kid[i][j] >> 16] << 16);
        d[4] = (unsigned)kc[kid2[i] & 0xFFFFu];
        if (z < 259) {
          LAS unsigned char* base = chan + z * 16;
          *(LAS v4u*)(base) = v4u{d[0], d[1], d[2], d[3]};
          *(LAS v4u*)(base + CV_KC) = v4u{__builtin_amdgcn_alignbit(d[1], d[0], 16), __builtin_amdgcn_alignbit(d[2], d[1], 16), __builtin_amdgcn_alignbit(d[3], d[2], 16), __builtin_amdgcn_alignbit(d[4], d[3], 16)};
        }
      }
      STAMPW(4, 7);
      STAMPV(10);
    }
  } else {
    __builtin_amdgcn_s_barrier();
    tag = GET_TAG();
  }
  if (!is_conv) {
    const int idx = (jj - 18) * 8 + xcd;
#define PT_DELAY 3
    for (int i = 0; i < PT_DELAY; ++i) __builtin_amdgcn_s_sleep(32);
    pretile_unit(lds, rs, ws, idx, tag, w1, w2, w3, w4);
    if (idx >= 48 && idx < 98) pretile_unit(lds, rs, ws, 112 + idx - 48, tag, w1, w2, w3, w4);
    STAMP(2);
  }
  if (is_conv) {
    const float dsk = bf2f(dskip[c0 + (wv & 3)]);
    __syncthreads();
    STAMP(8);
    STAMPC(31);
    const int cl = wv & 3, kh = (wv >> 2) & 1;
    f32x4 acc[8], base[4];
    float ksc = 0.f;
    if (wv < 8) {
#pragma unroll
      for (int t = 0; t < 8; ++t) acc[t] = f32x4{0, 0, 0, 0};
      const int bb = lane & 15, h = lane >> 4;
      const int cpy = (bb & 1) ^ 1;
      LAS unsigned char* abase = lds + cl * CV_CH + cpy * CV_KC + (2047 - 16 - bb + 8 * h - cpy) * 2 - 64 * 4 * kh;
      LAS unsigned char* bbase = lds + CV_U + (32 + 2 * (bb - 1) + h) * 128;
      v4u A[8], B[8];
#define LDA(i, rho) do { LAS unsigned* pa_ = (LAS unsigned*)(abase - 64 * ((rho) + 8 * (i))); A[i] = v4u{pa_[0], pa_[1], pa_[2], pa_[3]}; } while (0)
#define LDB(m, rho) B[m] = *(LAS v4u*)(bbase - 512 * (4 * kh + (rho)) + ((cl ^ ((bb - 1 + (h >> 1) - 2 * (4 * kh + (rho))) & 7)) << 4) + 4096 * (m))
#pragma unroll
      for (int i = 0; i < 8; ++i) { LDB(i, 0); LDA(i, 0); }
#pragma unroll
      for (int rho = 0; rho < 4; ++rho) {
#pragma unroll
        for (int i = 0; i < 8; ++i) {
#pragma unroll
          for (int m = 7 - i; m >= 0; --m) acc[i + m] = mfma_h(A[i], B[m], acc[i + m]);
          if (rho < 3) { LDA(i, rho + 1); LDB(7 - i, rho + 1); }
          __builtin_amdgcn_sched_barrier(0);
        }
      }
#undef LDA
#undef LDB
      LAS unsigned char* xo = lds + cl * CV_CH + CV_XCH + kh * 4096 + lane * 16;
#pragma unroll
      for (int t = 0; t < 4; ++t) {
        f32x4 lo = acc[t], hi = acc[4 + t];
        asm volatile("" : "+v"(lo), "+v"(hi));
        *(LAS f32x4*)(xo + t * 1024) = kh ? lo : hi;
        acc[t] = kh ? hi : lo;
      }
      const int a = lane & 15, q = lane >> 4;
#pragma unroll
      for (int t = 0; t < 4; ++t) {
        LAS unsigned char* po = lds + CV_U + (32 + 32 * (4 * kh + t) + 2 * a + (q >> 1)) * 128 + (q & 1) * 8;
        const v2u uu = *(LAS v2u*)(po + ((cl ^ (a & 7)) << 4));
        const v2u xx = *(LAS v2u*)(po + (((4 + cl) ^ (a & 7)) << 4));
#pragma unroll
        for (int e = 0; e < 2; ++e) {
          const unsigned uw = e ? uu.y : uu.x, xw = e ? xx.y : xx.x;
          const h16x2 u2 = __builtin_bit_cast(h16x2, uw);
          const f32x2 bv = __builtin_elementwise_fma(f32x2{(float)u2[0], (float)u2[1]}, f32x2{dsk, dsk}, f32x2{bflo(xw), bfhi(xw)});
          base[t][2 * e] = bv[0]; base[t][2 * e + 1] = bv[1];
        }
      }
      ksc = kscs[cl];
#pragma unroll
      for (int t = 0; t < 4; ++t) base[t] += acc[t] * ksc;
    }
    STAMP(9);
    STAMPV(0);
    __syncthreads();
    STAMP(32);
    if (wv < 8) {
      const int ch = c0 + cl;
      const int a = lane & 15, q = lane >> 4;
      LAS unsigned char* xi = lds + cl * CV_CH + CV_XCH + (kh ^ 1) * 4096 + lane * 16;
      f32x4 o[4];
#pragma unroll
      for (int t = 0; t < 4; ++t) o[t] = *(LAS f32x4*)(xi + t * 1024);
      unsigned pk[4][2];
#pragma unroll
      for (int t = 0; t < 4; ++t) {
        const f32x4 hv = base[t] + o[t] * ksc;
        pk[t][0] = cvtpk(hv[0], hv[1]);
        pk[t][1] = cvtpk(hv[2], hv[3]);
      }
#pragma unroll
      for (int t = 0; t < 4; t += 2) {
        auto s0 = __builtin_amdgcn_permlane16_swap(pk[t][0], pk[t + 1][0], false, false);
        auto s1 = __builtin_amdgcn_permlane16_swap(pk[t][1], pk[t + 1][1], false, false);
        const int tok = 256 * (4 * kh + t + (q & 1)) + 16 * a + 8 * (q >> 1);
        st16(rs, v4u{s0[0], s1[0], s0[1], s1[1]}, WS_HT + (ch * L + tok) * 2);
      }
      STAMP(33);
      STAMPV(1);
      if (drain_arrive_last(cnt + 8, 8, lane)) { if (lane < 8) __hip_atomic_store((gu64*)(ws + WS_F2) + lane * 256 + (18 * xcd + jj), tag, RLX); STAMPL(43); }
    }
    STAMP(10);
  }

  const int tt = 4 * xcd + jj / 6, sl = jj % 6;
  if (is_gemm) {
    const int cl = wv % 6, kh = wv / 6, ct = 6 * sl + cl;
    const int tok = lane & 15, q = lane >> 4;
    if (wv == 0) {
      const int u0 = (96 * sl) >> 6;
      poll_tag((gu64*)(ws + WS_FPT + (b & 3) * 2048) + (lane & 1) + u0 + ((lane & 2) ? 9 : 0), tag);
    }
    __syncthreads();
    v4u bw[9];
    if (wv != 0) {
#pragma unroll
      for (int ks = 0; ks < 9; ++ks) bw[ks] = ld16(rs, lane * 16, WS_W1T + (ct * 18 + 9 * kh + ks) * 1024);
    }
    int aoff[6];
#pragma unroll
    for (int i = 0; i < 6; ++i) {
      const int p = tid + NT * i, c = p >> 3, pp = p & 7;
      const int f = ((c >> 1) & 1) | (((c >> 3) & 1) << 1);
      aoff[i] = WS_HT + (c * L + tt * 64 + ((((pp >> 1) ^ f) << 1) | (pp & 1)) * 8) * 2;
      asm volatile("" : "+v"(aoff[i]));
    }
    if (wv == 0) {
      const int fo = WS_F2 + (b & 7) * 2048 + (lane < 36 ? lane : 0) * 32;
      const unsigned tl = (unsigned)tag, th = (unsigned)(tag >> 32);
      for (unsigned spins = 0; spins < SPIN_CAP; ++spins) {
        asm volatile("" ::: "memory");
        const v4u f0 = ld16(rs, fo), f1 = ld16(rs, fo + 16);
        if (__all(f0.x == tl && f0.y == th && f0.z == tl && f0.w == th && f1.x == tl && f1.y == th && f1.z == tl && f1.w == th)) break;
        if (PSLEEP) __builtin_amdgcn_s_sleep(PSLEEP);
      }
      ORDER();
#pragma unroll
      for (int ks = 0; ks < 9; ++ks) bw[ks] = ld16(rs, lane * 16, WS_W1T + (ct * 18 + 9 * kh + ks) * 1024);
    }
    const f32x4 cs = __builtin_bit_cast(f32x4, ld16(rs, WS_CS1 + (ct * 16 + 4 * q) * 4));
    __syncthreads();
    STAMP(11);
#pragma unroll
    for (int i = 0; i < 6; ++i) dma16(rs, lds + G_A + (wv * 64 + NT * i) * 16, aoff[i]);
    __syncthreads();
    v4u bw2[9];
    v4u hpiece;
    STAMP(12);
    STAMPV(2);
    f32x4 acc[4];
#pragma unroll
    for (int r = 0; r < 4; ++r) acc[r] = f32x4{0, 0, 0, 0};
    {
      const int pq = (lane & 15) >> 2, pp = lane & 3;
      const int f = ((pq >> 1) & 1) | ((q & 1) << 1);
      const unsigned ones = 0x3F803F80u;
      LAS unsigned char* pa0 = lds + G_A + (8 * q + pq) * 128 + pp * 8;
      LAS unsigned char* par[4];
#pragma unroll
      for (int r = 0; r < 4; ++r) par[r] = pa0 + ((r ^ f) << 5);
      v4u af[2][4];
#pragma unroll
      for (int r = 0; r < 4; ++r) { v2u lo = tr_read(par[r] + 9 * kh * 4096), hi = tr_read(par[r] + 9 * kh * 4096 + 512); af[0][r] = v4u{lo.x, lo.y, hi.x, hi.y}; }
#pragma unroll
      for (int ks = 0; ks < 9; ++ks) {
        if (ks < 8) {
#pragma unroll
          for (int r = 0; r < 4; ++r) {
            v2u lo = tr_read(par[r] + (9 * kh + ks + 1) * 4096), hi = tr_read(par[r] + (9 * kh + ks + 1) * 4096 + 512);
            af[(ks + 1) & 1][r] = v4u{lo.x, lo.y, hi.x, hi.y};
          }
        }
        __builtin_amdgcn_sched_barrier(0);
#pragma unroll
        for (int r = 0; r < 4; ++r) acc[r] = mfma_bf(bw[ks], af[ks & 1][r], acc[r]);
        __builtin_amdgcn_sched_barrier(0);
      }
    }
#pragma unroll
    for (int ks = 0; ks < 9; ++ks) bw2[ks] = ld16(rs, lane * 16, WS_W2T + (ct * 18 + 9 * kh + ks) * 1024);
    hpiece = ld16(rs, WS_HT + ((ct * 16 + (lane >> 2)) * L + tt * 64 + kh * 32 + (lane & 3) * 8) * 2);
    __builtin_amdgcn_sched_barrier(0);
    STAMP(35);
    STAMPV(3);
    {
      LAS unsigned char* xo = lds + G_X + wv * 2048 + lane * 16;
#pragma unroll
      for (int r = 0; r < 2; ++r) {
        f32x4 lo = acc[r], hi = acc[2 + r];
        asm volatile("" : "+v"(lo), "+v"(hi));
        *(LAS f32x4*)(xo + r * 1024) = kh ? lo : hi;
        acc[r] = kh ? hi : lo;
      }
    }
    if (wv < 4) {
      const int pq = (lane & 15) >> 2, pp = lane & 3;
      const int f = ((pq >> 1) & 1) | ((q & 1) << 1);
      LAS unsigned char* ps0 = lds + G_A + (8 * q + pq) * 128 + pp * 8 + ((wv ^ f) << 5);
      f32x4 dsum = {0, 0, 0, 0}, dsq = {0, 0, 0, 0};
      const v4u onesv = {0x3F803F80u, 0x3F803F80u, 0x3F803F80u, 0x3F803F80u};
#pragma unroll
      for (int hf = 0; hf < 2; ++hf) {
        v4u as[9];
#pragma unroll
        for (int i = 0; i < 9; ++i) { v2u lo = tr_read(ps0 + (9 * hf + i) * 4096), hi = tr_read(ps0 + (9 * hf + i) * 4096 + 512); as[i] = v4u{lo.x, lo.y, hi.x, hi.y}; }
#pragma unroll
        for (int i = 0; i < 9; ++i) { dsum = mfma_bf(onesv, as[i], dsum); dsq = mfma_bf(as[i], as[i], dsq); }
      }
      const int e = tok & 3;
      const float sq = e == 0 ? dsq[0] : (e == 1 ? dsq[1] : (e == 2 ? dsq[2] : dsq[3]));
      const float s_ = dsum[0], ss = sq;
      float m1, s1;
      ln_scalars(s_, ss, m1, s1);
      const float mean = s_ * (1.0f / D);
      float var = __builtin_fmaf(-mean, mean, ss * (1.0f / D));
      var = var < 0.f ? 0.f : var;
      const float r1 = __builtin_amdgcn_rcpf(s1);
      const float m2 = bfr((mean - m1) * r1);
      const float s2 = bfr(__builtin_amdgcn_sqrtf(bfr(bfr(var * r1 * r1) + 1e-5f)));
      if (q == (tok >> 2)) *(LAS f32x4*)(lds + G_ST + 2048 + (wv * 16 + tok) * 16) = f32x4{__builtin_fmaf(m2, s1, m1), r1 * __builtin_amdgcn_rcpf(s2), m1, r1};
    }
    __syncthreads();
    STAMP(13);
    STAMPV(4);
    f32x2 st1[2];
    {
      LAS unsigned char* xi = lds + G_X + (kh ? wv - 6 : wv + 6) * 2048 + lane * 16;
      f32x4 o[2], sc[2];
#pragma unroll
      for (int r = 0; r < 2; ++r) { o[r] = *(LAS f32x4*)(xi + r * 1024); sc[r] = *(LAS f32x4*)(lds + G_ST + 2048 + ((2 * kh + r) * 16 + tok) * 16); }
      __builtin_amdgcn_sched_barrier(0);
      unsigned pk[2][2];
#pragma unroll
      for (int r = 0; r < 2; ++r) {
        st1[r] = f32x2{sc[r][2], sc[r][3]};
        const f32x4 v = (acc[r] + o[r] - cs * sc[r][0]) * sc[r][1];
        const f32x2 ga = gelu2(f32x2{v[0], v[1]}), gb = gelu2(f32x2{v[2], v[3]});
        pk[r][0] = cvtpk(ga[0], ga[1]);
        pk[r][1] = cvtpk(gb[0], gb[1]);
      }
#pragma unroll
      for (int r = 0; r < 2; ++r) *(LAS v2u*)(lds + G_O + ((2 * kh + r) * 16 + tok) * G_OROW + cl * 32 + q * 8) = v2u{pk[r][0], pk[r][1]};
    }
    const int orow = tid / 12, ocol = tid % 12;
    __syncthreads();
    st16(rs, *(LAS v4u*)(lds + G_O + orow * G_OROW + ocol * 16), WS_H1 + ((tt * 64 + orow) * D + sl * 96 + ocol * 8) * 2);
    STAMP(36);
    STAMPV(5);
    if (drain_arrive_last(cnt + 9, 12, lane)) { if (lane == 0) __hip_atomic_store((gu64*)(ws + WS_F3) + tt * 8 + sl, tag, RLX); STAMPL(44); }
    STAMP(14);

    {
      const int c = lane >> 2, pc = lane & 3;
      *(LAS v4u*)(lds + G_H + wv * 1024 + c * 64 + ((((pc >> 1) ^ ((c >> 3) & 1)) << 1 | (pc & 1)) << 4)) = hpiece;
    }
#pragma unroll
    for (int i = 0; i < 6; ++i) {
      const int p = tid + NT * i, r = p / 72, c = (p % 72) ^ ((r >> 1) & 7);
      const int cg = c / 18;
      const int sh = cg == 0 ? -1 : (cg == 1 ? 1 : (cg == 2 ? -2 : 2));
      const int t = tt * 64 + r + sh;
      aoff[i] = (t >= 0 && t < L) ? WS_H1 + (t * D + c * 8) * 2 : WS_END;
      asm volatile("" : "+v"(aoff[i]));
    }
    if (wv == 0) {
      int t = tt - 1 + (lane < 18 ? lane / 6 : 1);
      t = t < 0 ? 0 : (t > 31 ? 31 : t);
      poll_tag((gu64*)(ws + WS_F3) + t * 8 + (lane < 18 ? lane % 6 : 0), tag);
    }
    __syncthreads();
    STAMP(15);
#pragma unroll
    for (int i = 0; i < 6; ++i) dma16(rs, lds + G_A + (wv * 64 + NT * i) * 16, aoff[i]);
    __syncthreads();
    STAMP(16);
#pragma unroll
    for (int r = 0; r < 4; ++r) acc[r] = f32x4{0, 0, 0, 0};
    {
      const int sw = (tok >> 1) & 7;
      LAS unsigned char* pa = lds + G_A + tok * ROWB;
      v4u af[2][4];
#pragma unroll
      for (int r = 0; r < 4; ++r) af[0][r] = *(LAS v4u*)(pa + r * 16 * ROWB + (((4 * 9 * kh + q) ^ sw) << 4));
#pragma unroll
      for (int ks = 0; ks < 9; ++ks) {
        if (ks < 8) {
#pragma unroll
          for (int r = 0; r < 4; ++r) af[(ks + 1) & 1][r] = *(LAS v4u*)(pa + r * 16 * ROWB + (((4 * (9 * kh + ks + 1) + q) ^ sw) << 4));
        }
        __builtin_amdgcn_sched_barrier(0);
#pragma unroll
        for (int r = 0; r < 4; ++r) acc[r] = mfma_bf(bw2[ks], af[ks & 1][r], acc[r]);
        __builtin_amdgcn_sched_barrier(0);
      }
    }
    v2u hh[2];
    {
      LAS unsigned char* xo = lds + G_X + wv * 2048 + lane * 16;
#pragma unroll
      for (int r = 0; r < 2; ++r) {
        f32x4 lo = acc[r], hi = acc[2 + r];
        asm volatile("" : "+v"(lo), "+v"(hi));
        *(LAS f32x4*)(xo + r * 1024) = kh ? lo : hi;
        acc[r] = kh ? hi : lo;
      }
#pragma unroll
      for (int r = 0; r < 2; ++r) {
        const int c = 4 * q + ((lane & 15) >> 2);
        hh[r] = tr_read(lds + G_H + wv * 1024 + c * 64 + ((r ^ ((c >> 3) & 1)) << 5) + (lane & 3) * 8);
        const float m1 = st1[r][0], r1 = st1[r][1];
        const f32x4 hv = {bflo(hh[r].x), bfhi(hh[r].x), bflo(hh[r].y), bfhi(hh[r].y)};
        acc[r] += (hv - m1) * r1;
        *(LAS v2u*)(lds + G_O + 64 * G_OROW + ((2 * kh + r) * 16 + tok) * G_OROW + cl * 32 + q * 8) = hh[r];
      }
    }
    __syncthreads();
    STAMP(17);
    {
      LAS unsigned char* xi = lds + G_X + (kh ? wv - 6 : wv + 6) * 2048 + lane * 16;
      f32x4 o[2];
#pragma unroll
      for (int r = 0; r < 2; ++r) o[r] = *(LAS f32x4*)(xi + r * 1024);
      unsigned pk[2][2];
#pragma unroll
      for (int r = 0; r < 2; ++r) {
        const f32x4 v = acc[r] + o[r];
        pk[r][0] = cvtpk(v[0], v[1]);
        pk[r][1] = cvtpk(v[2], v[3]);
      }
#pragma unroll
      for (int r = 0; r < 2; ++r) *(LAS v2u*)(lds + G_O + ((2 * kh + r) * 16 + tok) * G_OROW + cl * 32 + q * 8) = v2u{pk[r][0], pk[r][1]};
    }
    __syncthreads();
    {
      const int off = ((tt * 64 + orow) * D + sl * 96 + ocol * 8) * 2;
      st16(rs, *(LAS v4u*)(lds + G_O + orow * G_OROW + ocol * 16), WS_X1 + off);
      st16(rs, *(LAS v4u*)(lds + G_O + 64 * G_OROW + orow * G_OROW + ocol * 16), WS_HR + off);
    }
    if (drain_arrive_last(cnt + 10, 12, lane)) { if (lane == 0) __hip_atomic_store((gu64*)(ws + WS_F4) + tt * 8 + sl, tag, RLX); STAMPL(45); }
    STAMP(18);
  }

  const int grp = 2 * (jj >> 1) + (xcd >> 2), s = 2 * (xcd & 3) + (jj & 1);
  {
    constexpr int DEPTH = 3;
    if (wv == 0) {
      poll_tag((gu64*)(ws + WS_FPT + (b & 3) * 2048) + (lane < 9 ? 18 + 9 * s + lane : (lane < 18 ? 90 + 9 * s + lane - 9 : 18 + 9 * s)), tag);
    }
    __syncthreads();
    const int o3 = WS_W3T + (36 * s + 3 * wv) * 18 * 1024;
    LAS unsigned* mcnt = (LAS unsigned*)(lds + ML_ST + 512);
    v4u bw[DEPTH][3];
    const v4u csl = ld16(rs, WS_CS3 + 36 * 16 * 4 * s + (tid < 144 ? tid : 0) * 16);
    if (wv != 0) {
#pragma unroll
      for (int d = 0; d < DEPTH; ++d)
#pragma unroll
        for (int c = 0; c < 3; ++c) bw[d][c] = ldw(rs, lane * 16, o3 + (c * 18 + d) * 1024);
    }
    if (tid < 144) *(LAS v4u*)(lds + ML_CS + tid * 16) = csl;
    if (tid >= 704 && tid < 714) mcnt[tid - 704] = 0;
    int xoff[6];
#pragma unroll
    for (int i = 0; i < 6; ++i) {
      const int p = tid + NT * i, r = p / 72, c = (p % 72) ^ ((r >> 1) & 7);
      xoff[i] = WS_X1 + grp * 64 * ROWB + r * ROWB + c * 16;
      asm volatile("" : "+v"(xoff[i]));
    }
    if (wv == 0) {
      poll_tag((gu64*)(ws + WS_F4) + grp * 8 + (lane < 6 ? lane : 0), tag);
#pragma unroll
      for (int d = 0; d < DEPTH; ++d)
#pragma unroll
        for (int c = 0; c < 3; ++c) bw[d][c] = ldw(rs, lane * 16, o3 + (c * 18 + d) * 1024);
    }
    __syncthreads();
    STAMP(19);
#pragma unroll
    for (int i = 0; i < 6; ++i) dma16(rs, lds + ML_A + (wv * 64 + NT * i) * 16, xoff[i]);
    __syncthreads();
    STAMP(20);
    STAMPV(6);
    STAMPC(28);
    const int tok = lane & 15, q = lane >> 4;
    const int sw = (tok >> 1) & 7;
    const int wg = wv >> 2, wi = wv & 3;
    LAS unsigned char* abase = lds + ML_A + tok * ROWB;
    if (wv < 4) {
      f32x4 dsum = {0, 0, 0, 0}, dsq = {0, 0, 0, 0};
      const v4u onesv = {0x3F803F80u, 0x3F803F80u, 0x3F803F80u, 0x3F803F80u};
      LAS unsigned char* pst = abase + wv * 16 * ROWB;
      v4u as[18];
#pragma unroll
      for (int ks = 0; ks < 18; ++ks) as[ks] = *(LAS v4u*)(pst + (((4 * ks + q) ^ sw) << 4));
#pragma unroll
      for (int ks = 0; ks < 18; ++ks) { dsum = mfma_bf(onesv, as[ks], dsum); dsq = mfma_bf(as[ks], as[ks], dsq); }
      const int e = tok & 3;
      const float sq = e == 0 ? dsq[0] : (e == 1 ? dsq[1] : (e == 2 ? dsq[2] : dsq[3]));
      float m, sd;
      ln_scalars(dsum[0], sq, m, sd);
      if (q == (tok >> 2)) *(LAS f32x2*)(lds + ML_ST + (wv * 16 + tok) * 8) = f32x2{m, __builtin_amdgcn_rcpf(sd)};
      lds_arrive(mcnt, lane);
    }
    LAS unsigned char* H = lds + ML_H;
    {
      f32x4 acc[4][3];
#pragma unroll
      for (int r = 0; r < 4; ++r)
#pragma unroll
        for (int c = 0; c < 3; ++c) acc[r][c] = f32x4{0, 0, 0, 0};
#define PRIO_P1 2
      __builtin_amdgcn_s_setprio(PRIO_P1);
      v4u af[2][4];
#pragma unroll
      for (int r = 0; r < 4; ++r) af[0][r] = *(LAS v4u*)(abase + r * 16 * ROWB + ((q ^ sw) << 4));
#pragma unroll
      for (int ks = 0; ks < 18; ++ks) {
        if (ks < 17) {
#pragma unroll
          for (int r = 0; r < 4; ++r) af[(ks + 1) & 1][r] = *(LAS v4u*)(abase + r * 16 * ROWB + (((4 * (ks + 1) + q) ^ sw) << 4));
        }
        __builtin_amdgcn_sched_barrier(0);
#pragma unroll
        for (int c = 0; c < 3; ++c) {
#pragma unroll
          for (int r = 0; r < 4; ++r) acc[r][c] = mfma_bf(bw[ks % DEPTH][c], af[ks & 1][r], acc[r][c]);
          const int nk = ks + DEPTH;
          if (nk < 18) bw[ks % DEPTH][c] = ldw(rs, lane * 16, o3 + (c * 18 + nk) * 1024);
        }
        __builtin_amdgcn_sched_barrier(0);
      }
#define PRIO_GELU 3
#define PRIO_P2 0
      __builtin_amdgcn_s_setprio(PRIO_GELU);
      STAMP(21);
      STAMPV(7);
      lds_wait(mcnt, 4);
      STAMP(22);
      f32x2 st[4];
#pragma unroll
      for (int r = 0; r < 4; ++r) st[r] = *(LAS f32x2*)(lds + ML_ST + (r * 16 + tok) * 8);
#pragma unroll
      for (int c = 0; c < 3; ++c) {
        const f32x4 cs = *(LAS f32x4*)(lds + ML_CS + ((3 * wv + c) * 16 + 4 * q) * 4);
#pragma unroll
        for (int r = 0; r < 4; ++r) {
          const f32x2 nm = {-st[r].x, -st[r].x}, rr = {st[r].y, st[r].y};
          const f32x2 ga = gelu2(__builtin_elementwise_fma(nm, f32x2{cs[0], cs[1]}, f32x2{acc[r][c][0], acc[r][c][1]}) * rr);
          const f32x2 gb = gelu2(__builtin_elementwise_fma(nm, f32x2{cs[2], cs[3]}, f32x2{acc[r][c][2], acc[r][c][3]}) * rr);
          v2u o = {cvtpk(ga[0], ga[1]), cvtpk(gb[0], gb[1])};
          const int chunk = 2 * (3 * wv + c) + (q >> 1);
          *(LAS v2u*)(H + (r * 16 + tok) * ROWB + ((chunk ^ sw) << 4) + (q & 1) * 8) = o;
        }
      }
      lds_arrive(mcnt + 1 + wg, lane);
#define PP0 0
#define PP1 1
#define PP2 2
      if (wg == 0) __builtin_amdgcn_s_setprio(PP0); else if (wg == 1) __builtin_amdgcn_s_setprio(PP1); else __builtin_amdgcn_s_setprio(PP2);
      STAMP(23);
      STAMPV(8);
    }
    const int rpo = s >> 2;
    if (wg == 0) mlp_second<NC0>(lds, rs, ws, out, tag, grp, s, NC0 * wi, wi, 0, mcnt, lane, tok, q, sw);
    else if (wg == 1) mlp_second<NC1>(lds, rs, ws, out, tag, grp, s, 4 * NC0 + NC1 * wi, wi, 1, mcnt, lane, tok, q, sw);
    else mlp_second<NC2>(lds, rs, ws, out, tag, grp, s, 4 * (NC0 + NC1) + NC2 * wi, wi, 2, mcnt, lane, tok, q, sw);
    (void)rpo;
  }
  if (b == 0 && tid == 0) __hip_atomic_store((gu64*)(ws + WS_CTRL), tag, RLX);
  STAMP(27);
}

extern "C" void kernel_launch(void* const* d_in, const int* in_sizes, int n_in, void* d_out, int out_size, void* d_ws, size_t ws_size,
                              hipStream_t stream) {
  (void)hipFuncSetAttribute((const void*)k_all, hipFuncAttributeMaxDynamicSharedMemorySize, LDS_BYTES);
  const unsigned short* const* in = (const unsigned short* const*)d_in;
  k_all<<<256, NT, LDS_BYTES, stream>>>(in[0], in[1], in[2], in[3], in[4], in[5], in[6], in[7], in[8], in[10], in[11], in[12], in[13], (unsigned char*)d_ws,
                                        (unsigned short*)d_out);
}
```

```cpp
#include <hip/hip_runtime.h>
#include <stdint.h>

typedef unsigned long long u64;
typedef unsigned v4u __attribute__((ext_vector_type(4)));
typedef unsigned v2u __attribute__((ext_vector_type(2)));
typedef float f32x4 __attribute__((ext_vector_type(4)));
typedef float f32x2 __attribute__((ext_vector_type(2)));
typedef short s16x8 __attribute__((ext_vector_type(8)));
typedef _Float16 h16x8 __attribute__((ext_vector_type(8)));
typedef _Float16 h16x2 __attribute__((ext_vector_type(2)));
typedef __bf16 bf16x2_t __attribute__((ext_vector_type(2)));
#define GAS __attribute__((address_space(1)))
#define LAS __attribute__((address_space(3)))
#define DI __device__ __forceinline__
#define RLX __ATOMIC_RELAXED, __HIP_MEMORY_SCOPE_AGENT
typedef GAS u64 gu64;
typedef __amdgpu_buffer_rsrc_t rsrc_t;

constexpr int L = 2048, D = 576, NS = 128, F = 4608;
constexpr int NT = 768;

constexpr int WS_CTRL = 0;
constexpr int WS_F1 = 4096;
constexpr int WS_FPT = WS_F1 + 8 * 2048;
constexpr int WS_F2 = WS_FPT + 4 * 2048;
constexpr int WS_F3 = WS_F2 + 8 * 2048;
constexpr int WS_F4 = WS_F3 + 32 * 64;
constexpr int WS_F5 = WS_F4 + 32 * 64;
constexpr int WS_W1T = 131072;
constexpr int WS_W2T = WS_W1T + 36 * 18 * 1024;
constexpr int WS_W3T = WS_W2T + 36 * 18 * 1024;
constexpr int WS_W4T = WS_W3T + 288 * 18 * 1024;
constexpr int WS_CS1 = WS_W4T + 36 * 144 * 1024;
constexpr int WS_CS3 = WS_CS1 + 4096;
constexpr int WS_HT = WS_CS3 + 4608 * 4;
constexpr int WS_H1 = WS_HT + D * L * 2;
constexpr int WS_X1 = WS_H1 + D * L * 2;
constexpr int WS_HR = WS_X1 + D * L * 2;
constexpr int WS_PART = WS_HR + D * L * 2;
constexpr int WS_UX = WS_PART + 8 * D * L * 2;
constexpr int WS_END = WS_UX + 256 * 144 * 128;
static_assert(WS_F5 + 32 * 192 <= WS_W1T, "control block");


DI float bf2f(unsigned short b) { return __builtin_bit_cast(float, (unsigned)b << 16); }
DI float asf(unsigned u) { return __builtin_bit_cast(float, u); }
DI unsigned asu(float f) { return __builtin_bit_cast(unsigned, f); }
DI float bflo(unsigned u) { return __builtin_bit_cast(float, u << 16); }
DI float bfhi(unsigned u) { return __builtin_bit_cast(float, u & 0xFFFF0000u); }
DI unsigned cvtpk(float lo, float hi) {
  f32x2 v = {lo, hi};
  bf16x2_t b = __builtin_convertvector(v, bf16x2_t);
  return __builtin_bit_cast(unsigned, b);
}
DI float bfr(float x) { return bflo(cvtpk(x, x)); }
DI unsigned cvtpkh(float lo, float hi) {
  f32x2 v = {lo, hi};
  h16x2 b = __builtin_convertvector(v, h16x2);
  return __builtin_bit_cast(unsigned, b);
}
DI f32x4 mfma_bf(v4u a, v4u b, f32x4 c) {
  return __builtin_amdgcn_mfma_f32_16x16x32_bf16(__builtin_bit_cast(s16x8, a), __builtin_bit_cast(s16x8, b), c, 0, 0, 0);
}
DI f32x4 mfma_h(v4u a, v4u b, f32x4 c) {
  return __builtin_amdgcn_mfma_f32_16x16x32_f16(__builtin_bit_cast(h16x8, a), __builtin_bit_cast(h16x8, b), c, 0, 0, 0);
}
DI float dot2bf(unsigned a, unsigned b, float acc) {
  return __builtin_amdgcn_fdot2_f32_bf16(__builtin_bit_cast(bf16x2_t, a), __builtin_bit_cast(bf16x2_t, b), acc, false);
}
DI v2u tr_read(LAS unsigned char* p) {
  typedef short v4i16_t __attribute__((ext_vector_type(4)));
  return __builtin_bit_cast(v2u, __builtin_amdgcn_ds_read_tr16_b64_v4i16((LAS v4i16_t*)p));
}
template <int CTRL> DI float dppf(float v) { return asf((unsigned)__builtin_amdgcn_update_dpp(0, (int)asu(v), CTRL, 0xF, 0xF, true)); }
DI float sum8(float v) { v += dppf<0xB1>(v); v += dppf<0x4E>(v); v += dppf<0x141>(v); return v; }
DI float sum_rows(float v) {
  auto a = __builtin_amdgcn_permlane16_swap(asu(v), asu(v), false, false);
  v = asf(a[0]) + asf(a[1]);
  auto c = __builtin_amdgcn_permlane32_swap(asu(v), asu(v), false, false);
  return asf(c[0]) + asf(c[1]);
}
DI float wave_max(float m) {
  m = fmaxf(m, dppf<0xB1>(m)); m = fmaxf(m, dppf<0x4E>(m)); m = fmaxf(m, dppf<0x141>(m)); m = fmaxf(m, dppf<0x140>(m));
  m = fmaxf(m, asf((unsigned)__builtin_amdgcn_update_dpp((int)asu(m), (int)asu(m), 0x142, 0xA, 0xF, false)));
  m = fmaxf(m, asf((unsigned)__builtin_amdgcn_update_dpp((int)asu(m), (int)asu(m), 0x143, 0xC, 0xF, false)));
  return asf((unsigned)__builtin_amdgcn_readlane((int)asu(m), 63));
}
DI f32x2 gelu2(f32x2 x) {
  const f32x2 c0 = {-2.3022081986f, -2.3022081986f}, c1 = {-0.10294324f, -0.10294324f}, one = {1.0f, 1.0f};
  const f32x2 e = x * __builtin_elementwise_fma(x * x, c1, c0);
  const f32x2 d = f32x2{__builtin_amdgcn_exp2f(e[0]), __builtin_amdgcn_exp2f(e[1])} + one;
  return x * f32x2{__builtin_amdgcn_rcpf(d[0]), __builtin_amdgcn_rcpf(d[1])};
}
DI void ln_scalars(float sum, float sumsq, float& m, float& s) {
  float mean = sum * (1.0f / D);
  float var = __builtin_fmaf(-mean, mean, sumsq * (1.0f / D));
  var = var < 0.f ? 0.f : var;
  m = bfr(mean);
  s = bfr(__builtin_amdgcn_sqrtf(bfr(bfr(var) + 1e-5f)));
}
constexpr int SC1 = 16;
DI v4u ld16(rsrc_t rs, int voff, int soff = 0) { return __builtin_amdgcn_raw_buffer_load_b128(rs, voff, soff, SC1); }
DI void dma16(rsrc_t rs, LAS unsigned char* dst, int voff) { __builtin_amdgcn_raw_ptr_buffer_load_lds(rs, dst, 16, voff, 0, 0, SC1); }
DI void st16(rsrc_t rs, v4u v, int voff, int soff = 0) { __builtin_amdgcn_raw_buffer_store_b128(v, rs, voff, soff, SC1); }
DI void st4(rsrc_t rs, unsigned v, int voff, int soff = 0) { __builtin_amdgcn_raw_buffer_store_b32(v, rs, voff, soff, SC1); }
#define DRAIN() asm volatile("s_waitcnt vmcnt(0)" ::: "memory")
#define ORDER() __builtin_amdgcn_fence(__ATOMIC_ACQUIRE, "wavefront")
constexpr unsigned SPIN_CAP = 1u << 15;

constexpr int PSLEEP = 8;
DI void poll_tag(gu64* p, u64 tag) {
  for (unsigned spins = 0; spins < SPIN_CAP; ++spins) {
    if (__all(__hip_atomic_load(p, RLX) == tag)) break;
    __builtin_amdgcn_s_sleep(PSLEEP);
  }
  ORDER();
}

struct KIdx { unsigned short v[2080]; };
constexpr KIdx make_kidx() {
  KIdx t{};
  for (int y = 0; y < 2080; ++y) {
    if (y >= 2048) { t.v[y] = 768; continue; }
    int j = 2047 - y;
    int v = j;
    if (j >= 256) {
      int sh = j >= 1024 ? 3 : (j >= 512 ? 2 : 1);
      int step = 1 << sh, lo = (j >> sh) << sh, rem = j - lo, half = step >> 1;
      if (rem > half || (rem == half && (((lo >> sh) & 1) != 0))) lo += step;
      v = lo;
    }
    int seg = v >= 1024 ? 3 : (v >= 512 ? 2 : (v >= 256 ? 1 : 0));
    int u = seg == 0 ? v : v - (128 << seg);
    int rb = seg == 0 ? 0 : (seg == 1 ? 16 : (seg == 2 ? 24 : 32));
    int row = rb + ((u & 7) >> seg) + (u >> 7) * (8 >> seg);
    int col = (u >> 3) & 15;
    t.v[y] = (unsigned short)(row * 16 + col);
  }
  return t;
}
__device__ const KIdx kIdxTab = make_kidx();

struct cplx { float r, i; };
DI cplx cmul(cplx a, cplx b) {
  const f32x2 t = f32x2{-a.i, a.i} * f32x2{b.i, b.r};
  const f32x2 r = __builtin_elementwise_fma(f32x2{a.r, a.r}, f32x2{b.r, b.i}, t);
  return cplx{r[0], r[1]};
}

DI cplx cexp_r(float a2, float th, float r) {
  float m = __builtin_amdgcn_exp2f(a2 * r);
  float ang = __builtin_amdgcn_fractf(th * r);
  return cplx{m * __builtin_amdgcn_cosf(ang), m * __builtin_amdgcn_sinf(ang)};
}

constexpr int CV_TAB = 57 * 256;
constexpr int CV_CH = 2 * CV_TAB;
constexpr int CV_KC = 4160;
constexpr int CV_XCH = 4 * CV_KC;
constexpr int CV_KCT = 152 * 1024;
constexpr int CV_U = 4 * CV_CH;
constexpr int LDS_BYTES = 160 * 1024;
constexpr int CV_MISC = LDS_BYTES - 512;
static_assert(4 * CV_KC + 8192 <= CV_CH, "conv channel region");
constexpr int PR_ROW = 1168;
constexpr int PR_U = CV_U, PR_X = CV_U + 8 * PR_ROW;
constexpr int ROWB = 1152;
constexpr int G_A = 0;
constexpr int G_ST = 73728;
constexpr int G_X = G_ST + 4096;
constexpr int G_H = G_X + 12 * 2048;
constexpr int G_O = G_H + 12 * 1024;
constexpr int G_OROW = 208;
constexpr int ML_A = 0, ML_H = 64 * ROWB, ML_ST = 2 * 64 * ROWB;
constexpr int ML_CS = ML_ST + 576;
static_assert(CV_U + 288 * 128 <= CV_KCT && CV_KCT + 4 * 1600 <= CV_MISC && ML_CS + 2304 <= CV_MISC && G_O + 2 * 64 * G_OROW <= CV_MISC && PR_X + 8 * PR_ROW <= CV_MISC, "LDS");

DI void lds_arrive_wait(LAS unsigned* ctr, unsigned target, int lane) {
  asm volatile("s_waitcnt lgkmcnt(0)" ::: "memory");
  if (lane == 0) __hip_atomic_fetch_add(ctr, 1u, __ATOMIC_RELAXED, __HIP_MEMORY_SCOPE_WORKGROUP);
  while (__hip_atomic_load(ctr, __ATOMIC_RELAXED, __HIP_MEMORY_SCOPE_WORKGROUP) < target) __builtin_amdgcn_s_sleep(1);
  asm volatile("" ::: "memory");
}

DI void lds_arrive(LAS unsigned* ctr, int lane) {
  asm volatile("s_waitcnt lgkmcnt(0)" ::: "memory");
  if (lane == 0) __hip_atomic_fetch_add(ctr, 1u, __ATOMIC_RELAXED, __HIP_MEMORY_SCOPE_WORKGROUP);
  asm volatile("" ::: "memory");
}
DI bool drain_arrive_last(LAS unsigned* ctr, unsigned n, int lane) {
  DRAIN();
  unsigned old = 0;
  if (lane == 0) old = __hip_atomic_fetch_add(ctr, 1u, __ATOMIC_RELAXED, __HIP_MEMORY_SCOPE_WORKGROUP);
  return (unsigned)__builtin_amdgcn_readfirstlane(old) == n - 1;
}
DI void lds_wait(LAS unsigned* ctr, unsigned target) {
  while (__hip_atomic_load(ctr, __ATOMIC_RELAXED, __HIP_MEMORY_SCOPE_WORKGROUP) < target) __builtin_amdgcn_s_sleep(1);
  asm volatile("" ::: "memory");
}
DI void pretile_unit(LAS unsigned char* lds, rsrc_t rs, unsigned char* ws, int unit, u64 tag, const unsigned short* __restrict__ w1, const unsigned short* __restrict__ w2,
                     const unsigned short* __restrict__ w3, const unsigned short* __restrict__ w4) {
  const int tid = threadIdx.x, lane = tid & 63, wv = __builtin_amdgcn_readfirstlane(tid >> 6);
  const unsigned short* w;
  int ldw, dst, ksn, cs = -1;
  if (unit < 9) { w = w1 + unit * 64; ldw = D; dst = WS_W1T + unit * 4 * 18 * 1024; ksn = 18; cs = WS_CS1 + unit * 256; }
  else if (unit < 18) { w = w2 + (unit - 9) * 64; ldw = D; dst = WS_W2T + (unit - 9) * 4 * 18 * 1024; ksn = 18; }
  else if (unit < 90) { w = w3 + (unit - 18) * 64; ldw = F; dst = WS_W3T + (unit - 18) * 4 * 18 * 1024; ksn = 18; cs = WS_CS3 + (unit - 18) * 256; }
  else { int u = unit - 90, kc = u / 9, cg = u % 9; w = w4 + (size_t)kc * 576 * D + cg * 64; ldw = D; dst = WS_W4T + (cg * 4 * 144 + kc * 18) * 1024; ksn = 144; }
  v4u a[6];
#pragma unroll
  for (int i = 0; i < 6; ++i) {
    int p = tid + NT * i, r = p >> 3, c = p & 7;
    a[i] = *(const GAS v4u*)(w + (size_t)r * ldw + c * 8);
  }
  __syncthreads();
#pragma unroll
  for (int i = 0; i < 6; ++i) {
    int p = tid + NT * i, r = p >> 3, c = p & 7;
    int f = ((r >> 1) & 1) | (((r >> 3) & 1) << 1);
    *(LAS v4u*)(lds + r * 128 + ((((c >> 1) ^ f) << 1 | (c & 1)) << 4)) = a[i];
  }
  __syncthreads();
  const int ct = wv & 3, k0 = 6 * (wv >> 2);
  const int g = lane >> 4, q = (lane & 15) >> 2, p = lane & 3;
  const int f = ((q >> 1) & 1) | ((g & 1) << 1);
  float csum = 0.f;
  const unsigned ones = 0x3F803F80u;
#pragma unroll
  for (int ks = 0; ks < 6; ++ks) {
    LAS unsigned char* pa = lds + (32 * (k0 + ks) + 8 * g + q) * 128 + ((ct ^ f) << 5) + p * 8;
    v2u lo = tr_read(pa), hi = tr_read(pa + 4 * 128);
    v4u o = {lo.x, lo.y, hi.x, hi.y};
    st16(rs, o, lane * 16, dst + (ct * ksn + k0 + ks) * 1024);
    csum = dot2bf(o.x, ones, csum); csum = dot2bf(o.y, ones, csum); csum = dot2bf(o.z, ones, csum); csum = dot2bf(o.w, ones, csum);
  }
  if (cs >= 0) {
    csum = sum_rows(csum);
    LAS float* pc = (LAS float*)(lds + 73728);
    if (lane < 16) pc[wv * 16 + lane] = csum;
    __syncthreads();
    if (tid < 64) st4(rs, __builtin_bit_cast(unsigned, pc[tid] + pc[64 + tid] + pc[128 + tid]), cs + tid * 4);
  }
  DRAIN();
  __syncthreads();
  if (tid < 4) __hip_atomic_store((gu64*)(ws + WS_FPT) + tid * 256 + unit, tag, RLX);
}

constexpr int NC0 = 4, NC1 = 3, NC2 = 2;
template <int NC>
DI void mlp_second(LAS unsigned char* lds, rsrc_t rs, unsigned char* ws, unsigned short* __restrict__ out, u64 tag, int grp, int s, int oc0, int wi, int g,
                   LAS unsigned* mcnt, int lane, int tok, int q, int sw) {
  constexpr int DEP = NC == 4 ? 2 : 3;
  const int o4 = WS_W4T + (oc0 * 144 + 18 * s) * 1024;
  v4u bw[DEP][NC];
#pragma unroll
  for (int d = 0; d < DEP; ++d)
#pragma unroll
    for (int c = 0; c < NC; ++c) bw[d][c] = ld16(rs, lane * 16, o4 + (c * 144 + d) * 1024);
  f32x4 acc[4][NC];
#pragma unroll
  for (int r = 0; r < 4; ++r)
#pragma unroll
    for (int c = 0; c < NC; ++c) acc[r][c] = f32x4{0, 0, 0, 0};
  LAS unsigned char* hbase = lds + ML_H + tok * ROWB;
  v4u af[2][4];
#pragma unroll
  for (int k2 = 0; k2 < 18; ++k2) {
    if (k2 % 6 == 0) {
      lds_wait(mcnt + 1 + k2 / 6, 4);
#pragma unroll
      for (int r = 0; r < 4; ++r) af[k2 & 1][r] = *(LAS v4u*)(hbase + r * 16 * ROWB + (((4 * k2 + q) ^ sw) << 4));
    }
    if (k2 % 6 != 5) {
#pragma unroll
      for (int r = 0; r < 4; ++r) af[(k2 + 1) & 1][r] = *(LAS v4u*)(hbase + r * 16 * ROWB + (((4 * (k2 + 1) + q) ^ sw) << 4));
    }
    __builtin_amdgcn_sched_barrier(0);
#pragma unroll
    for (int c = 0; c < NC; ++c) {
#pragma unroll
      for (int r = 0; r < 4; ++r) acc[r][c] = mfma_bf(bw[k2 % DEP][c], af[k2 & 1][r], acc[r][c]);
      const int nk = k2 + DEP;
      if (nk < 18) bw[k2 % DEP][c] = ld16(rs, lane * 16, o4 + (c * 144 + nk) * 1024);
    }
    __builtin_amdgcn_sched_barrier(0);
  }
  const int rpo = s >> 2, slot0 = g == 0 ? 0 : (g == 1 ? NC0 : NC0 + NC1);
  LAS unsigned char* own = lds + ML_A + (rpo ^ 1) * 32 * ROWB + slot0 * 1024;
  {
    const int po = WS_PART + (s * 32 + grp) * 72 * 1024;
#pragma unroll
    for (int c = 0; c < NC; ++c)
#pragma unroll
      for (int rp = 0; rp < 2; ++rp) {
        unsigned x0 = cvtpk(acc[2 * rp][c][0], acc[2 * rp][c][1]), x1 = cvtpk(acc[2 * rp][c][2], acc[2 * rp][c][3]);
        unsigned y0 = cvtpk(acc[2 * rp + 1][c][0], acc[2 * rp + 1][c][1]), y1 = cvtpk(acc[2 * rp + 1][c][2], acc[2 * rp + 1][c][3]);
        auto s0 = __builtin_amdgcn_permlane16_swap(x0, y0, false, false);
        auto s1 = __builtin_amdgcn_permlane16_swap(x1, y1, false, false);
        const v4u o = {s0[0], s1[0], s0[1], s1[1]};
        if (rp == rpo && wi == (s & 3)) *(LAS v4u*)(own + c * 1024 + lane * 16) = o;
        else st16(rs, o, lane * 16, po + (rp * 36 + oc0 + c) * 1024);
      }
  }
  asm volatile("s_waitcnt lgkmcnt(0)" ::: "memory");
  if (drain_arrive_last(mcnt + 4 + g, 4, lane)) { if (lane == 0) __hip_atomic_store((gu64*)(ws + WS_F5) + (grp * 3 + g) * 8 + s, tag, RLX); }
  if (wi < NC) {
    if (wi == 0) { poll_tag((gu64*)(ws + WS_F5) + (grp * 3 + g) * 8 + (lane & 7), tag); lds_arrive(mcnt + 7 + g, lane); }
    else lds_wait(mcnt + 7 + g, 1);
    lds_wait(mcnt + 4 + g, 4);
    const int oc = (g == 0 ? 0 : (g == 1 ? 4 * NC0 : 4 * (NC0 + NC1))) + (s & 3) * NC + wi, u = rpo * 36 + oc;
    const int row = rpo * 32 + (q & 1) * 16 + tok;
    const int off = ((grp * 64 + row) * D + oc * 16 + 8 * (q >> 1)) * 2;
    v4u p[8];
#pragma unroll
    for (int k = 0; k < 7; ++k) p[k] = ld16(rs, lane * 16, WS_PART + ((((s + 1 + k) & 7) * 32 + grp) * 72 + u) * 1024);
    const v4u hv = ld16(rs, WS_HR + off);
    p[7] = *(LAS v4u*)(own + wi * 1024 + lane * 16);
    const v4u xv = *(LAS v4u*)(lds + ML_A + row * ROWB + (((2 * oc + (q >> 1)) ^ ((row >> 1) & 7)) << 4));
    v4u o;
#pragma unroll
    for (int j = 0; j < 4; ++j) {
      f32x2 sum = f32x2{bflo(xv[j]), bfhi(xv[j])} + f32x2{bflo(hv[j]), bfhi(hv[j])};
#pragma unroll
      for (int k = 0; k < 8; ++k) sum += f32x2{bflo(p[k][j]), bfhi(p[k][j])};
      o[j] = cvtpk(sum[0], sum[1]);
    }
    *(GAS v4u*)((GAS unsigned char*)out + off) = o;
  }
}

__global__ __launch_bounds__(NT, 3) void k_all(const unsigned short* __restrict__ x, const unsigned short* __restrict__ Are, const unsigned short* __restrict__ Aim,
                                               const unsigned short* __restrict__ Bre, const unsigned short* __restrict__ Bim, const unsigned short* __restrict__ Cre,
                                               const unsigned short* __restrict__ Cim, const unsigned short* __restrict__ logdt, const unsigned short* __restrict__ dskip,
                                               const unsigned short* __restrict__ w1, const unsigned short* __restrict__ w2, const unsigned short* __restrict__ w3,
                                               const unsigned short* __restrict__ w4, unsigned char* __restrict__ ws, unsigned short* __restrict__ out) {
  extern __shared__ __attribute__((aligned(16))) unsigned char smem[];
  LAS unsigned char* lds = (LAS unsigned char*)smem;
  const int tid = threadIdx.x, lane = tid & 63, wv = __builtin_amdgcn_readfirstlane(tid >> 6);
  const int b = blockIdx.x, xcd = b & 7, jj = b >> 3;
  const rsrc_t rs = __builtin_amdgcn_make_buffer_rsrc((void*)ws, 0, WS_END, 0x00020000);
  const bool is_conv = jj < 18, is_gemm = jj < 24;
  const int c0 = 4 * (18 * xcd + jj);
  if (tid < 16) ((LAS unsigned*)(lds + CV_MISC))[tid] = 0;
  asm volatile("s_waitcnt lgkmcnt(0)" ::: "memory");
  asm volatile("" ::"s"(x), "s"(Are), "s"(Aim), "s"(Bre), "s"(Bim), "s"(Cre), "s"(Cim), "s"(logdt), "s"(ws));
  const u64 epoch = __hip_atomic_load((gu64*)(ws + WS_CTRL), RLX);
#define GET_TAG() ((((u64)(unsigned)__builtin_amdgcn_readfirstlane((unsigned)(epoch >> 32)) << 32) | (unsigned)__builtin_amdgcn_readfirstlane((unsigned)epoch)) + 1)
  u64 tag;
  LAS unsigned* cnt = (LAS unsigned*)(lds + CV_MISC);
  LAS float* smx = (LAS float*)(lds + CV_MISC + 64);
  LAS float* kscs = (LAS float*)(lds + CV_MISC + 96);

  if (wv < 4) {
    if (wv < 3) {
      const int t8 = lane >> 3, sub = lane & 7;
      const GAS v4u* row = (const GAS v4u*)(x + (size_t)(b * 8 + t8) * D) + 24 * wv + sub;
      v4u a[3];
#pragma unroll
      for (int i = 0; i < 3; ++i) a[i] = row[8 * i];
      __builtin_amdgcn_sched_barrier(0);
      __builtin_amdgcn_s_barrier();
      tag = GET_TAG();
      float s = 0.f, ss = 0.f;
      const unsigned ones = 0x3F803F80u;
#pragma unroll
      for (int i = 0; i < 3; ++i) {
#pragma unroll
        for (int j = 0; j < 4; ++j) { s = dot2bf(a[i][j], ones, s); ss = dot2bf(a[i][j], a[i][j], ss); }
      }
#pragma unroll
      for (int o = 0; o < 1; ++o) { s = sum8(s); ss = sum8(ss); }
      LAS f32x2* part = (LAS f32x2*)(lds + CV_MISC + 128);
      if (sub == 0) part[wv * 8 + t8] = f32x2{s, ss};
#pragma unroll
      for (int i = 0; i < 3; ++i) *(LAS v4u*)(lds + PR_X + t8 * PR_ROW + (24 * wv + 8 * i + sub) * 16) = a[i];
      lds_arrive_wait(cnt + 4, 3, lane);
      {
        const f32x2 p0 = part[t8], p1 = part[8 + t8], p2 = part[16 + t8];
        float m, sd;
        ln_scalars(p0.x + p1.x + p2.x, p0.y + p1.y + p2.y, m, sd);
        const float r = __builtin_amdgcn_rcpf(sd), nmr = -m * r;
#pragma unroll
        for (int i = 0; i < 3; ++i) {
          v4u up;
#pragma unroll
          for (int j = 0; j < 4; ++j) {
            const f32x2 xv = {bflo(a[i][j]), bfhi(a[i][j])};
            const f32x2 uv = __builtin_elementwise_fma(xv, f32x2{r, r}, f32x2{nmr, nmr});
            up[j] = cvtpkh(uv[0], uv[1]);
          }
          *(LAS v4u*)(lds + PR_U + t8 * PR_ROW + (24 * wv + 8 * i + sub) * 16) = up;
        }
      }
      {
        const int g = lane >> 4, q = (lane & 15) >> 2, p = lane & 3;
        LAS unsigned char* pa = lds + ((p & 1) ? PR_X : PR_U) + q * PR_ROW + (192 * wv + 8 * g + 4 * (p >> 1)) * 2;
#pragma unroll
        for (int k = 0; k < 6; ++k) {
          v2u lo = tr_read(pa + k * 64), hi = tr_read(pa + k * 64 + 4 * PR_ROW);
          st16(rs, v4u{lo.x, lo.y, hi.x, hi.y}, lane * 16, WS_UX + b * 18432 + (6 * wv + k) * 1024);
        }
      }
      DRAIN();
      lds_arrive_wait(cnt + 4, 6, lane);
      if (tid < 8) __hip_atomic_store((gu64*)(ws + WS_F1) + tid * 256 + b, tag, RLX);
    } else {
      __builtin_amdgcn_s_barrier();
      tag = GET_TAG();
    }
    if (is_conv) {
      poll_tag((gu64*)(ws + WS_F1 + (b & 7) * 2048) + 64 * wv + lane, tag);
      if (wv == 3) lds_arrive_wait(cnt + 4, 6, 64);
#pragma unroll
      for (int i = 0; i < 8; ++i) {
        const int oct = 64 * wv + 8 * i + (lane >> 3);
        dma16(rs, lds + CV_U + (32 + 64 * wv + 8 * i) * 128, WS_UX + oct * 18432 + (c0 >> 2) * 128 + (((lane & 7) ^ ((oct >> 1) & 7)) << 4));
      }
      *(LAS v4u*)(lds + CV_U + tid * 16) = v4u{0, 0, 0, 0};
    }
  } else if (is_conv) {
    const int cl = (wv - 4) >> 1, half = wv & 1, ch = c0 + cl, n = half * 64 + lane;
    const int gi = ch * NS + n;
    const int t2 = half * 64 + lane;
    const unsigned short rld = logdt[ch], rar = Are[gi], rai = Aim[gi], rbr = Bre[gi], rbi = Bim[gi], rcr = Cre[gi], rci = Cim[gi];
    v4u kid[3];
    unsigned kid2[3];
#pragma unroll
    for (int i = 0; i < 3; ++i) {
      const int p = t2 + 128 * i < 259 ? t2 + 128 * i : 258;
      kid[i] = *(const GAS v4u*)(kIdxTab.v + p * 8);
      kid2[i] = *(const GAS unsigned*)(kIdxTab.v + p * 8 + 8);
    }
    __builtin_amdgcn_sched_barrier(0);
    __builtin_amdgcn_s_barrier();
    tag = GET_TAG();
    {
      const float dt = bfr(expf(bf2f(rld)));
      const float zr = dt * bf2f(rar), zi = dt * bf2f(rai);
      const float dBr = dt * bf2f(rbr), dBi = dt * bf2f(rbi);
      cplx W = cmul(cplx{bf2f(rcr), bf2f(rci)}, cplx{dBr, dBi});
      float smax = fmaxf(fabsf(W.r), fabsf(W.i));
      smax = wave_max(smax);
      const float inv = smax > 0.f ? 1.0f / smax : 0.f;
      W.r *= inv; W.i *= inv;
      const float a2 = zr * 1.44269504089f, th = zi * 0.15915494309f;
      const cplx e1 = cexp_r(a2, th, 1.f), e8 = cexp_r(a2, th, 8.f), e128 = cexp_r(a2, th, 128.f);
      const cplx e256 = cexp_r(a2, th, 256.f), e512 = cexp_r(a2, th, 512.f), e1024 = cexp_r(a2, th, 1024.f);
      LAS unsigned char* chan = lds + cl * CV_CH;
      LAS unsigned char* tab = chan + half * CV_TAB;
      const int nsw = lane >> 2, nlo = (lane & 3) * 4;
#define PUT(row, val) *(LAS unsigned*)(tab + (row) * 256 + ((nsw ^ ((row) & 15)) << 4) + nlo) = (val)
#define PUTA(row, z) PUT(row, cvtpkh((z).r, -(z).i))
#define PUTB(row, z) PUT(41 + (row), cvtpkh((z).r, (z).i))
      {
        cplx c[16];
        c[0] = cplx{1.f, 0.f}; c[1] = e8; c[2] = cmul(e8, e8); c[3] = cmul(c[2], e8); c[4] = cmul(c[2], c[2]);
        c[5] = cmul(c[4], c[1]); c[6] = cmul(c[4], c[2]); c[7] = cmul(c[4], c[3]); c[8] = cmul(c[4], c[4]);
#pragma unroll
        for (int j = 9; j < 16; ++j) c[j] = cmul(c[8], c[j - 8]);
#pragma unroll
        for (int j = 0; j < 16; ++j) PUTB(j, c[j]);
      }
      {
        const cplx e2 = cmul(e1, e1), e4 = cmul(e2, e2);
        cplx p[8];
        p[0] = W; p[1] = cmul(W, e1); p[2] = cmul(W, e2); p[3] = cmul(p[1], e2);
#pragma unroll
        for (int c = 4; c < 8; ++c) p[c] = cmul(p[c - 4], e4);
#pragma unroll
        for (int c = 0; c < 8; ++c) {
          const cplx qv = cmul(p[c], e128);
          PUTA(c, p[c]);
          PUTA(8 + c, qv);
          if ((c & 1) == 0) {
            PUTA(16 + (c >> 1), cmul(p[c], e256));
            PUTA(20 + (c >> 1), cmul(qv, e256));
          }
        }
        const cplx e384 = cmul(e256, e128);
        const cplx s0 = cmul(p[0], e512), s1 = cmul(p[4], e512);
        PUTA(24, s0); PUTA(25, s1);
        PUTA(26, cmul(s0, e128)); PUTA(27, cmul(s1, e128));
        PUTA(28, cmul(s0, e256)); PUTA(29, cmul(s1, e256));
        PUTA(30, cmul(s0, e384)); PUTA(31, cmul(s1, e384));
        const cplx t0 = cmul(W, e1024), t4 = cmul(t0, e512);
        PUTA(32, t0); PUTA(33, cmul(t0, e128)); PUTA(34, cmul(t0, e256)); PUTA(35, cmul(t0, e384));
        PUTA(36, t4); PUTA(37, cmul(t4, e128)); PUTA(38, cmul(t4, e256)); PUTA(39, cmul(t4, e384));
        PUTA(40, cmul(t0, e1024));
      }
#undef PUT
#undef PUTA
#undef PUTB
      if (lane == 0) smx[wv - 4] = smax;
      __builtin_amdgcn_s_setprio(2);
      lds_arrive_wait(cnt + cl, 2, lane);
      const float s0_ = smx[2 * cl], s1_ = smx[2 * cl + 1];
      const float sm = fmaxf(s0_, s1_);
      const float ksc = sm * (182.f / 1024.f);
      const float kinv = sm > 0.f ? 1.0f / ksc : 0.f;
      LAS unsigned short* kc = (LAS unsigned short*)(lds + CV_KCT + cl * 1600);
      {
        const int r = lane & 15, h = lane >> 4;
        const float f0 = s0_ * kinv, f1 = s1_ * kinv;
#pragma unroll
        for (int tI = 0; tI < 2; ++tI) {
          if (tI == 1 && half == 1) break;
          const int t = half ? 1 : 2 * tI;
          f32x4 d0 = {0, 0, 0, 0}, d1 = {0, 0, 0, 0};
#pragma unroll
          for (int ks = 0; ks < 4; ++ks) {
            const int ob = (41 + r) * 256 + (((4 * ks + h) ^ ((41 + r) & 15)) << 4), oa = (16 * t + r) * 256 + (((4 * ks + h) ^ r) << 4);
            d0 = mfma_h(*(LAS v4u*)(chan + oa), *(LAS v4u*)(chan + ob), d0);
            d1 = mfma_h(*(LAS v4u*)(chan + CV_TAB + oa), *(LAS v4u*)(chan + CV_TAB + ob), d1);
          }
#pragma unroll
          for (int e = 0; e < 4; ++e) {
            const float v = __builtin_fmaf(d0[e], f0, d1[e] * f1);
            kc[(16 * t + 4 * h + e) * 16 + r] = (unsigned short)(cvtpkh(v, v) & 0xFFFFu);
          }
        }
        if (half == 1 && lane == 0) { kc[768] = 0; kscs[cl] = ksc; }
      }
      __builtin_amdgcn_s_setprio(0);
      lds_arrive_wait(cnt + cl, 4, lane);
#pragma unroll
      for (int i = 0; i < 3; ++i) {
        const int z = t2 + 128 * i;
        unsigned d[5];
#pragma unroll
        for (int j = 0; j < 4; ++j) d[j] = (unsigned)kc[kid[i][j] & 0xFFFFu] | ((unsigned)kc[kid[i][j] >> 16] << 16);
        d[4] = (unsigned)kc[kid2[i] & 0xFFFFu];
        if (z < 259) {
          LAS unsigned char* base = chan + z * 16;
          *(LAS v4u*)(base) = v4u{d[0], d[1], d[2], d[3]};
          *(LAS v4u*)(base + CV_KC) = v4u{__builtin_amdgcn_alignbit(d[1], d[0], 16), __builtin_amdgcn_alignbit(d[2], d[1], 16), __builtin_amdgcn_alignbit(d[3], d[2], 16), __builtin_amdgcn_alignbit(d[4], d[3], 16)};
        }
      }
    }
  } else {
    __builtin_amdgcn_s_barrier();
    tag = GET_TAG();
  }
  if (!is_conv) {
    const int idx = (jj - 18) * 8 + xcd;
    for (int i = 0; i < 3; ++i) __builtin_amdgcn_s_sleep(32);
    pretile_unit(lds, rs, ws, idx, tag, w1, w2, w3, w4);
    if (idx >= 48 && idx < 98) pretile_unit(lds, rs, ws, 112 + idx - 48, tag, w1, w2, w3, w4);
  }
  if (is_conv) {
    const float dsk = bf2f(dskip[c0 + (wv & 3)]);
    __syncthreads();
    const int cl = wv & 3, kh = (wv >> 2) & 1;
    f32x4 acc[8], base[4];
    float ksc = 0.f;
    if (wv < 8) {
#pragma unroll
      for (int t = 0; t < 8; ++t) acc[t] = f32x4{0, 0, 0, 0};
      const int bb = lane & 15, h = lane >> 4;
      const int cpy = (bb & 1) ^ 1;
      LAS unsigned char* abase = lds + cl * CV_CH + cpy * CV_KC + (2047 - 16 - bb + 8 * h - cpy) * 2 - 64 * 4 * kh;
      LAS unsigned char* bbase = lds + CV_U + (32 + 2 * (bb - 1) + h) * 128;
      v4u A[8], B[8];
#define LDA(i, rho) do { LAS unsigned* pa_ = (LAS unsigned*)(abase - 64 * ((rho) + 8 * (i))); A[i] = v4u{pa_[0], pa_[1], pa_[2], pa_[3]}; } while (0)
#define LDB(m, rho) B[m] = *(LAS v4u*)(bbase - 512 * (4 * kh + (rho)) + ((cl ^ ((bb - 1 + (h >> 1) - 2 * (4 * kh + (rho))) & 7)) << 4) + 4096 * (m))
#pragma unroll
      for (int i = 0; i < 8; ++i) { LDB(i, 0); LDA(i, 0); }
#pragma unroll
      for (int rho = 0; rho < 4; ++rho) {
#pragma unroll
        for (int i = 0; i < 8; ++i) {
#pragma unroll
          for (int m = 7 - i; m >= 0; --m) acc[i + m] = mfma_h(A[i], B[m], acc[i + m]);
          if (rho < 3) { LDA(i, rho + 1); LDB(7 - i, rho + 1); }
          __builtin_amdgcn_sched_barrier(0);
        }
      }
#undef LDA
#undef LDB
      LAS unsigned char* xo = lds + cl * CV_CH + CV_XCH + kh * 4096 + lane * 16;
#pragma unroll
      for (int t = 0; t < 4; ++t) {
        f32x4 lo = acc[t], hi = acc[4 + t];
        asm volatile("" : "+v"(lo), "+v"(hi));
        *(LAS f32x4*)(xo + t * 1024) = kh ? lo : hi;
        acc[t] = kh ? hi : lo;
      }
      const int a = lane & 15, q = lane >> 4;
#pragma unroll
      for (int t = 0; t < 4; ++t) {
        LAS unsigned char* po = lds + CV_U + (32 + 32 * (4 * kh + t) + 2 * a + (q >> 1)) * 128 + (q & 1) * 8;
        const v2u uu = *(LAS v2u*)(po + ((cl ^ (a & 7)) << 4));
        const v2u xx = *(LAS v2u*)(po + (((4 + cl) ^ (a & 7)) << 4));
#pragma unroll
        for (int e = 0; e < 2; ++e) {
          const unsigned uw = e ? uu.y : uu.x, xw = e ? xx.y : xx.x;
          const h16x2 u2 = __builtin_bit_cast(h16x2, uw);
          const f32x2 bv = __builtin_elementwise_fma(f32x2{(float)u2[0], (float)u2[1]}, f32x2{dsk, dsk}, f32x2{bflo(xw), bfhi(xw)});
          base[t][2 * e] = bv[0]; base[t][2 * e + 1] = bv[1];
        }
      }
      ksc = kscs[cl];
#pragma unroll
      for (int t = 0; t < 4; ++t) base[t] += acc[t] * ksc;
    }
    __syncthreads();
    if (wv < 8) {
      const int ch = c0 + cl;
      const int a = lane & 15, q = lane >> 4;
      LAS unsigned char* xi = lds + cl * CV_CH + CV_XCH + (kh ^ 1) * 4096 + lane * 16;
      f32x4 o[4];
#pragma unroll
      for (int t = 0; t < 4; ++t) o[t] = *(LAS f32x4*)(xi + t * 1024);
      unsigned pk[4][2];
#pragma unroll
      for (int t = 0; t < 4; ++t) {
        const f32x4 hv = base[t] + o[t] * ksc;
        pk[t][0] = cvtpk(hv[0], hv[1]);
        pk[t][1] = cvtpk(hv[2], hv[3]);
      }
#pragma unroll
      for (int t = 0; t < 4; t += 2) {
        auto s0 = __builtin_amdgcn_permlane16_swap(pk[t][0], pk[t + 1][0], false, false);
        auto s1 = __builtin_amdgcn_permlane16_swap(pk[t][1], pk[t + 1][1], false, false);
        const int tok = 256 * (4 * kh + t + (q & 1)) + 16 * a + 8 * (q >> 1);
        st16(rs, v4u{s0[0], s1[0], s0[1], s1[1]}, WS_HT + (ch * L + tok) * 2);
      }
      if (drain_arrive_last(cnt + 8, 8, lane)) { if (lane < 8) __hip_atomic_store((gu64*)(ws + WS_F2) + lane * 256 + (18 * xcd + jj), tag, RLX); }
    }
  }

  const int tt = 4 * xcd + jj / 6, sl = jj % 6;
  if (is_gemm) {
    const int cl = wv % 6, kh = wv / 6, ct = 6 * sl + cl;
    const int tok = lane & 15, q = lane >> 4;
    if (wv == 0) {
      const int u0 = (96 * sl) >> 6;
      poll_tag((gu64*)(ws + WS_FPT + (b & 3) * 2048) + (lane & 1) + u0 + ((lane & 2) ? 9 : 0), tag);
    }
    __syncthreads();
    v4u bw[9];
    if (wv != 0) {
#pragma unroll
      for (int ks = 0; ks < 9; ++ks) bw[ks] = ld16(rs, lane * 16, WS_W1T + (ct * 18 + 9 * kh + ks) * 1024);
    }
    int aoff[6];
#pragma unroll
    for (int i = 0; i < 6; ++i) {
      const int p = tid + NT * i, c = p >> 3, pp = p & 7;
      const int f = ((c >> 1) & 1) | (((c >> 3) & 1) << 1);
      aoff[i] = WS_HT + (c * L + tt * 64 + ((((pp >> 1) ^ f) << 1) | (pp & 1)) * 8) * 2;
      asm volatile("" : "+v"(aoff[i]));
    }
    if (wv == 0) {
      const int fo = WS_F2 + (b & 7) * 2048 + (lane < 36 ? lane : 0) * 32;
      const unsigned tl = (unsigned)tag, th = (unsigned)(tag >> 32);
      for (unsigned spins = 0; spins < SPIN_CAP; ++spins) {
        asm volatile("" ::: "memory");
        const v4u f0 = ld16(rs, fo), f1 = ld16(rs, fo + 16);
        if (__all(f0.x == tl && f0.y == th && f0.z == tl && f0.w == th && f1.x == tl && f1.y == th && f1.z == tl && f1.w == th)) break;
        __builtin_amdgcn_s_sleep(PSLEEP);
      }
      ORDER();
#pragma unroll
      for (int ks = 0; ks < 9; ++ks) bw[ks] = ld16(rs, lane * 16, WS_W1T + (ct * 18 + 9 * kh + ks) * 1024);
    }
    const f32x4 cs = __builtin_bit_cast(f32x4, ld16(rs, WS_CS1 + (ct * 16 + 4 * q) * 4));
    __syncthreads();
#pragma unroll
    for (int i = 0; i < 6; ++i) dma16(rs, lds + G_A + (wv * 64 + NT * i) * 16, aoff[i]);
    __syncthreads();
    v4u bw2[9];
    v4u hpiece;
    f32x4 acc[4];
#pragma unroll
    for (int r = 0; r < 4; ++r) acc[r] = f32x4{0, 0, 0, 0};
    {
      const int pq = (lane & 15) >> 2, pp = lane & 3;
      const int f = ((pq >> 1) & 1) | ((q & 1) << 1);
      const unsigned ones = 0x3F803F80u;
      LAS unsigned char* pa0 = lds + G_A + (8 * q + pq) * 128 + pp * 8;
      LAS unsigned char* par[4];
#pragma unroll
      for (int r = 0; r < 4; ++r) par[r] = pa0 + ((r ^ f) << 5);
      v4u af[2][4];
#pragma unroll
      for (int r = 0; r < 4; ++r) { v2u lo = tr_read(par[r] + 9 * kh * 4096), hi = tr_read(par[r] + 9 * kh * 4096 + 512); af[0][r] = v4u{lo.x, lo.y, hi.x, hi.y}; }
#pragma unroll
      for (int ks = 0; ks < 9; ++ks) {
        if (ks < 8) {
#pragma unroll
          for (int r = 0; r < 4; ++r) {
            v2u lo = tr_read(par[r] + (9 * kh + ks + 1) * 4096), hi = tr_read(par[r] + (9 * kh + ks + 1) * 4096 + 512);
            af[(ks + 1) & 1][r] = v4u{lo.x, lo.y, hi.x, hi.y};
          }
        }
        __builtin_amdgcn_sched_barrier(0);
#pragma unroll
        for (int r = 0; r < 4; ++r) acc[r] = mfma_bf(bw[ks], af[ks & 1][r], acc[r]);
        __builtin_amdgcn_sched_barrier(0);
      }
    }
#pragma unroll
    for (int ks = 0; ks < 9; ++ks) bw2[ks] = ld16(rs, lane * 16, WS_W2T + (ct * 18 + 9 * kh + ks) * 1024);
    hpiece = ld16(rs, WS_HT + ((ct * 16 + (lane >> 2)) * L + tt * 64 + kh * 32 + (lane & 3) * 8) * 2);
    __builtin_amdgcn_sched_barrier(0);
    {
      LAS unsigned char* xo = lds + G_X + wv * 2048 + lane * 16;
#pragma unroll
      for (int r = 0; r < 2; ++r) {
        f32x4 lo = acc[r], hi = acc[2 + r];
        asm volatile("" : "+v"(lo), "+v"(hi));
        *(LAS f32x4*)(xo + r * 1024) = kh ? lo : hi;
        acc[r] = kh ? hi : lo;
      }
    }
    if (wv < 4) {
      const int pq = (lane & 15) >> 2, pp = lane & 3;
      const int f = ((pq >> 1) & 1) | ((q & 1) << 1);
      LAS unsigned char* ps0 = lds + G_A + (8 * q + pq) * 128 + pp * 8 + ((wv ^ f) << 5);
      f32x4 dsum = {0, 0, 0, 0}, dsq = {0, 0, 0, 0};
      const v4u onesv = {0x3F803F80u, 0x3F803F80u, 0x3F803F80u, 0x3F803F80u};
#pragma unroll
      for (int hf = 0; hf < 2; ++hf) {
        v4u as[9];
#pragma unroll
        for (int i = 0; i < 9; ++i) { v2u lo = tr_read(ps0 + (9 * hf + i) * 4096), hi = tr_read(ps0 + (9 * hf + i) * 4096 + 512); as[i] = v4u{lo.x, lo.y, hi.x, hi.y}; }
#pragma unroll
        for (int i = 0; i < 9; ++i) { dsum = mfma_bf(onesv, as[i], dsum); dsq = mfma_bf(as[i], as[i], dsq); }
      }
      const int e = tok & 3;
      const float sq = e == 0 ? dsq[0] : (e == 1 ? dsq[1] : (e == 2 ? dsq[2] : dsq[3]));
      const float s_ = dsum[0], ss = sq;
      float m1, s1;
      ln_scalars(s_, ss, m1, s1);
      const float mean = s_ * (1.0f / D);
      float var = __builtin_fmaf(-mean, mean, ss * (1.0f / D));
      var = var < 0.f ? 0.f : var;
      const float r1 = __builtin_amdgcn_rcpf(s1);
      const float m2 = bfr((mean - m1) * r1);
      const float s2 = bfr(__builtin_amdgcn_sqrtf(bfr(bfr(var * r1 * r1) + 1e-5f)));
      if (q == (tok >> 2)) *(LAS f32x4*)(lds + G_ST + 2048 + (wv * 16 + tok) * 16) = f32x4{__builtin_fmaf(m2, s1, m1), r1 * __builtin_amdgcn_rcpf(s2), m1, r1};
    }
    __syncthreads();
    f32x2 st1[2];
    {
      LAS unsigned char* xi = lds + G_X + (kh ? wv - 6 : wv + 6) * 2048 + lane * 16;
      f32x4 o[2], sc[2];
#pragma unroll
      for (int r = 0; r < 2; ++r) { o[r] = *(LAS f32x4*)(xi + r * 1024); sc[r] = *(LAS f32x4*)(lds + G_ST + 2048 + ((2 * kh + r) * 16 + tok) * 16); }
      __builtin_amdgcn_sched_barrier(0);
      unsigned pk[2][2];
#pragma unroll
      for (int r = 0; r < 2; ++r) {
        st1[r] = f32x2{sc[r][2], sc[r][3]};
        const f32x4 v = (acc[r] + o[r] - cs * sc[r][0]) * sc[r][1];
        const f32x2 ga = gelu2(f32x2{v[0], v[1]}), gb = gelu2(f32x2{v[2], v[3]});
        pk[r][0] = cvtpk(ga[0], ga[1]);
        pk[r][1] = cvtpk(gb[0], gb[1]);
      }
#pragma unroll
      for (int r = 0; r < 2; ++r) *(LAS v2u*)(lds + G_O + ((2 * kh + r) * 16 + tok) * G_OROW + cl * 32 + q * 8) = v2u{pk[r][0], pk[r][1]};
    }
    const int orow = tid / 12, ocol = tid % 12;
    __syncthreads();
    st16(rs, *(LAS v4u*)(lds + G_O + orow * G_OROW + ocol * 16), WS_H1 + ((tt * 64 + orow) * D + sl * 96 + ocol * 8) * 2);
    if (drain_arrive_last(cnt + 9, 12, lane)) { if (lane == 0) __hip_atomic_store((gu64*)(ws + WS_F3) + tt * 8 + sl, tag, RLX); }

    {
      const int c = lane >> 2, pc = lane & 3;
      *(LAS v4u*)(lds + G_H + wv * 1024 + c * 64 + ((((pc >> 1) ^ ((c >> 3) & 1)) << 1 | (pc & 1)) << 4)) = hpiece;
    }
#pragma unroll
    for (int i = 0; i < 6; ++i) {
      const int p = tid + NT * i, r = p / 72, c = (p % 72) ^ ((r >> 1) & 7);
      const int cg = c / 18;
      const int sh = cg == 0 ? -1 : (cg == 1 ? 1 : (cg == 2 ? -2 : 2));
      const int t = tt * 64 + r + sh;
      aoff[i] = (t >= 0 && t < L) ? WS_H1 + (t * D + c * 8) * 2 : WS_END;
      asm volatile("" : "+v"(aoff[i]));
    }
    if (wv == 0) {
      int t = tt - 1 + (lane < 18 ? lane / 6 : 1);
      t = t < 0 ? 0 : (t > 31 ? 31 : t);
      poll_tag((gu64*)(ws + WS_F3) + t * 8 + (lane < 18 ? lane % 6 : 0), tag);
    }
    __syncthreads();
#pragma unroll
    for (int i = 0; i < 6; ++i) dma16(rs, lds + G_A + (wv * 64 + NT * i) * 16, aoff[i]);
    __syncthreads();
#pragma unroll
    for (int r = 0; r < 4; ++r) acc[r] = f32x4{0, 0, 0, 0};
    {
      const int sw = (tok >> 1) & 7;
      LAS unsigned char* pa = lds + G_A + tok * ROWB;
      v4u af[2][4];
#pragma unroll
      for (int r = 0; r < 4; ++r) af[0][r] = *(LAS v4u*)(pa + r * 16 * ROWB + (((4 * 9 * kh + q) ^ sw) << 4));
#pragma unroll
      for (int ks = 0; ks < 9; ++ks) {
        if (ks < 8) {
#pragma unroll
          for (int r = 0; r < 4; ++r) af[(ks + 1) & 1][r] = *(LAS v4u*)(pa + r * 16 * ROWB + (((4 * (9 * kh + ks + 1) + q) ^ sw) << 4));
        }
        __builtin_amdgcn_sched_barrier(0);
#pragma unroll
        for (int r = 0; r < 4; ++r) acc[r] = mfma_bf(bw2[ks], af[ks & 1][r], acc[r]);
        __builtin_amdgcn_sched_barrier(0);
      }
    }
    v2u hh[2];
    {
      LAS unsigned char* xo = lds + G_X + wv * 2048 + lane * 16;
#pragma unroll
      for (int r = 0; r < 2; ++r) {
        f32x4 lo = acc[r], hi = acc[2 + r];
        asm volatile("" : "+v"(lo), "+v"(hi));
        *(LAS f32x4*)(xo + r * 1024) = kh ? lo : hi;
        acc[r] = kh ? hi : lo;
      }
#pragma unroll
      for (int r = 0; r < 2; ++r) {
        const int c = 4 * q + ((lane & 15) >> 2);
        hh[r] = tr_read(lds + G_H + wv * 1024 + c * 64 + ((r ^ ((c >> 3) & 1)) << 5) + (lane & 3) * 8);
        const float m1 = st1[r][0], r1 = st1[r][1];
        const f32x4 hv = {bflo(hh[r].x), bfhi(hh[r].x), bflo(hh[r].y), bfhi(hh[r].y)};
        acc[r] += (hv - m1) * r1;
        *(LAS v2u*)(lds + G_O + 64 * G_OROW + ((2 * kh + r) * 16 + tok) * G_OROW + cl * 32 + q * 8) = hh[r];
      }
    }
    __syncthreads();
    {
      LAS unsigned char* xi = lds + G_X + (kh ? wv - 6 : wv + 6) * 2048 + lane * 16;
      f32x4 o[2];
#pragma unroll
      for (int r = 0; r < 2; ++r) o[r] = *(LAS f32x4*)(xi + r * 1024);
      unsigned pk[2][2];
#pragma unroll
      for (int r = 0; r < 2; ++r) {
        const f32x4 v = acc[r] + o[r];
        pk[r][0] = cvtpk(v[0], v[1]);
        pk[r][1] = cvtpk(v[2], v[3]);
      }
#pragma unroll
      for (int r = 0; r < 2; ++r) *(LAS v2u*)(lds + G_O + ((2 * kh + r) * 16 + tok) * G_OROW + cl * 32 + q * 8) = v2u{pk[r][0], pk[r][1]};
    }
    __syncthreads();
    {
      const int off = ((tt * 64 + orow) * D + sl * 96 + ocol * 8) * 2;
      st16(rs, *(LAS v4u*)(lds + G_O + orow * G_OROW + ocol * 16), WS_X1 + off);
      st16(rs, *(LAS v4u*)(lds + G_O + 64 * G_OROW + orow * G_OROW + ocol * 16), WS_HR + off);
    }
    if (drain_arrive_last(cnt + 10, 12, lane)) { if (lane == 0) __hip_atomic_store((gu64*)(ws + WS_F4) + tt * 8 + sl, tag, RLX); }
  }

  const int grp = 2 * (jj >> 1) + (xcd >> 2), s = 2 * (xcd & 3) + (jj & 1);
  {
    constexpr int DEPTH = 3;
    if (wv == 0) {
      poll_tag((gu64*)(ws + WS_FPT + (b & 3) * 2048) + (lane < 9 ? 18 + 9 * s + lane : (lane < 18 ? 90 + 9 * s + lane - 9 : 18 + 9 * s)), tag);
    }
    __syncthreads();
    const int o3 = WS_W3T + (36 * s + 3 * wv) * 18 * 1024;
    LAS unsigned* mcnt = (LAS unsigned*)(lds + ML_ST + 512);
    v4u bw[DEPTH][3];
    const v4u csl = ld16(rs, WS_CS3 + 36 * 16 * 4 * s + (tid < 144 ? tid : 0) * 16);
    if (wv != 0) {
#pragma unroll
      for (int d = 0; d < DEPTH; ++d)
#pragma unroll
        for (int c = 0; c < 3; ++c) bw[d][c] = ld16(rs, lane * 16, o3 + (c * 18 + d) * 1024);
    }
    if (tid < 144) *(LAS v4u*)(lds + ML_CS + tid * 16) = csl;
    if (tid >= 704 && tid < 714) mcnt[tid - 704] = 0;
    int xoff[6];
#pragma unroll
    for (int i = 0; i < 6; ++i) {
      const int p = tid + NT * i, r = p / 72, c = (p % 72) ^ ((r >> 1) & 7);
      xoff[i] = WS_X1 + grp * 64 * ROWB + r * ROWB + c * 16;
      asm volatile("" : "+v"(xoff[i]));
    }
    if (wv == 0) {
      poll_tag((gu64*)(ws + WS_F4) + grp * 8 + (lane < 6 ? lane : 0), tag);
#pragma unroll
      for (int d = 0; d < DEPTH; ++d)
#pragma unroll
        for (int c = 0; c < 3; ++c) bw[d][c] = ld16(rs, lane * 16, o3 + (c * 18 + d) * 1024);
    }
    __syncthreads();
#pragma unroll
    for (int i = 0; i < 6; ++i) dma16(rs, lds + ML_A + (wv * 64 + NT * i) * 16, xoff[i]);
    __syncthreads();
    const int tok = lane & 15, q = lane >> 4;
    const int sw = (tok >> 1) & 7;
    const int wg = wv >> 2, wi = wv & 3;
    LAS unsigned char* abase = lds + ML_A + tok * ROWB;
    if (wv < 4) {
      f32x4 dsum = {0, 0, 0, 0}, dsq = {0, 0, 0, 0};
      const v4u onesv = {0x3F803F80u, 0x3F803F80u, 0x3F803F80u, 0x3F803F80u};
      LAS unsigned char* pst = abase + wv * 16 * ROWB;
      v4u as[18];
#pragma unroll
      for (int ks = 0; ks < 18; ++ks) as[ks] = *(LAS v4u*)(pst + (((4 * ks + q) ^ sw) << 4));
#pragma unroll
      for (int ks = 0; ks < 18; ++ks) { dsum = mfma_bf(onesv, as[ks], dsum); dsq = mfma_bf(as[ks], as[ks], dsq); }
      const int e = tok & 3;
      const float sq = e == 0 ? dsq[0] : (e == 1 ? dsq[1] : (e == 2 ? dsq[2] : dsq[3]));
      float m, sd;
      ln_scalars(dsum[0], sq, m, sd);
      if (q == (tok >> 2)) *(LAS f32x2*)(lds + ML_ST + (wv * 16 + tok) * 8) = f32x2{m, __builtin_amdgcn_rcpf(sd)};
      lds_arrive(mcnt, lane);
    }
    LAS unsigned char* H = lds + ML_H;
    {
      f32x4 acc[4][3];
#pragma unroll
      for (int r = 0; r < 4; ++r)
#pragma unroll
        for (int c = 0; c < 3; ++c) acc[r][c] = f32x4{0, 0, 0, 0};
      __builtin_amdgcn_s_setprio(2);
      v4u af[2][4];
#pragma unroll
      for (int r = 0; r < 4; ++r) af[0][r] = *(LAS v4u*)(abase + r * 16 * ROWB + ((q ^ sw) << 4));
#pragma unroll
      for (int ks = 0; ks < 18; ++ks) {
        if (ks < 17) {
#pragma unroll
          for (int r = 0; r < 4; ++r) af[(ks + 1) & 1][r] = *(LAS v4u*)(abase + r * 16 * ROWB + (((4 * (ks + 1) + q) ^ sw) << 4));
        }
        __builtin_amdgcn_sched_barrier(0);
#pragma unroll
        for (int c = 0; c < 3; ++c) {
#pragma unroll
          for (int r = 0; r < 4; ++r) acc[r][c] = mfma_bf(bw[ks % DEPTH][c], af[ks & 1][r], acc[r][c]);
          const int nk = ks + DEPTH;
          if (nk < 18) bw[ks % DEPTH][c] = ld16(rs, lane * 16, o3 + (c * 18 + nk) * 1024);
        }
        __builtin_amdgcn_sched_barrier(0);
      }
      __builtin_amdgcn_s_setprio(3);
      lds_wait(mcnt, 4);
      f32x2 st[4];
#pragma unroll
      for (int r = 0; r < 4; ++r) st[r] = *(LAS f32x2*)(lds + ML_ST + (r * 16 + tok) * 8);
#pragma unroll
      for (int c = 0; c < 3; ++c) {
        const f32x4 cs = *(LAS f32x4*)(lds + ML_CS + ((3 * wv + c) * 16 + 4 * q) * 4);
#pragma unroll
        for (int r = 0; r < 4; ++r) {
          const f32x2 nm = {-st[r].x, -st[r].x}, rr = {st[r].y, st[r].y};
          const f32x2 ga = gelu2(__builtin_elementwise_fma(nm, f32x2{cs[0], cs[1]}, f32x2{acc[r][c][0], acc[r][c][1]}) * rr);
          const f32x2 gb = gelu2(__builtin_elementwise_fma(nm, f32x2{cs[2], cs[3]}, f32x2{acc[r][c][2], acc[r][c][3]}) * rr);
          v2u o = {cvtpk(ga[0], ga[1]), cvtpk(gb[0], gb[1])};
          const int chunk = 2 * (3 * wv + c) + (q >> 1);
          *(LAS v2u*)(H + (r * 16 + tok) * ROWB + ((chunk ^ sw) << 4) + (q & 1) * 8) = o;
        }
      }
      lds_arrive(mcnt + 1 + wg, lane);
      if (wg == 0) __builtin_amdgcn_s_setprio(0); else if (wg == 1) __builtin_amdgcn_s_setprio(1); else __builtin_amdgcn_s_setprio(2);
    }
    if (wg == 0) mlp_second<NC0>(lds, rs, ws, out, tag, grp, s, NC0 * wi, wi, 0, mcnt, lane, tok, q, sw);
    else if (wg == 1) mlp_second<NC1>(lds, rs, ws, out, tag, grp, s, 4 * NC0 + NC1 * wi, wi, 1, mcnt, lane, tok, q, sw);
    else mlp_second<NC2>(lds, rs, ws, out, tag, grp, s, 4 * (NC0 + NC1) + NC2 * wi, wi, 2, mcnt, lane, tok, q, sw);
  }
  if (b == 0 && tid == 0) __hip_atomic_store((gu64*)(ws + WS_CTRL), tag, RLX);
}

extern "C" void kernel_launch(void* const* d_in, const int* in_sizes, int n_in, void* d_out, int out_size, void* d_ws, size_t ws_size,
                              hipStream_t stream) {
  (void)hipFuncSetAttribute((const void*)k_all, hipFuncAttributeMaxDynamicSharedMemorySize, LDS_BYTES);
  const unsigned short* const* in = (const unsigned short* const*)d_in;
  k_all<<<256, NT, LDS_BYTES, stream>>>(in[0], in[1], in[2], in[3], in[4], in[5], in[6], in[7], in[8], in[10], in[11], in[12], in[13], (unsigned char*)d_ws,
                                        (unsigned short*)d_out);
}
```

```cpp
#include <hip/hip_runtime.h>
#include <stdint.h>

typedef unsigned long long u64;
typedef unsigned v4u __attribute__((ext_vector_type(4)));
typedef unsigned v2u __attribute__((ext_vector_type(2)));
typedef float f32x4 __attribute__((ext_vector_type(4)));
typedef float f32x2 __attribute__((ext_vector_type(2)));
typedef short s16x8 __attribute__((ext_vector_type(8)));
typedef _Float16 h16x8 __attribute__((ext_vector_type(8)));
typedef _Float16 h16x2 __attribute__((ext_vector_type(2)));
typedef __bf16 bf16x2_t __attribute__((ext_vector_type(2)));
#define GAS __attribute__((address_space(1)))
#define LAS __attribute__((address_space(3)))
#define DI __device__ __forceinline__
#define RLX __ATOMIC_RELAXED, __HIP_MEMORY_SCOPE_AGENT
typedef GAS u64 gu64;
typedef __amdgpu_buffer_rsrc_t rsrc_t;

constexpr int L = 2048, D = 576, NS = 128, F = 4608;
constexpr int NT = 768;

constexpr int WS_CTRL = 0;
constexpr int WS_F1 = 4096;
constexpr int WS_FPT = WS_F1 + 8 * 2048;
constexpr int WS_F2 = WS_FPT + 4 * 2048;
constexpr int WS_F3 = WS_F2 + 8 * 2048;
constexpr int WS_F4 = WS_F3 + 32 * 64;
constexpr int WS_F5 = WS_F4 + 32 * 64;
constexpr int WS_W1T = 131072;
constexpr int WS_W2T = WS_W1T + 36 * 18 * 1024;
constexpr int WS_W3T = WS_W2T + 36 * 18 * 1024;
constexpr int WS_W4T = WS_W3T + 288 * 18 * 1024;
constexpr int WS_CS1 = WS_W4T + 36 * 144 * 1024;
constexpr int WS_CS3 = WS_CS1 + 4096;
constexpr int WS_HT = WS_CS3 + 4608 * 4;
constexpr int WS_H1 = WS_HT + D * L * 2;
constexpr int WS_X1 = WS_H1 + D * L * 2;
constexpr int WS_HR = WS_X1 + D * L * 2;
constexpr int WS_PART = WS_HR + D * L * 2;
constexpr int WS_UX = WS_PART + 8 * D * L * 2;
constexpr int WS_END = WS_UX + 256 * 144 * 128;
static_assert(WS_F5 + 32 * 192 <= WS_W1T, "control block");


DI float bf2f(unsigned short b) { return __builtin_bit_cast(float, (unsigned)b << 16); }
DI float asf(unsigned u) { return __builtin_bit_cast(float, u); }
DI unsigned asu(float f) { return __builtin_bit_cast(unsigned, f); }
DI float bflo(unsigned u) { return __builtin_bit_cast(float, u << 16); }
DI float bfhi(unsigned u) { return __builtin_bit_cast(float, u & 0xFFFF0000u); }
DI unsigned cvtpk(float lo, float hi) {
  f32x2 v = {lo, hi};
  bf16x2_t b = __builtin_convertvector(v, bf16x2_t);
  return __builtin_bit_cast(unsigned, b);
}
DI float bfr(float x) { return bflo(cvtpk(x, x)); }
DI unsigned cvtpkh(float lo, float hi) {
  f32x2 v = {lo, hi};
  h16x2 b = __builtin_convertvector(v, h16x2);
  return __builtin_bit_cast(unsigned, b);
}
DI f32x4 mfma_bf(v4u a, v4u b, f32x4 c) {
  return __builtin_amdgcn_mfma_f32_16x16x32_bf16(__builtin_bit_cast(s16x8, a), __builtin_bit_cast(s16x8, b), c, 0, 0, 0);
}
DI f32x4 mfma_h(v4u a, v4u b, f32x4 c) {
  return __builtin_amdgcn_mfma_f32_16x16x32_f16(__builtin_bit_cast(h16x8, a), __builtin_bit_cast(h16x8, b), c, 0, 0, 0);
}
DI float dot2bf(unsigned a, unsigned b, float acc) {
  return __builtin_amdgcn_fdot2_f32_bf16(__builtin_bit_cast(bf16x2_t, a), __builtin_bit_cast(bf16x2_t, b), acc, false);
}
DI v2u tr_read(LAS unsigned char* p) {
  typedef short v4i16_t __attribute__((ext_vector_type(4)));
  return __builtin_bit_cast(v2u, __builtin_amdgcn_ds_read_tr16_b64_v4i16((LAS v4i16_t*)p));
}
template <int CTRL> DI float dppf(float v) { return asf((unsigned)__builtin_amdgcn_update_dpp(0, (int)asu(v), CTRL, 0xF, 0xF, true)); }
DI float sum8(float v) { v += dppf<0xB1>(v); v += dppf<0x4E>(v); v += dppf<0x141>(v); return v; }
DI float sum_rows(float v) {
  auto a = __builtin_amdgcn_permlane16_swap(asu(v), asu(v), false, false);
  v = asf(a[0]) + asf(a[1]);
  auto c = __builtin_amdgcn_permlane32_swap(asu(v), asu(v), false, false);
  return asf(c[0]) + asf(c[1]);
}
DI float wave_max(float m) {
  m = fmaxf(m, dppf<0xB1>(m)); m = fmaxf(m, dppf<0x4E>(m)); m = fmaxf(m, dppf<0x141>(m)); m = fmaxf(m, dppf<0x140>(m));
  m = fmaxf(m, asf((unsigned)__builtin_amdgcn_update_dpp((int)asu(m), (int)asu(m), 0x142, 0xA, 0xF, false)));
  m = fmaxf(m, asf((unsigned)__builtin_amdgcn_update_dpp((int)asu(m), (int)asu(m), 0x143, 0xC, 0xF, false)));
  return asf((unsigned)__builtin_amdgcn_readlane((int)asu(m), 63));
}
DI f32x2 gelu2(f32x2 x) {
  const f32x2 c0 = {-2.3022081986f, -2.3022081986f}, c1 = {-0.10294324f, -0.10294324f}, one = {1.0f, 1.0f};
  const f32x2 e = x * __builtin_elementwise_fma(x * x, c1, c0);
  const f32x2 d = f32x2{__builtin_amdgcn_exp2f(e[0]), __builtin_amdgcn_exp2f(e[1])} + one;
  return x * f32x2{__builtin_amdgcn_rcpf(d[0]), __builtin_amdgcn_rcpf(d[1])};
}
DI void ln_scalars(float sum, float sumsq, float& m, float& s) {
  float mean = sum * (1.0f / D);
  float var = __builtin_fmaf(-mean, mean, sumsq * (1.0f / D));
  var = var < 0.f ? 0.f : var;
  m = bfr(mean);
  s = bfr(__builtin_amdgcn_sqrtf(bfr(bfr(var) + 1e-5f)));
}
constexpr int SC1 = 16;
DI v4u ld16(rsrc_t rs, int voff, int soff = 0) { return __builtin_amdgcn_raw_buffer_load_b128(rs, voff, soff, SC1); }
DI void dma16(rsrc_t rs, LAS unsigned char* dst, int voff) { __builtin_amdgcn_raw_ptr_buffer_load_lds(rs, dst, 16, voff, 0, 0, SC1); }
DI void st16(rsrc_t rs, v4u v, int voff, int soff = 0) { __builtin_amdgcn_raw_buffer_store_b128(v, rs, voff, soff, SC1); }
DI void st4(rsrc_t rs, unsigned v, int voff, int soff = 0) { __builtin_amdgcn_raw_buffer_store_b32(v, rs, voff, soff, SC1); }
#define DRAIN() asm volatile("s_waitcnt vmcnt(0)" ::: "memory")
#define ORDER() __builtin_amdgcn_fence(__ATOMIC_ACQUIRE, "wavefront")
constexpr unsigned SPIN_CAP = 1u << 15;

constexpr int PSLEEP = 8;
DI void poll_tag(gu64* p, u64 tag) {
  for (unsigned spins = 0; spins < SPIN_CAP; ++spins) {
    if (__all(__hip_atomic_load(p, RLX) == tag)) break;
    __builtin_amdgcn_s_sleep(PSLEEP);
  }
  ORDER();
}

struct KIdx { unsigned short v[2080]; };
constexpr KIdx make_kidx() {
  KIdx t{};
  for (int y = 0; y < 2080; ++y) {
    if (y >= 2048) { t.v[y] = 768; continue; }
    int j = 2047 - y;
    int v = j;
    if (j >= 256) {
      int sh = j >= 1024 ? 3 : (j >= 512 ? 2 : 1);
      int step = 1 << sh, lo = (j >> sh) << sh, rem = j - lo, half = step >> 1;
      if (rem > half || (rem == half && (((lo >> sh) & 1) != 0))) lo += step;
      v = lo;
    }
    int seg = v >= 1024 ? 3 : (v >= 512 ? 2 : (v >= 256 ? 1 : 0));
    int u = seg == 0 ? v : v - (128 << seg);
    int rb = seg == 0 ? 0 : (seg == 1 ? 16 : (seg == 2 ? 24 : 32));
    int row = rb + ((u & 7) >> seg) + (u >> 7) * (8 >> seg);
    int col = (u >> 3) & 15;
    t.v[y] = (unsigned short)(row * 16 + col);
  }
  return t;
}
__device__ const KIdx kIdxTab = make_kidx();

struct cplx { float r, i; };
DI cplx cmul(cplx a, cplx b) {
  const f32x2 t = f32x2{-a.i, a.i} * f32x2{b.i, b.r};
  const f32x2 r = __builtin_elementwise_fma(f32x2{a.r, a.r}, f32x2{b.r, b.i}, t);
  return cplx{r[0], r[1]};
}

DI cplx cexp_r(float a2, float th, float r) {
  float m = __builtin_amdgcn_exp2f(a2 * r);
  float ang = __builtin_amdgcn_fractf(th * r);
  return cplx{m * __builtin_amdgcn_cosf(ang), m * __builtin_amdgcn_sinf(ang)};
}

constexpr int CV_TAB = 57 * 256;
constexpr int CV_CH = 2 * CV_TAB;
constexpr int CV_KC = 4160;
constexpr int CV_XCH = 4 * CV_KC;
constexpr int CV_KCT = 152 * 1024;
constexpr int CV_U = 4 * CV_CH;
constexpr int LDS_BYTES = 160 * 1024;
constexpr int CV_MISC = LDS_BYTES - 512;
static_assert(4 * CV_KC + 8192 <= CV_CH, "conv channel region");
constexpr int PR_ROW = 1168;
constexpr int PR_U = CV_U, PR_X = CV_U + 8 * PR_ROW;
constexpr int ROWB = 1152;
constexpr int G_A = 0;
constexpr int G_ST = 73728;
constexpr int G_X = G_ST + 4096;
constexpr int G_H = G_X + 12 * 2048;
constexpr int G_O = G_H + 12 * 1024;
constexpr int G_OROW = 208;
constexpr int ML_A = 0, ML_H = 64 * ROWB, ML_ST = 2 * 64 * ROWB;
constexpr int ML_CS = ML_ST + 576;
static_assert(CV_U + 288 * 128 <= CV_KCT && CV_KCT + 4 * 1600 <= CV_MISC && ML_CS + 2304 <= CV_MISC && G_O + 2 * 64 * G_OROW <= CV_MISC && PR_X + 8 * PR_ROW <= CV_MISC, "LDS");

DI void lds_arrive_wait(LAS unsigned* ctr, unsigned target, int lane) {
  asm volatile("s_waitcnt lgkmcnt(0)" ::: "memory");
  if (lane == 0) __hip_atomic_fetch_add(ctr, 1u, __ATOMIC_RELAXED, __HIP_MEMORY_SCOPE_WORKGROUP);
  while (__hip_atomic_load(ctr, __ATOMIC_RELAXED, __HIP_MEMORY_SCOPE_WORKGROUP) < target) __builtin_amdgcn_s_sleep(1);
  asm volatile("" ::: "memory");
}

DI void lds_arrive(LAS unsigned* ctr, int lane) {
  asm volatile("s_waitcnt lgkmcnt(0)" ::: "memory");
  if (lane == 0) __hip_atomic_fetch_add(ctr, 1u, __ATOMIC_RELAXED, __HIP_MEMORY_SCOPE_WORKGROUP);
  asm volatile("" ::: "memory");
}
DI bool drain_arrive_last(LAS unsigned* ctr, unsigned n, int lane) {
  DRAIN();
  unsigned old = 0;
  if (lane == 0) old = __hip_atomic_fetch_add(ctr, 1u, __ATOMIC_RELAXED, __HIP_MEMORY_SCOPE_WORKGROUP);
  return (unsigned)__builtin_amdgcn_readfirstlane(old) == n - 1;
}
DI void lds_wait(LAS unsigned* ctr, unsigned target) {
  while (__hip_atomic_load(ctr, __ATOMIC_RELAXED, __HIP_MEMORY_SCOPE_WORKGROUP) < target) __builtin_amdgcn_s_sleep(1);
  asm volatile("" ::: "memory");
}
DI void pretile_unit(LAS unsigned char* lds, rsrc_t rs, unsigned char* ws, int unit, u64 tag, const unsigned short* __restrict__ w1, const unsigned short* __restrict__ w2,
                     const unsigned short* __restrict__ w3, const unsigned short* __restrict__ w4) {
  const int tid = threadIdx.x, lane = tid & 63, wv = __builtin_amdgcn_readfirstlane(tid >> 6);
  const unsigned short* w;
  int ldw, dst, ksn, cs = -1;
  if (unit < 9) { w = w1 + unit * 64; ldw = D; dst = WS_W1T + unit * 4 * 18 * 1024; ksn = 18; cs = WS_CS1 + unit * 256; }
  else if (unit < 18) { w = w2 + (unit - 9) * 64; ldw = D; dst = WS_W2T + (unit - 9) * 4 * 18 * 1024; ksn = 18; }
  else if (unit < 90) { w = w3 + (unit - 18) * 64; ldw = F; dst = WS_W3T + (unit - 18) * 4 * 18 * 1024; ksn = 18; cs = WS_CS3 + (unit - 18) * 256; }
  else { int u = unit - 90, kc = u / 9, cg = u % 9; w = w4 + (size_t)kc * 576 * D + cg * 64; ldw = D; dst = WS_W4T + (cg * 4 * 144 + kc * 18) * 1024; ksn = 144; }
  v4u a[6];
#pragma unroll
  for (int i = 0; i < 6; ++i) {
    int p = tid + NT * i, r = p >> 3, c = p & 7;
    a[i] = *(const GAS v4u*)(w + (size_t)r * ldw + c * 8);
  }
  __syncthreads();
#pragma unroll
  for (int i = 0; i < 6; ++i) {
    int p = tid + NT * i, r = p >> 3, c = p & 7;
    int f = ((r >> 1) & 1) | (((r >> 3) & 1) << 1);
    *(LAS v4u*)(lds + r * 128 + ((((c >> 1) ^ f) << 1 | (c & 1)) << 4)) = a[i];
  }
  __syncthreads();
  const int ct = wv & 3, k0 = 6 * (wv >> 2);
  const int g = lane >> 4, q = (lane & 15) >> 2, p = lane & 3;
  const int f = ((q >> 1) & 1) | ((g & 1) << 1);
  float csum = 0.f;
  const unsigned ones = 0x3F803F80u;
#pragma unroll
  for (int ks = 0; ks < 6; ++ks) {
    LAS unsigned char* pa = lds + (32 * (k0 + ks) + 8 * g + q) * 128 + ((ct ^ f) << 5) + p * 8;
    v2u lo = tr_read(pa), hi = tr_read(pa + 4 * 128);
    v4u o = {lo.x, lo.y, hi.x, hi.y};
    st16(rs, o, lane * 16, dst + (ct * ksn + k0 + ks) * 1024);
    csum = dot2bf(o.x, ones, csum); csum = dot2bf(o.y, ones, csum); csum = dot2bf(o.z, ones, csum); csum = dot2bf(o.w, ones, csum);
  }
  if (cs >= 0) {
    csum = sum_rows(csum);
    LAS float* pc = (LAS float*)(lds + 73728);
    if (lane < 16) pc[wv * 16 + lane] = csum;
    __syncthreads();
    if (tid < 64) st4(rs, __builtin_bit_cast(unsigned, pc[tid] + pc[64 + tid] + pc[128 + tid]), cs + tid * 4);
  }
  DRAIN();
  __syncthreads();
  if (tid < 4) __hip_atomic_store((gu64*)(ws + WS_FPT) + tid * 256 + unit, tag, RLX);
}

constexpr int NC0 = 4, NC1 = 3, NC2 = 2;
template <int NC>
DI void mlp_second(LAS unsigned char* lds, rsrc_t rs, unsigned char* ws, unsigned short* __restrict__ out, u64 tag, int grp, int s, int oc0, int wi, int g,
                   LAS unsigned* mcnt, int lane, int tok, int q, int sw) {
  constexpr int DEP = NC == 4 ? 2 : 3;
  const int o4 = WS_W4T + (oc0 * 144 + 18 * s) * 1024;
  v4u bw[DEP][NC];
#pragma unroll
  for (int d = 0; d < DEP; ++d)
#pragma unroll
    for (int c = 0; c < NC; ++c) bw[d][c] = ld16(rs, lane * 16, o4 + (c * 144 + d) * 1024);
  f32x4 acc[4][NC];
#pragma unroll
  for (int r = 0; r < 4; ++r)
#pragma unroll
    for (int c = 0; c < NC; ++c) acc[r][c] = f32x4{0, 0, 0, 0};
  LAS unsigned char* hbase = lds + ML_H + tok * ROWB;
  v4u af[2][4];
#pragma unroll
  for (int k2 = 0; k2 < 18; ++k2) {
    if (k2 % 6 == 0) {
      lds_wait(mcnt + 1 + k2 / 6, 4);
#pragma unroll
      for (int r = 0; r < 4; ++r) af[k2 & 1][r] = *(LAS v4u*)(hbase + r * 16 * ROWB + (((4 * k2 + q) ^ sw) << 4));
    }
    if (k2 % 6 != 5) {
#pragma unroll
      for (int r = 0; r < 4; ++r) af[(k2 + 1) & 1][r] = *(LAS v4u*)(hbase + r * 16 * ROWB + (((4 * (k2 + 1) + q) ^ sw) << 4));
    }
    __builtin_amdgcn_sched_barrier(0);
#pragma unroll
    for (int c = 0; c < NC; ++c) {
#pragma unroll
      for (int r = 0; r < 4; ++r) acc[r][c] = mfma_bf(bw[k2 % DEP][c], af[k2 & 1][r], acc[r][c]);
      const int nk = k2 + DEP;
      if (nk < 18) bw[k2 % DEP][c] = ld16(rs, lane * 16, o4 + (c * 144 + nk) * 1024);
    }
    __builtin_amdgcn_sched_barrier(0);
  }
  const int rpo = s >> 2, slot0 = g == 0 ? 0 : (g == 1 ? NC0 : NC0 + NC1);
  LAS unsigned char* own = lds + ML_A + (rpo ^ 1) * 32 * ROWB + slot0 * 1024;
  {
    const int po = WS_PART + (s * 32 + grp) * 72 * 1024;
#pragma unroll
    for (int c = 0; c < NC; ++c)
#pragma unroll
      for (int rp = 0; rp < 2; ++rp) {
        unsigned x0 = cvtpk(acc[2 * rp][c][0], acc[2 * rp][c][1]), x1 = cvtpk(acc[2 * rp][c][2], acc[2 * rp][c][3]);
        unsigned y0 = cvtpk(acc[2 * rp + 1][c][0], acc[2 * rp + 1][c][1]), y1 = cvtpk(acc[2 * rp + 1][c][2], acc[2 * rp + 1][c][3]);
        auto s0 = __builtin_amdgcn_permlane16_swap(x0, y0, false, false);
        auto s1 = __builtin_amdgcn_permlane16_swap(x1, y1, false, false);
        const v4u o = {s0[0], s1[0], s0[1], s1[1]};
        if (rp == rpo && wi == (s & 3)) *(LAS v4u*)(own + c * 1024 + lane * 16) = o;
        else st16(rs, o, lane * 16, po + (rp * 36 + oc0 + c) * 1024);
      }
  }
  asm volatile("s_waitcnt lgkmcnt(0)" ::: "memory");
  if (drain_arrive_last(mcnt + 4 + g, 4, lane)) { if (lane == 0) __hip_atomic_store((gu64*)(ws + WS_F5) + (grp * 3 + g) * 8 + s, tag, RLX); }
  if (wi < NC) {
    if (wi == 0) { poll_tag((gu64*)(ws + WS_F5) + (grp * 3 + g) * 8 + (lane & 7), tag); lds_arrive(mcnt + 7 + g, lane); }
    else lds_wait(mcnt + 7 + g, 1);
    lds_wait(mcnt + 4 + g, 4);
    const int oc = (g == 0 ? 0 : (g == 1 ? 4 * NC0 : 4 * (NC0 + NC1))) + (s & 3) * NC + wi, u = rpo * 36 + oc;
    const int row = rpo * 32 + (q & 1) * 16 + tok;
    const int off = ((grp * 64 + row) * D + oc * 16 + 8 * (q >> 1)) * 2;
    v4u p[8];
#pragma unroll
    for (int k = 0; k < 7; ++k) p[k] = ld16(rs, lane * 16, WS_PART + ((((s + 1 + k) & 7) * 32 + grp) * 72 + u) * 1024);
    const v4u hv = ld16(rs, WS_HR + off);
    p[7] = *(LAS v4u*)(own + wi * 1024 + lane * 16);
    const v4u xv = *(LAS v4u*)(lds + ML_A + row * ROWB + (((2 * oc + (q >> 1)) ^ ((row >> 1) & 7)) << 4));
    v4u o;
#pragma unroll
    for (int j = 0; j < 4; ++j) {
      f32x2 sum = f32x2{bflo(xv[j]), bfhi(xv[j])} + f32x2{bflo(hv[j]), bfhi(hv[j])};
#pragma unroll
      for (int k = 0; k < 8; ++k) sum += f32x2{bflo(p[k][j]), bfhi(p[k][j])};
      o[j] = cvtpk(sum[0], sum[1]);
    }
    *(GAS v4u*)((GAS unsigned char*)out + off) = o;
  }
}

__global__ __launch_bounds__(NT, 3) void k_all(const unsigned short* __restrict__ x, const unsigned short* __restrict__ Are, const unsigned short* __restrict__ Aim,
                                               const unsigned short* __restrict__ Bre, const unsigned short* __restrict__ Bim, const unsigned short* __restrict__ Cre,
                                               const unsigned short* __restrict__ Cim, const unsigned short* __restrict__ logdt, const unsigned short* __restrict__ dskip,
                                               const unsigned short* __restrict__ w1, const unsigned short* __restrict__ w2, const unsigned short* __restrict__ w3,
                                               const unsigned short* __restrict__ w4, unsigned char* __restrict__ ws, unsigned short* __restrict__ out) {
  extern __shared__ __attribute__((aligned(16))) unsigned char smem[];
  LAS unsigned char* lds = (LAS unsigned char*)smem;
  const int tid = threadIdx.x, lane = tid & 63, wv = __builtin_amdgcn_readfirstlane(tid >> 6);
  const int b = blockIdx.x, xcd = b & 7, jj = b >> 3;
  const rsrc_t rs = __builtin_amdgcn_make_buffer_rsrc((void*)ws, 0, WS_END, 0x00020000);
  const bool is_conv = jj < 18, is_gemm = jj < 24;
  const int c0 = 4 * (18 * xcd + jj);
  if (tid < 16) ((LAS unsigned*)(lds + CV_MISC))[tid] = 0;
  asm volatile("s_waitcnt lgkmcnt(0)" ::: "memory");
  asm volatile("" ::"s"(x), "s"(Are), "s"(Aim), "s"(Bre), "s"(Bim), "s"(Cre), "s"(Cim), "s"(logdt), "s"(ws));
  const u64 epoch = __hip_atomic_load((gu64*)(ws + WS_CTRL), RLX);
#define GET_TAG() ((((u64)(unsigned)__builtin_amdgcn_readfirstlane((unsigned)(epoch >> 32)) << 32) | (unsigned)__builtin_amdgcn_readfirstlane((unsigned)epoch)) + 1)
  u64 tag;
  LAS unsigned* cnt = (LAS unsigned*)(lds + CV_MISC);
  LAS float* smx = (LAS float*)(lds + CV_MISC + 64);
  LAS float* kscs = (LAS float*)(lds + CV_MISC + 96);

  if (wv < 4) {
    if (wv < 3) {
      const int t8 = lane >> 3, sub = lane & 7;
      const GAS v4u* row = (const GAS v4u*)(x + (size_t)(b * 8 + t8) * D) + 24 * wv + sub;
      v4u a[3];
#pragma unroll
      for (int i = 0; i < 3; ++i) a[i] = row[8 * i];
      __builtin_amdgcn_sched_barrier(0);
      __builtin_amdgcn_s_barrier();
      tag = GET_TAG();
      float s = 0.f, ss = 0.f;
      const unsigned ones = 0x3F803F80u;
#pragma unroll
      for (int i = 0; i < 3; ++i) {
#pragma unroll
        for (int j = 0; j < 4; ++j) { s = dot2bf(a[i][j], ones, s); ss = dot2bf(a[i][j], a[i][j], ss); }
      }
#pragma unroll
      for (int o = 0; o < 1; ++o) { s = sum8(s); ss = sum8(ss); }
      LAS f32x2* part = (LAS f32x2*)(lds + CV_MISC + 128);
      if (sub == 0) part[wv * 8 + t8] = f32x2{s, ss};
#pragma unroll
      for (int i = 0; i < 3; ++i) *(LAS v4u*)(lds + PR_X + t8 * PR_ROW + (24 * wv + 8 * i + sub) * 16) = a[i];
      lds_arrive_wait(cnt + 4, 3, lane);
      {
        const f32x2 p0 = part[t8], p1 = part[8 + t8], p2 = part[16 + t8];
        float m, sd;
        ln_scalars(p0.x + p1.x + p2.x, p0.y + p1.y + p2.y, m, sd);
        const float r = __builtin_amdgcn_rcpf(sd), nmr = -m * r;
#pragma unroll
        for (int i = 0; i < 3; ++i) {
          v4u up;
#pragma unroll
          for (int j = 0; j < 4; ++j) {
            const f32x2 xv = {bflo(a[i][j]), bfhi(a[i][j])};
            const f32x2 uv = __builtin_elementwise_fma(xv, f32x2{r, r}, f32x2{nmr, nmr});
            up[j] = cvtpkh(uv[0], uv[1]);
          }
          *(LAS v4u*)(lds + PR_U + t8 * PR_ROW + (24 * wv + 8 * i + sub) * 16) = up;
        }
      }
      {
        const int g = lane >> 4, q = (lane & 15) >> 2, p = lane & 3;
        LAS unsigned char* pa = lds + ((p & 1) ? PR_X : PR_U) + q * PR_ROW + (192 * wv + 8 * g + 4 * (p >> 1)) * 2;
#pragma unroll
        for (int k = 0; k < 6; ++k) {
          v2u lo = tr_read(pa + k * 64), hi = tr_read(pa + k * 64 + 4 * PR_ROW);
          st16(rs, v4u{lo.x, lo.y, hi.x, hi.y}, lane * 16, WS_UX + b * 18432 + (6 * wv + k) * 1024);
        }
      }
      DRAIN();
      lds_arrive_wait(cnt + 4, 6, lane);
      if (tid < 8) __hip_atomic_store((gu64*)(ws + WS_F1) + tid * 256 + b, tag, RLX);
    } else {
      __builtin_amdgcn_s_barrier();
      tag = GET_TAG();
    }
    if (is_conv) {
      poll_tag((gu64*)(ws + WS_F1 + (b & 7) * 2048) + 64 * wv + lane, tag);
      if (wv == 3) lds_arrive_wait(cnt + 4, 6, 64);
#pragma unroll
      for (int i = 0; i < 8; ++i) {
        const int oct = 64 * wv + 8 * i + (lane >> 3);
        dma16(rs, lds + CV_U + (32 + 64 * wv + 8 * i) * 128, WS_UX + oct * 18432 + (c0 >> 2) * 128 + (((lane & 7) ^ ((oct >> 1) & 7)) << 4));
      }
      *(LAS v4u*)(lds + CV_U + tid * 16) = v4u{0, 0, 0, 0};
    }
  } else if (is_conv) {
    const int cl = (wv - 4) >> 1, half = wv & 1, ch = c0 + cl, n = half * 64 + lane;
    const int gi = ch * NS + n;
    const int t2 = half * 64 + lane;
    const unsigned short rld = logdt[ch], rar = Are[gi], rai = Aim[gi], rbr = Bre[gi], rbi = Bim[gi], rcr = Cre[gi], rci = Cim[gi];
    v4u kid[3];
    unsigned kid2[3];
#pragma unroll
    for (int i = 0; i < 3; ++i) {
      const int p = t2 + 128 * i < 259 ? t2 + 128 * i : 258;
      kid[i] = *(const GAS v4u*)(kIdxTab.v + p * 8);
      kid2[i] = *(const GAS unsigned*)(kIdxTab.v + p * 8 + 8);
    }
    __builtin_amdgcn_sched_barrier(0);
    __builtin_amdgcn_s_barrier();
    tag = GET_TAG();
    {
      const float dt = bfr(__builtin_amdgcn_exp2f(bf2f(rld) * 1.44269504089f));
      const float zr = dt * bf2f(rar), zi = dt * bf2f(rai);
      const float dBr = dt * bf2f(rbr), dBi = dt * bf2f(rbi);
      cplx W = cmul(cplx{bf2f(rcr), bf2f(rci)}, cplx{dBr, dBi});
      float smax = fmaxf(fabsf(W.r), fabsf(W.i));
      smax = wave_max(smax);
      const float inv = smax > 1e-30f ? __builtin_amdgcn_rcpf(smax) : 0.f;
      W.r *= inv; W.i *= inv;
      const float a2 = zr * 1.44269504089f, th = zi * 0.15915494309f;
      W.i = -W.i;
      const cplx e1 = cexp_r(a2, -th, 1.f), e8 = cexp_r(a2, th, 8.f), e128 = cexp_r(a2, -th, 128.f);
      const cplx e256 = cexp_r(a2, -th, 256.f), e512 = cexp_r(a2, -th, 512.f), e1024 = cexp_r(a2, -th, 1024.f);
      LAS unsigned char* chan = lds + cl * CV_CH;
      LAS unsigned char* tab = chan + half * CV_TAB;
      const int nsw = lane >> 2, nlo = (lane & 3) * 4;
#define PUT(row, val) *(LAS unsigned*)(tab + (row) * 256 + ((nsw ^ (((row) & 3) << 2)) << 4) + nlo) = (val)
#define PUTA(row, z) PUT(row, cvtpkh((z).r, (z).i))
#define PUTB(row, z) PUT(41 + (row), cvtpkh((z).r, (z).i))
      {
        cplx c[16];
        c[0] = cplx{1.f, 0.f}; c[1] = e8; c[2] = cmul(e8, e8); c[3] = cmul(c[2], e8); c[4] = cmul(c[2], c[2]);
        c[5] = cmul(c[4], c[1]); c[6] = cmul(c[4], c[2]); c[7] = cmul(c[4], c[3]); c[8] = cmul(c[4], c[4]);
#pragma unroll
        for (int j = 9; j < 16; ++j) c[j] = cmul(c[8], c[j - 8]);
#pragma unroll
        for (int j = 0; j < 16; ++j) PUTB(j, c[j]);
      }
      {
        const cplx e2 = cmul(e1, e1), e4 = cmul(e2, e2);
        cplx p[8];
        p[0] = W; p[1] = cmul(W, e1); p[2] = cmul(W, e2); p[3] = cmul(p[1], e2);
#pragma unroll
        for (int c = 4; c < 8; ++c) p[c] = cmul(p[c - 4], e4);
#pragma unroll
        for (int c = 0; c < 8; ++c) {
          const cplx qv = cmul(p[c], e128);
          PUTA(c, p[c]);
          PUTA(8 + c, qv);
          if ((c & 1) == 0) {
            PUTA(16 + (c >> 1), cmul(p[c], e256));
            PUTA(20 + (c >> 1), cmul(qv, e256));
          }
        }
        const cplx e384 = cmul(e256, e128);
        const cplx s0 = cmul(p[0], e512), s1 = cmul(p[4], e512);
        PUTA(24, s0); PUTA(25, s1);
        PUTA(26, cmul(s0, e128)); PUTA(27, cmul(s1, e128));
        PUTA(28, cmul(s0, e256)); PUTA(29, cmul(s1, e256));
        PUTA(30, cmul(s0, e384)); PUTA(31, cmul(s1, e384));
        const cplx t0 = cmul(W, e1024), t4 = cmul(t0, e512);
        PUTA(32, t0); PUTA(33, cmul(t0, e128)); PUTA(34, cmul(t0, e256)); PUTA(35, cmul(t0, e384));
        PUTA(36, t4); PUTA(37, cmul(t4, e128)); PUTA(38, cmul(t4, e256)); PUTA(39, cmul(t4, e384));
        PUTA(40, cmul(t0, e1024));
      }
#undef PUT
#undef PUTA
#undef PUTB
      if (lane == 0) smx[wv - 4] = smax;
      __builtin_amdgcn_s_setprio(2);
      lds_arrive_wait(cnt + cl, 2, lane);
      const float s0_ = smx[2 * cl], s1_ = smx[2 * cl + 1];
      const float sm = fmaxf(s0_, s1_);
      const float ksc = sm * (182.f / 1024.f);
      const float kinv = sm > 1e-30f ? __builtin_amdgcn_rcpf(ksc) : 0.f;
      LAS unsigned short* kc = (LAS unsigned short*)(lds + CV_KCT + cl * 1600);
      {
        const int r = lane & 15, h = lane >> 4;
        const float f0 = s0_ * kinv, f1 = s1_ * kinv;
#pragma unroll
        for (int tI = 0; tI < 2; ++tI) {
          if (tI == 1 && half == 1) break;
          const int t = half ? 1 : 2 * tI;
          f32x4 d0 = {0, 0, 0, 0}, d1 = {0, 0, 0, 0};
#pragma unroll
          for (int ks = 0; ks < 4; ++ks) {
            const int ob = (41 + r) * 256 + (((4 * ks + h) ^ (((41 + r) & 3) << 2)) << 4), oa = (16 * t + r) * 256 + (((4 * ks + h) ^ ((r & 3) << 2)) << 4);
            d0 = mfma_h(*(LAS v4u*)(chan + oa), *(LAS v4u*)(chan + ob), d0);
            d1 = mfma_h(*(LAS v4u*)(chan + CV_TAB + oa), *(LAS v4u*)(chan + CV_TAB + ob), d1);
          }
#pragma unroll
          for (int e = 0; e < 4; ++e) {
            const float v = __builtin_fmaf(d0[e], f0, d1[e] * f1);
            kc[(16 * t + 4 * h + e) * 16 + r] = (unsigned short)(cvtpkh(v, v) & 0xFFFFu);
          }
        }
        if (half == 1 && lane == 0) { kc[768] = 0; kscs[cl] = ksc; }
      }
      __builtin_amdgcn_s_setprio(0);
      lds_arrive_wait(cnt + cl, 4, lane);
#pragma unroll
      for (int i = 0; i < 3; ++i) {
        const int z = t2 + 128 * i;
        unsigned d[5];
#pragma unroll
        for (int j = 0; j < 4; ++j) d[j] = (unsigned)kc[kid[i][j] & 0xFFFFu] | ((unsigned)kc[kid[i][j] >> 16] << 16);
        d[4] = (unsigned)kc[kid2[i] & 0xFFFFu];
        if (z < 259) {
          LAS unsigned char* base = chan + z * 16;
          *(LAS v4u*)(base) = v4u{d[0], d[1], d[2], d[3]};
          *(LAS v4u*)(base + CV_KC) = v4u{__builtin_amdgcn_alignbit(d[1], d[0], 16), __builtin_amdgcn_alignbit(d[2], d[1], 16), __builtin_amdgcn_alignbit(d[3], d[2], 16), __builtin_amdgcn_alignbit(d[4], d[3], 16)};
        }
      }
    }
  } else {
    __builtin_amdgcn_s_barrier();
    tag = GET_TAG();
  }
  if (!is_conv) {
    const int idx = (jj - 18) * 8 + xcd;
    for (int i = 0; i < 3; ++i) __builtin_amdgcn_s_sleep(32);
    pretile_unit(lds, rs, ws, idx, tag, w1, w2, w3, w4);
    if (idx >= 48 && idx < 98) pretile_unit(lds, rs, ws, 112 + idx - 48, tag, w1, w2, w3, w4);
  }
  if (is_conv) {
    const float dsk = bf2f(dskip[c0 + (wv & 3)]);
    __syncthreads();
    const int cl = wv & 3, kh = (wv >> 2) & 1;
    f32x4 acc[8], base[4];
    float ksc = 0.f;
    if (wv < 8) {
#pragma unroll
      for (int t = 0; t < 8; ++t) acc[t] = f32x4{0, 0, 0, 0};
      const int bb = lane & 15, h = lane >> 4;
      const int cpy = (bb & 1) ^ 1;
      LAS unsigned char* abase = lds + cl * CV_CH + cpy * CV_KC + (2047 - 16 - bb + 8 * h - cpy) * 2 - 64 * 4 * kh;
      LAS unsigned char* bbase = lds + CV_U + (32 + 2 * (bb - 1) + h) * 128;
      v4u A[8], B[8];
#define LDA(i, rho) do { LAS unsigned* pa_ = (LAS unsigned*)(abase - 64 * ((rho) + 8 * (i))); A[i] = v4u{pa_[0], pa_[1], pa_[2], pa_[3]}; } while (0)
#define LDB(m, rho) B[m] = *(LAS v4u*)(bbase - 512 * (4 * kh + (rho)) + ((cl ^ ((bb - 1 + (h >> 1) - 2 * (4 * kh + (rho))) & 7)) << 4) + 4096 * (m))
#pragma unroll
      for (int i = 0; i < 8; ++i) { LDB(i, 0); LDA(i, 0); }
#pragma unroll
      for (int rho = 0; rho < 4; ++rho) {
#pragma unroll
        for (int i = 0; i < 8; ++i) {
#pragma unroll
          for (int m = 7 - i; m >= 0; --m) acc[i + m] = mfma_h(A[i], B[m], acc[i + m]);
          if (rho < 3) { LDA(i, rho + 1); LDB(7 - i, rho + 1); }
          __builtin_amdgcn_sched_barrier(0);
        }
      }
#undef LDA
#undef LDB
      LAS unsigned char* xo = lds + cl * CV_CH + CV_XCH + kh * 4096 + lane * 16;
#pragma unroll
      for (int t = 0; t < 4; ++t) {
        f32x4 lo = acc[t], hi = acc[4 + t];
        asm volatile("" : "+v"(lo), "+v"(hi));
        *(LAS f32x4*)(xo + t * 1024) = kh ? lo : hi;
        acc[t] = kh ? hi : lo;
      }
      const int a = lane & 15, q = lane >> 4;
#pragma unroll
      for (int t = 0; t < 4; ++t) {
        LAS unsigned char* po = lds + CV_U + (32 + 32 * (4 * kh + t) + 2 * a + (q >> 1)) * 128 + (q & 1) * 8;
        const v2u uu = *(LAS v2u*)(po + ((cl ^ (a & 7)) << 4));
        const v2u xx = *(LAS v2u*)(po + (((4 + cl) ^ (a & 7)) << 4));
#pragma unroll
        for (int e = 0; e < 2; ++e) {
          const unsigned uw = e ? uu.y : uu.x, xw = e ? xx.y : xx.x;
          const h16x2 u2 = __builtin_bit_cast(h16x2, uw);
          const f32x2 bv = __builtin_elementwise_fma(f32x2{(float)u2[0], (float)u2[1]}, f32x2{dsk, dsk}, f32x2{bflo(xw), bfhi(xw)});
          base[t][2 * e] = bv[0]; base[t][2 * e + 1] = bv[1];
        }
      }
      ksc = kscs[cl];
#pragma unroll
      for (int t = 0; t < 4; ++t) base[t] += acc[t] * ksc;
    }
    __syncthreads();
    if (wv < 8) {
      const int ch = c0 + cl;
      const int a = lane & 15, q = lane >> 4;
      LAS unsigned char* xi = lds + cl * CV_CH + CV_XCH + (kh ^ 1) * 4096 + lane * 16;
      f32x4 o[4];
#pragma unroll
      for (int t = 0; t < 4; ++t) o[t] = *(LAS f32x4*)(xi + t * 1024);
      unsigned pk[4][2];
#pragma unroll
      for (int t = 0; t < 4; ++t) {
        const f32x4 hv = base[t] + o[t] * ksc;
        pk[t][0] = cvtpk(hv[0], hv[1]);
        pk[t][1] = cvtpk(hv[2], hv[3]);
      }
#pragma unroll
      for (int t = 0; t < 4; t += 2) {
        auto s0 = __builtin_amdgcn_permlane16_swap(pk[t][0], pk[t + 1][0], false, false);
        auto s1 = __builtin_amdgcn_permlane16_swap(pk[t][1], pk[t + 1][1], false, false);
        const int tok = 256 * (4 * kh + t + (q & 1)) + 16 * a + 8 * (q >> 1);
        st16(rs, v4u{s0[0], s1[0], s0[1], s1[1]}, WS_HT + (ch * L + tok) * 2);
      }
      if (drain_arrive_last(cnt + 8, 8, lane)) { if (lane < 8) __hip_atomic_store((gu64*)(ws + WS_F2) + lane * 256 + (18 * xcd + jj), tag, RLX); }
    }
  }

  const int tt = 4 * xcd + jj / 6, sl = jj % 6;
  if (is_gemm) {
    const int cl = wv % 6, kh = wv / 6, ct = 6 * sl + cl;
    const int tok = lane & 15, q = lane >> 4;
    if (wv == 0) {
      const int u0 = (96 * sl) >> 6;
      poll_tag((gu64*)(ws + WS_FPT + (b & 3) * 2048) + (lane & 1) + u0 + ((lane & 2) ? 9 : 0), tag);
    }
    __syncthreads();
    v4u bw[9];
    if (wv != 0) {
#pragma unroll
      for (int ks = 0; ks < 9; ++ks) bw[ks] = ld16(rs, lane * 16, WS_W1T + (ct * 18 + 9 * kh + ks) * 1024);
    }
    int aoff[6];
#pragma unroll
    for (int i = 0; i < 6; ++i) {
      const int p = tid + NT * i, c = p >> 3, pp = p & 7;
      const int f = ((c >> 1) & 1) | (((c >> 3) & 1) << 1);
      aoff[i] = WS_HT + (c * L + tt * 64 + ((((pp >> 1) ^ f) << 1) | (pp & 1)) * 8) * 2;
      asm volatile("" : "+v"(aoff[i]));
    }
    if (wv == 0) {
      const int fo = WS_F2 + (b & 7) * 2048 + (lane < 36 ? lane : 0) * 32;
      const unsigned tl = (unsigned)tag, th = (unsigned)(tag >> 32);
      for (unsigned spins = 0; spins < SPIN_CAP; ++spins) {
        asm volatile("" ::: "memory");
        const v4u f0 = ld16(rs, fo), f1 = ld16(rs, fo + 16);
        if (__all(f0.x == tl && f0.y == th && f0.z == tl && f0.w == th && f1.x == tl && f1.y == th && f1.z == tl && f1.w == th)) break;
        __builtin_amdgcn_s_sleep(PSLEEP);
      }
      ORDER();
#pragma unroll
      for (int ks = 0; ks < 9; ++ks) bw[ks] = ld16(rs, lane * 16, WS_W1T + (ct * 18 + 9 * kh + ks) * 1024);
    }
    const f32x4 cs = __builtin_bit_cast(f32x4, ld16(rs, WS_CS1 + (ct * 16 + 4 * q) * 4));
    __syncthreads();
#pragma unroll
    for (int i = 0; i < 6; ++i) dma16(rs, lds + G_A + (wv * 64 + NT * i) * 16, aoff[i]);
    __syncthreads();
    v4u bw2[9];
    v4u hpiece;
    f32x4 acc[4];
#pragma unroll
    for (int r = 0; r < 4; ++r) acc[r] = f32x4{0, 0, 0, 0};
    {
      const int pq = (lane & 15) >> 2, pp = lane & 3;
      const int f = ((pq >> 1) & 1) | ((q & 1) << 1);
      const unsigned ones = 0x3F803F80u;
      LAS unsigned char* pa0 = lds + G_A + (8 * q + pq) * 128 + pp * 8;
      LAS unsigned char* par[4];
#pragma unroll
      for (int r = 0; r < 4; ++r) par[r] = pa0 + ((r ^ f) << 5);
      v4u af[2][4];
#pragma unroll
      for (int r = 0; r < 4; ++r) { v2u lo = tr_read(par[r] + 9 * kh * 4096), hi = tr_read(par[r] + 9 * kh * 4096 + 512); af[0][r] = v4u{lo.x, lo.y, hi.x, hi.y}; }
#pragma unroll
      for (int ks = 0; ks < 9; ++ks) {
        if (ks < 8) {
#pragma unroll
          for (int r = 0; r < 4; ++r) {
            v2u lo = tr_read(par[r] + (9 * kh + ks + 1) * 4096), hi = tr_read(par[r] + (9 * kh + ks + 1) * 4096 + 512);
            af[(ks + 1) & 1][r] = v4u{lo.x, lo.y, hi.x, hi.y};
          }
        }
        __builtin_amdgcn_sched_barrier(0);
#pragma unroll
        for (int r = 0; r < 4; ++r) acc[r] = mfma_bf(bw[ks], af[ks & 1][r], acc[r]);
        __builtin_amdgcn_sched_barrier(0);
      }
    }
#pragma unroll
    for (int ks = 0; ks < 9; ++ks) bw2[ks] = ld16(rs, lane * 16, WS_W2T + (ct * 18 + 9 * kh + ks) * 1024);
    hpiece = ld16(rs, WS_HT + ((ct * 16 + (lane >> 2)) * L + tt * 64 + kh * 32 + (lane & 3) * 8) * 2);
    __builtin_amdgcn_sched_barrier(0);
    {
      LAS unsigned char* xo = lds + G_X + wv * 2048 + lane * 16;
#pragma unroll
      for (int r = 0; r < 2; ++r) {
        f32x4 lo = acc[r], hi = acc[2 + r];
        asm volatile("" : "+v"(lo), "+v"(hi));
        *(LAS f32x4*)(xo + r * 1024) = kh ? lo : hi;
        acc[r] = kh ? hi : lo;
      }
    }
    if (wv < 4) {
      const int pq = (lane & 15) >> 2, pp = lane & 3;
      const int f = ((pq >> 1) & 1) | ((q & 1) << 1);
      LAS unsigned char* ps0 = lds + G_A + (8 * q + pq) * 128 + pp * 8 + ((wv ^ f) << 5);
      f32x4 dsum = {0, 0, 0, 0}, dsq = {0, 0, 0, 0};
      const v4u onesv = {0x3F803F80u, 0x3F803F80u, 0x3F803F80u, 0x3F803F80u};
#pragma unroll
      for (int hf = 0; hf < 2; ++hf) {
        v4u as[9];
#pragma unroll
        for (int i = 0; i < 9; ++i) { v2u lo = tr_read(ps0 + (9 * hf + i) * 4096), hi = tr_read(ps0 + (9 * hf + i) * 4096 + 512); as[i] = v4u{lo.x, lo.y, hi.x, hi.y}; }
#pragma unroll
        for (int i = 0; i < 9; ++i) { dsum = mfma_bf(onesv, as[i], dsum); dsq = mfma_bf(as[i], as[i], dsq); }
      }
      const int e = tok & 3;
      const float sq = e == 0 ? dsq[0] : (e == 1 ? dsq[1] : (e == 2 ? dsq[2] : dsq[3]));
      const float s_ = dsum[0], ss = sq;
      float m1, s1;
      ln_scalars(s_, ss, m1, s1);
      const float mean = s_ * (1.0f / D);
      float var = __builtin_fmaf(-mean, mean, ss * (1.0f / D));
      var = var < 0.f ? 0.f : var;
      const float r1 = __builtin_amdgcn_rcpf(s1);
      const float m2 = bfr((mean - m1) * r1);
      const float s2 = bfr(__builtin_amdgcn_sqrtf(bfr(bfr(var * r1 * r1) + 1e-5f)));
      if (q == (tok >> 2)) *(LAS f32x4*)(lds + G_ST + 2048 + (wv * 16 + tok) * 16) = f32x4{__builtin_fmaf(m2, s1, m1), r1 * __builtin_amdgcn_rcpf(s2), m1, r1};
    }
    __syncthreads();
    f32x2 st1[2];
    {
      LAS unsigned char* xi = lds + G_X + (kh ? wv - 6 : wv + 6) * 2048 + lane * 16;
      f32x4 o[2], sc[2];
#pragma unroll
      for (int r = 0; r < 2; ++r) { o[r] = *(LAS f32x4*)(xi + r * 1024); sc[r] = *(LAS f32x4*)(lds + G_ST + 2048 + ((2 * kh + r) * 16 + tok) * 16); }
      __builtin_amdgcn_sched_barrier(0);
      unsigned pk[2][2];
#pragma unroll
      for (int r = 0; r < 2; ++r) {
        st1[r] = f32x2{sc[r][2], sc[r][3]};
        const f32x4 v = (acc[r] + o[r] - cs * sc[r][0]) * sc[r][1];
        const f32x2 ga = gelu2(f32x2{v[0], v[1]}), gb = gelu2(f32x2{v[2], v[3]});
        pk[r][0] = cvtpk(ga[0], ga[1]);
        pk[r][1] = cvtpk(gb[0], gb[1]);
      }
#pragma unroll
      for (int r = 0; r < 2; ++r) *(LAS v2u*)(lds + G_O + ((2 * kh + r) * 16 + tok) * G_OROW + cl * 32 + q * 8) = v2u{pk[r][0], pk[r][1]};
    }
    const int orow = tid / 12, ocol = tid % 12;
    __syncthreads();
    st16(rs, *(LAS v4u*)(lds + G_O + orow * G_OROW + ocol * 16), WS_H1 + ((tt * 64 + orow) * D + sl * 96 + ocol * 8) * 2);
    if (drain_arrive_last(cnt + 9, 12, lane)) { if (lane == 0) __hip_atomic_store((gu64*)(ws + WS_F3) + tt * 8 + sl, tag, RLX); }

    {
      const int c = lane >> 2, pc = lane & 3;
      *(LAS v4u*)(lds + G_H + wv * 1024 + c * 64 + ((((pc >> 1) ^ ((c >> 3) & 1)) << 1 | (pc & 1)) << 4)) = hpiece;
    }
#pragma unroll
    for (int i = 0; i < 6; ++i) {
      const int p = tid + NT * i, r = p / 72, c = (p % 72) ^ ((r >> 1) & 7);
      const int cg = c / 18;
      const int sh = cg == 0 ? -1 : (cg == 1 ? 1 : (cg == 2 ? -2 : 2));
      const int t = tt * 64 + r + sh;
      aoff[i] = (t >= 0 && t < L) ? WS_H1 + (t * D + c * 8) * 2 : WS_END;
      asm volatile("" : "+v"(aoff[i]));
    }
    if (wv == 0) {
      int t = tt - 1 + (lane < 18 ? lane / 6 : 1);
      t = t < 0 ? 0 : (t > 31 ? 31 : t);
      poll_tag((gu64*)(ws + WS_F3) + t * 8 + (lane < 18 ? lane % 6 : 0), tag);
    }
    __syncthreads();
#pragma unroll
    for (int i = 0; i < 6; ++i) dma16(rs, lds + G_A + (wv * 64 + NT * i) * 16, aoff[i]);
    __syncthreads();
#pragma unroll
    for (int r = 0; r < 4; ++r) acc[r] = f32x4{0, 0, 0, 0};
    {
      const int sw = (tok >> 1) & 7;
      LAS unsigned char* pa = lds + G_A + tok * ROWB;
      v4u af[2][4];
#pragma unroll
      for (int r = 0; r < 4; ++r) af[0][r] = *(LAS v4u*)(pa + r * 16 * ROWB + (((4 * 9 * kh + q) ^ sw) << 4));
#pragma unroll
      for (int ks = 0; ks < 9; ++ks) {
        if (ks < 8) {
#pragma unroll
          for (int r = 0; r < 4; ++r) af[(ks + 1) & 1][r] = *(LAS v4u*)(pa + r * 16 * ROWB + (((4 * (9 * kh + ks + 1) + q) ^ sw) << 4));
        }
        __builtin_amdgcn_sched_barrier(0);
#pragma unroll
        for (int r = 0; r < 4; ++r) acc[r] = mfma_bf(bw2[ks], af[ks & 1][r], acc[r]);
        __builtin_amdgcn_sched_barrier(0);
      }
    }
    v2u hh[2];
    {
      LAS unsigned char* xo = lds + G_X + wv * 2048 + lane * 16;
#pragma unroll
      for (int r = 0; r < 2; ++r) {
        f32x4 lo = acc[r], hi = acc[2 + r];
        asm volatile("" : "+v"(lo), "+v"(hi));
        *(LAS f32x4*)(xo + r * 1024) = kh ? lo : hi;
        acc[r] = kh ? hi : lo;
      }
#pragma unroll
      for (int r = 0; r < 2; ++r) {
        const int c = 4 * q + ((lane & 15) >> 2);
        hh[r] = tr_read(lds + G_H + wv * 1024 + c * 64 + ((r ^ ((c >> 3) & 1)) << 5) + (lane & 3) * 8);
        const float m1 = st1[r][0], r1 = st1[r][1];
        const f32x4 hv = {bflo(hh[r].x), bfhi(hh[r].x), bflo(hh[r].y), bfhi(hh[r].y)};
        acc[r] += (hv - m1) * r1;
        *(LAS v2u*)(lds + G_O + 64 * G_OROW + ((2 * kh + r) * 16 + tok) * G_OROW + cl * 32 + q * 8) = hh[r];
      }
    }
    __syncthreads();
    {
      LAS unsigned char* xi = lds + G_X + (kh ? wv - 6 : wv + 6) * 2048 + lane * 16;
      f32x4 o[2];
#pragma unroll
      for (int r = 0; r < 2; ++r) o[r] = *(LAS f32x4*)(xi + r * 1024);
      unsigned pk[2][2];
#pragma unroll
      for (int r = 0; r < 2; ++r) {
        const f32x4 v = acc[r] + o[r];
        pk[r][0] = cvtpk(v[0], v[1]);
        pk[r][1] = cvtpk(v[2], v[3]);
      }
#pragma unroll
      for (int r = 0; r < 2; ++r) *(LAS v2u*)(lds + G_O + ((2 * kh + r) * 16 + tok) * G_OROW + cl * 32 + q * 8) = v2u{pk[r][0], pk[r][1]};
    }
    __syncthreads();
    {
      const int off = ((tt * 64 + orow) * D + sl * 96 + ocol * 8) * 2;
      st16(rs, *(LAS v4u*)(lds + G_O + orow * G_OROW + ocol * 16), WS_X1 + off);
      st16(rs, *(LAS v4u*)(lds + G_O + 64 * G_OROW + orow * G_OROW + ocol * 16), WS_HR + off);
    }
    if (drain_arrive_last(cnt + 10, 12, lane)) { if (lane == 0) __hip_atomic_store((gu64*)(ws + WS_F4) + tt * 8 + sl, tag, RLX); }
  }

  const int grp = 2 * (jj >> 1) + (xcd >> 2), s = 2 * (xcd & 3) + (jj & 1);
  {
    constexpr int DEPTH = 3;
    if (wv == 0) {
      poll_tag((gu64*)(ws + WS_FPT + (b & 3) * 2048) + (lane < 9 ? 18 + 9 * s + lane : (lane < 18 ? 90 + 9 * s + lane - 9 : 18 + 9 * s)), tag);
    }
    __syncthreads();
    const int o3 = WS_W3T + (36 * s + 3 * wv) * 18 * 1024;
    LAS unsigned* mcnt = (LAS unsigned*)(lds + ML_ST + 512);
    v4u bw[DEPTH][3];
    const v4u csl = ld16(rs, WS_CS3 + 36 * 16 * 4 * s + (tid < 144 ? tid : 0) * 16);
    if (wv != 0) {
#pragma unroll
      for (int d = 0; d < DEPTH; ++d)
#pragma unroll
        for (int c = 0; c < 3; ++c) bw[d][c] = ld16(rs, lane * 16, o3 + (c * 18 + d) * 1024);
    }
    if (tid < 144) *(LAS v4u*)(lds + ML_CS + tid * 16) = csl;
    if (tid >= 704 && tid < 714) mcnt[tid - 704] = 0;
    int xoff[6];
#pragma unroll
    for (int i = 0; i < 6; ++i) {
      const int p = tid + NT * i, r = p / 72, c = (p % 72) ^ ((r >> 1) & 7);
      xoff[i] = WS_X1 + grp * 64 * ROWB + r * ROWB + c * 16;
      asm volatile("" : "+v"(xoff[i]));
    }
    if (wv == 0) {
      poll_tag((gu64*)(ws + WS_F4) + grp * 8 + (lane < 6 ? lane : 0), tag);
#pragma unroll
      for (int d = 0; d < DEPTH; ++d)
#pragma unroll
        for (int c = 0; c < 3; ++c) bw[d][c] = ld16(rs, lane * 16, o3 + (c * 18 + d) * 1024);
    }
    __syncthreads();
#pragma unroll
    for (int i = 0; i < 6; ++i) dma16(rs, lds + ML_A + (wv * 64 + NT * i) * 16, xoff[i]);
    __syncthreads();
    const int tok = lane & 15, q = lane >> 4;
    const int sw = (tok >> 1) & 7;
    const int wg = wv >> 2, wi = wv & 3;
    LAS unsigned char* abase = lds + ML_A + tok * ROWB;
    if (wv < 4) {
      f32x4 dsum = {0, 0, 0, 0}, dsq = {0, 0, 0, 0};
      const v4u onesv = {0x3F803F80u, 0x3F803F80u, 0x3F803F80u, 0x3F803F80u};
      LAS unsigned char* pst = abase + wv * 16 * ROWB;
      v4u as[18];
#pragma unroll
      for (int ks = 0; ks < 18; ++ks) as[ks] = *(LAS v4u*)(pst + (((4 * ks + q) ^ sw) << 4));
#pragma unroll
      for (int ks = 0; ks < 18; ++ks) { dsum = mfma_bf(onesv, as[ks], dsum); dsq = mfma_bf(as[ks], as[ks], dsq); }
      const int e = tok & 3;
      const float sq = e == 0 ? dsq[0] : (e == 1 ? dsq[1] : (e == 2 ? dsq[2] : dsq[3]));
      float m, sd;
      ln_scalars(dsum[0], sq, m, sd);
      if (q == (tok >> 2)) *(LAS f32x2*)(lds + ML_ST + (wv * 16 + tok) * 8) = f32x2{m, __builtin_amdgcn_rcpf(sd)};
      lds_arrive(mcnt, lane);
    }
    LAS unsigned char* H = lds + ML_H;
    {
      f32x4 acc[4][3];
#pragma unroll
      for (int r = 0; r < 4; ++r)
#pragma unroll
        for (int c = 0; c < 3; ++c) acc[r][c] = f32x4{0, 0, 0, 0};
      __builtin_amdgcn_s_setprio(2);
      v4u af[2][4];
#pragma unroll
      for (int r = 0; r < 4; ++r) af[0][r] = *(LAS v4u*)(abase + r * 16 * ROWB + ((q ^ sw) << 4));
#pragma unroll
      for (int ks = 0; ks < 18; ++ks) {
        if (ks < 17) {
#pragma unroll
          for (int r = 0; r < 4; ++r) af[(ks + 1) & 1][r] = *(LAS v4u*)(abase + r * 16 * ROWB + (((4 * (ks + 1) + q) ^ sw) << 4));
        }
        __builtin_amdgcn_sched_barrier(0);
#pragma unroll
        for (int c = 0; c < 3; ++c) {
#pragma unroll
          for (int r = 0; r < 4; ++r) acc[r][c] = mfma_bf(bw[ks % DEPTH][c], af[ks & 1][r], acc[r][c]);
          const int nk = ks + DEPTH;
          if (nk < 18) bw[ks % DEPTH][c] = ld16(rs, lane * 16, o3 + (c * 18 + nk) * 1024);
        }
        __builtin_amdgcn_sched_barrier(0);
      }
      __builtin_amdgcn_s_setprio(3);
      lds_wait(mcnt, 4);
      f32x2 st[4];
#pragma unroll
      for (int r = 0; r < 4; ++r) st[r] = *(LAS f32x2*)(lds + ML_ST + (r * 16 + tok) * 8);
#pragma unroll
      for (int c = 0; c < 3; ++c) {
        const f32x4 cs = *(LAS f32x4*)(lds + ML_CS + ((3 * wv + c) * 16 + 4 * q) * 4);
#pragma unroll
        for (int r = 0; r < 4; ++r) {
          const f32x2 nm = {-st[r].x, -st[r].x}, rr = {st[r].y, st[r].y};
          const f32x2 ga = gelu2(__builtin_elementwise_fma(nm, f32x2{cs[0], cs[1]}, f32x2{acc[r][c][0], acc[r][c][1]}) * rr);
          const f32x2 gb = gelu2(__builtin_elementwise_fma(nm, f32x2{cs[2], cs[3]}, f32x2{acc[r][c][2], acc[r][c][3]}) * rr);
          v2u o = {cvtpk(ga[0], ga[1]), cvtpk(gb[0], gb[1])};
          const int chunk = 2 * (3 * wv + c) + (q >> 1);
          *(LAS v2u*)(H + (r * 16 + tok) * ROWB + ((chunk ^ sw) << 4) + (q & 1) * 8) = o;
        }
      }
      lds_arrive(mcnt + 1 + wg, lane);
      if (wg == 0) __builtin_amdgcn_s_setprio(0); else if (wg == 1) __builtin_amdgcn_s_setprio(1); else __builtin_amdgcn_s_setprio(2);
    }
    if (wg == 0) mlp_second<NC0>(lds, rs, ws, out, tag, grp, s, NC0 * wi, wi, 0, mcnt, lane, tok, q, sw);
    else if (wg == 1) mlp_second<NC1>(lds, rs, ws, out, tag, grp, s, 4 * NC0 + NC1 * wi, wi, 1, mcnt, lane, tok, q, sw);
    else mlp_second<NC2>(lds, rs, ws, out, tag, grp, s, 4 * (NC0 + NC1) + NC2 * wi, wi, 2, mcnt, lane, tok, q, sw);
  }
  if (b == 0 && tid == 0) __hip_atomic_store((gu64*)(ws + WS_CTRL), tag, RLX);
}

extern "C" void kernel_launch(void* const* d_in, const int* in_sizes, int n_in, void* d_out, int out_size, void* d_ws, size_t ws_size,
                              hipStream_t stream) {
  (void)hipFuncSetAttribute((const void*)k_all, hipFuncAttributeMaxDynamicSharedMemorySize, LDS_BYTES);
  const unsigned short* const* in = (const unsigned short* const*)d_in;
  k_all<<<256, NT, LDS_BYTES, stream>>>(in[0], in[1], in[2], in[3], in[4], in[5], in[6], in[7], in[8], in[10], in[11], in[12], in[13], (unsigned char*)d_ws,
                                        (unsigned short*)d_out);
}
```

```cpp
#include <hip/hip_runtime.h>
#include <stdint.h>

typedef unsigned long long u64;
typedef unsigned v4u __attribute__((ext_vector_type(4)));
typedef unsigned v2u __attribute__((ext_vector_type(2)));
typedef float f32x4 __attribute__((ext_vector_type(4)));
typedef float f32x2 __attribute__((ext_vector_type(2)));
typedef short s16x8 __attribute__((ext_vector_type(8)));
typedef _Float16 h16x8 __attribute__((ext_vector_type(8)));
typedef _Float16 h16x2 __attribute__((ext_vector_type(2)));
typedef __bf16 bf16x2_t __attribute__((ext_vector_type(2)));
#define GAS __attribute__((address_space(1)))
#define LAS __attribute__((address_space(3)))
#define DI __device__ __forceinline__
#define RLX __ATOMIC_RELAXED, __HIP_MEMORY_SCOPE_AGENT
typedef GAS u64 gu64;
typedef __amdgpu_buffer_rsrc_t rsrc_t;

constexpr int L = 2048, D = 576, NS = 128, F = 4608;
constexpr int NT = 768;

constexpr int WS_CTRL = 0;
constexpr int WS_F1 = 4096;
constexpr int WS_FPT = WS_F1 + 8 * 2048;
constexpr int WS_F2 = WS_FPT + 4 * 2048;
constexpr int WS_F3 = WS_F2 + 8 * 2048;
constexpr int WS_F4 = WS_F3 + 32 * 64;
constexpr int WS_F5 = WS_F4 + 32 * 64;
constexpr int WS_W1T = 131072;
constexpr int WS_W2T = WS_W1T + 36 * 18 * 1024;
constexpr int WS_W3T = WS_W2T + 36 * 18 * 1024;
constexpr int WS_W4T = WS_W3T + 288 * 18 * 1024;
constexpr int WS_CS1 = WS_W4T + 36 * 144 * 1024;
constexpr int WS_CS3 = WS_CS1 + 4096;
constexpr int WS_HT = WS_CS3 + 4608 * 4;
constexpr int WS_H1 = WS_HT + D * L * 2;
constexpr int WS_X1 = WS_H1 + D * L * 2;
constexpr int WS_HR = WS_X1 + D * L * 2;
constexpr int WS_PART = WS_HR + D * L * 2;
constexpr int WS_UX = WS_PART + 8 * D * L * 2;
constexpr int WS_END = WS_UX + 256 * 144 * 128;
static_assert(WS_F5 + 32 * 192 <= WS_W1T, "control block");


DI float bf2f(unsigned short b) { return __builtin_bit_cast(float, (unsigned)b << 16); }
DI float asf(unsigned u) { return __builtin_bit_cast(float, u); }
DI unsigned asu(float f) { return __builtin_bit_cast(unsigned, f); }
DI float bflo(unsigned u) { return __builtin_bit_cast(float, u << 16); }
DI float bfhi(unsigned u) { return __builtin_bit_cast(float, u & 0xFFFF0000u); }
DI unsigned cvtpk(float lo, float hi) {
  f32x2 v = {lo, hi};
  bf16x2_t b = __builtin_convertvector(v, bf16x2_t);
  return __builtin_bit_cast(unsigned, b);
}
DI float bfr(float x) { return bflo(cvtpk(x, x)); }
DI unsigned cvtpkh(float lo, float hi) {
  f32x2 v = {lo, hi};
  h16x2 b = __builtin_convertvector(v, h16x2);
  return __builtin_bit_cast(unsigned, b);
}
DI f32x4 mfma_bf(v4u a, v4u b, f32x4 c) {
  return __builtin_amdgcn_mfma_f32_16x16x32_bf16(__builtin_bit_cast(s16x8, a), __builtin_bit_cast(s16x8, b), c, 0, 0, 0);
}
DI f32x4 mfma_h(v4u a, v4u b, f32x4 c) {
  return __builtin_amdgcn_mfma_f32_16x16x32_f16(__builtin_bit_cast(h16x8, a), __builtin_bit_cast(h16x8, b), c, 0, 0, 0);
}
DI float dot2bf(unsigned a, unsigned b, float acc) {
  return __builtin_amdgcn_fdot2_f32_bf16(__builtin_bit_cast(bf16x2_t, a), __builtin_bit_cast(bf16x2_t, b), acc, false);
}
DI v2u tr_read(LAS unsigned char* p) {
  typedef short v4i16_t __attribute__((ext_vector_type(4)));
  return __builtin_bit_cast(v2u, __builtin_amdgcn_ds_read_tr16_b64_v4i16((LAS v4i16_t*)p));
}
template <int CTRL> DI float dppf(float v) { return asf((unsigned)__builtin_amdgcn_update_dpp(0, (int)asu(v), CTRL, 0xF, 0xF, true)); }
DI float sum8(float v) { v += dppf<0xB1>(v); v += dppf<0x4E>(v); v += dppf<0x141>(v); return v; }
DI float sum_rows(float v) {
  auto a = __builtin_amdgcn_permlane16_swap(asu(v), asu(v), false, false);
  v = asf(a[0]) + asf(a[1]);
  auto c = __builtin_amdgcn_permlane32_swap(asu(v), asu(v), false, false);
  return asf(c[0]) + asf(c[1]);
}
DI float wave_max(float m) {
  m = fmaxf(m, dppf<0xB1>(m)); m = fmaxf(m, dppf<0x4E>(m)); m = fmaxf(m, dppf<0x141>(m)); m = fmaxf(m, dppf<0x140>(m));
  m = fmaxf(m, asf((unsigned)__builtin_amdgcn_update_dpp((int)asu(m), (int)asu(m), 0x142, 0xA, 0xF, false)));
  m = fmaxf(m, asf((unsigned)__builtin_amdgcn_update_dpp((int)asu(m), (int)asu(m), 0x143, 0xC, 0xF, false)));
  return asf((unsigned)__builtin_amdgcn_readlane((int)asu(m), 63));
}
DI f32x2 gelu2(f32x2 x) {
  const f32x2 c0 = {-2.3022081986f, -2.3022081986f}, c1 = {-0.10294324f, -0.10294324f}, one = {1.0f, 1.0f};
  const f32x2 e = x * __builtin_elementwise_fma(x * x, c1, c0);
  const f32x2 d = f32x2{__builtin_amdgcn_exp2f(e[0]), __builtin_amdgcn_exp2f(e[1])} + one;
  return x * f32x2{__builtin_amdgcn_rcpf(d[0]), __builtin_amdgcn_rcpf(d[1])};
}
DI void ln_scalars(float sum, float sumsq, float& m, float& s) {
  float mean = sum * (1.0f / D);
  float var = __builtin_fmaf(-mean, mean, sumsq * (1.0f / D));
  var = var < 0.f ? 0.f : var;
  m = bfr(mean);
  s = bfr(__builtin_amdgcn_sqrtf(bfr(bfr(var) + 1e-5f)));
}
constexpr int SC1 = 16;
DI v4u ld16(rsrc_t rs, int voff, int soff = 0) { return __builtin_amdgcn_raw_buffer_load_b128(rs, voff, soff, SC1); }
DI void dma16(rsrc_t rs, LAS unsigned char* dst, int voff) { __builtin_amdgcn_raw_ptr_buffer_load_lds(rs, dst, 16, voff, 0, 0, SC1); }
DI void st16(rsrc_t rs, v4u v, int voff, int soff = 0) { __builtin_amdgcn_raw_buffer_store_b128(v, rs, voff, soff, SC1); }
DI void st4(rsrc_t rs, unsigned v, int voff, int soff = 0) { __builtin_amdgcn_raw_buffer_store_b32(v, rs, voff, soff, SC1); }
#define DRAIN() asm volatile("s_waitcnt vmcnt(0)" ::: "memory")
#define ORDER() __builtin_amdgcn_fence(__ATOMIC_ACQUIRE, "wavefront")
constexpr unsigned SPIN_CAP = 1u << 15;

constexpr int PSLEEP = 8;
DI void poll_tag(gu64* p, u64 tag) {
  for (unsigned spins = 0; spins < SPIN_CAP; ++spins) {
    if (__all(__hip_atomic_load(p, RLX) == tag)) break;
    __builtin_amdgcn_s_sleep(PSLEEP);
  }
  ORDER();
}

struct KIdx { unsigned short v[2080]; };
constexpr KIdx make_kidx() {
  KIdx t{};
  for (int y = 0; y < 2080; ++y) {
    if (y >= 2048) { t.v[y] = 768; continue; }
    int j = 2047 - y;
    int v = j;
    if (j >= 256) {
      int sh = j >= 1024 ? 3 : (j >= 512 ? 2 : 1);
      int step = 1 << sh, lo = (j >> sh) << sh, rem = j - lo, half = step >> 1;
      if (rem > half || (rem == half && (((lo >> sh) & 1) != 0))) lo += step;
      v = lo;
    }
    int seg = v >= 1024 ? 3 : (v >= 512 ? 2 : (v >= 256 ? 1 : 0));
    int u = seg == 0 ? v : v - (128 << seg);
    int rb = seg == 0 ? 0 : (seg == 1 ? 16 : (seg == 2 ? 24 : 32));
    int row = rb + ((u & 7) >> seg) + (u >> 7) * (8 >> seg);
    int col = (u >> 3) & 15;
    t.v[y] = (unsigned short)(row * 16 + col);
  }
  return t;
}
__device__ const KIdx kIdxTab = make_kidx();

struct cplx { float r, i; };
DI cplx cmul(cplx a, cplx b) {
  const f32x2 t = f32x2{-a.i, a.i} * f32x2{b.i, b.r};
  const f32x2 r = __builtin_elementwise_fma(f32x2{a.r, a.r}, f32x2{b.r, b.i}, t);
  return cplx{r[0], r[1]};
}

DI cplx cexp_r(float a2, float th, float r) {
  float m = __builtin_amdgcn_exp2f(a2 * r);
  float ang = __builtin_amdgcn_fractf(th * r);
  return cplx{m * __builtin_amdgcn_cosf(ang), m * __builtin_amdgcn_sinf(ang)};
}

constexpr int CV_TAB = 57 * 256;
constexpr int CV_CH = 2 * CV_TAB;
constexpr int CV_KC = 4160;
constexpr int CV_XCH = 4 * CV_KC;
constexpr int CV_KCT = 152 * 1024;
constexpr int CV_U = 4 * CV_CH;
constexpr int LDS_BYTES = 160 * 1024;
constexpr int CV_MISC = LDS_BYTES - 512;
static_assert(4 * CV_KC + 8192 <= CV_CH, "conv channel region");
constexpr int PR_ROW = 1168;
constexpr int PR_U = CV_U, PR_X = CV_U + 8 * PR_ROW;
constexpr int ROWB = 1152;
constexpr int G_A = 0;
constexpr int G_ST = 73728;
constexpr int G_X = G_ST + 4096;
constexpr int G_H = G_X + 12 * 2048;
constexpr int G_O = G_H + 12 * 1024;
constexpr int G_OROW = 208;
constexpr int ML_A = 0, ML_H = 64 * ROWB, ML_ST = 2 * 64 * ROWB;
constexpr int ML_CS = ML_ST + 576;
static_assert(CV_U + 288 * 128 <= CV_KCT && CV_KCT + 4 * 1600 <= CV_MISC && ML_CS + 2304 <= CV_MISC && G_O + 2 * 64 * G_OROW <= CV_MISC && PR_X + 8 * PR_ROW <= CV_MISC, "LDS");

DI void lds_arrive_wait(LAS unsigned* ctr, unsigned target, int lane) {
  asm volatile("s_waitcnt lgkmcnt(0)" ::: "memory");
  if (lane == 0) __hip_atomic_fetch_add(ctr, 1u, __ATOMIC_RELAXED, __HIP_MEMORY_SCOPE_WORKGROUP);
  while (__hip_atomic_load(ctr, __ATOMIC_RELAXED, __HIP_MEMORY_SCOPE_WORKGROUP) < target) __builtin_amdgcn_s_sleep(1);
  asm volatile("" ::: "memory");
}

DI void lds_arrive(LAS unsigned* ctr, int lane) {
  asm volatile("s_waitcnt lgkmcnt(0)" ::: "memory");
  if (lane == 0) __hip_atomic_fetch_add(ctr, 1u, __ATOMIC_RELAXED, __HIP_MEMORY_SCOPE_WORKGROUP);
  asm volatile("" ::: "memory");
}
DI bool drain_arrive_last(LAS unsigned* ctr, unsigned n, int lane) {
  DRAIN();
  unsigned old = 0;
  if (lane == 0) old = __hip_atomic_fetch_add(ctr, 1u, __ATOMIC_RELAXED, __HIP_MEMORY_SCOPE_WORKGROUP);
  return (unsigned)__builtin_amdgcn_readfirstlane(old) == n - 1;
}
DI void lds_wait(LAS unsigned* ctr, unsigned target) {
  while (__hip_atomic_load(ctr, __ATOMIC_RELAXED, __HIP_MEMORY_SCOPE_WORKGROUP) < target) __builtin_amdgcn_s_sleep(1);
  asm volatile("" ::: "memory");
}
DI void pretile_unit(LAS unsigned char* lds, rsrc_t rs, unsigned char* ws, int unit, u64 tag, const unsigned short* __restrict__ w1, const unsigned short* __restrict__ w2,
                     const unsigned short* __restrict__ w3, const unsigned short* __restrict__ w4) {
  const int tid = threadIdx.x, lane = tid & 63, wv = __builtin_amdgcn_readfirstlane(tid >> 6);
  const unsigned short* w;
  int ldw, dst, ksn, cs = -1;
  if (unit < 9) { w = w1 + unit * 64; ldw = D; dst = WS_W1T + unit * 4 * 18 * 1024; ksn = 18; cs = WS_CS1 + unit * 256; }
  else if (unit < 18) { w = w2 + (unit - 9) * 64; ldw = D; dst = WS_W2T + (unit - 9) * 4 * 18 * 1024; ksn = 18; }
  else if (unit < 90) { w = w3 + (unit - 18) * 64; ldw = F; dst = WS_W3T + (unit - 18) * 4 * 18 * 1024; ksn = 18; cs = WS_CS3 + (unit - 18) * 256; }
  else { int u = unit - 90, kc = u / 9, cg = u % 9; w = w4 + (size_t)kc * 576 * D + cg * 64; ldw = D; dst = WS_W4T + (cg * 4 * 144 + kc * 18) * 1024; ksn = 144; }
  v4u a[6];
#pragma unroll
  for (int i = 0; i < 6; ++i) {
    int p = tid + NT * i, r = p >> 3, c = p & 7;
    a[i] = *(const GAS v4u*)(w + (size_t)r * ldw + c * 8);
  }
  __syncthreads();
#pragma unroll
  for (int i = 0; i < 6; ++i) {
    int p = tid + NT * i, r = p >> 3, c = p & 7;
    int f = ((r >> 1) & 1) | (((r >> 3) & 1) << 1);
    *(LAS v4u*)(lds + r * 128 + ((((c >> 1) ^ f) << 1 | (c & 1)) << 4)) = a[i];
  }
  __syncthreads();
  const int ct = wv & 3, k0 = 6 * (wv >> 2);
  const int g = lane >> 4, q = (lane & 15) >> 2, p = lane & 3;
  const int f = ((q >> 1) & 1) | ((g & 1) << 1);
  float csum = 0.f;
  const unsigned ones = 0x3F803F80u;
#pragma unroll
  for (int ks = 0; ks < 6; ++ks) {
    LAS unsigned char* pa = lds + (32 * (k0 + ks) + 8 * g + q) * 128 + ((ct ^ f) << 5) + p * 8;
    v2u lo = tr_read(pa), hi = tr_read(pa + 4 * 128);
    v4u o = {lo.x, lo.y, hi.x, hi.y};
    st16(rs, o, lane * 16, dst + (ct * ksn + k0 + ks) * 1024);
    csum = dot2bf(o.x, ones, csum); csum = dot2bf(o.y, ones, csum); csum = dot2bf(o.z, ones, csum); csum = dot2bf(o.w, ones, csum);
  }
  if (cs >= 0) {
    csum = sum_rows(csum);
    LAS float* pc = (LAS float*)(lds + 73728);
    if (lane < 16) pc[wv * 16 + lane] = csum;
    __syncthreads();
    if (tid < 64) st4(rs, __builtin_bit_cast(unsigned, pc[tid] + pc[64 + tid] + pc[128 + tid]), cs + tid * 4);
  }
  DRAIN();
  __syncthreads();
  if (tid < 4) __hip_atomic_store((gu64*)(ws + WS_FPT) + tid * 256 + unit, tag, RLX);
}

constexpr int NC0 = 4, NC1 = 3, NC2 = 2;
template <int NC>
DI void mlp_second(LAS unsigned char* lds, rsrc_t rs, unsigned char* ws, unsigned short* __restrict__ out, u64 tag, int grp, int s, int oc0, int wi, int g,
                   LAS unsigned* mcnt, int lane, int tok, int q, int sw) {
  constexpr int DEP = NC == 4 ? 2 : 3;
  const int o4 = WS_W4T + (oc0 * 144 + 18 * s) * 1024;
  v4u bw[DEP][NC];
#pragma unroll
  for (int d = 0; d < DEP; ++d)
#pragma unroll
    for (int c = 0; c < NC; ++c) bw[d][c] = ld16(rs, lane * 16, o4 + (c * 144 + d) * 1024);
  f32x4 acc[4][NC];
#pragma unroll
  for (int r = 0; r < 4; ++r)
#pragma unroll
    for (int c = 0; c < NC; ++c) acc[r][c] = f32x4{0, 0, 0, 0};
  LAS unsigned char* hbase = lds + ML_H + tok * ROWB;
  v4u af[2][4];
#pragma unroll
  for (int k2 = 0; k2 < 18; ++k2) {
    if (k2 % 6 == 0) {
      lds_wait(mcnt + 1 + k2 / 6, 4);
#pragma unroll
      for (int r = 0; r < 4; ++r) af[k2 & 1][r] = *(LAS v4u*)(hbase + r * 16 * ROWB + (((4 * k2 + q) ^ sw) << 4));
    }
    if (k2 % 6 != 5) {
#pragma unroll
      for (int r = 0; r < 4; ++r) af[(k2 + 1) & 1][r] = *(LAS v4u*)(hbase + r * 16 * ROWB + (((4 * (k2 + 1) + q) ^ sw) << 4));
    }
    __builtin_amdgcn_sched_barrier(0);
#pragma unroll
    for (int c = 0; c < NC; ++c) {
#pragma unroll
      for (int r = 0; r < 4; ++r) acc[r][c] = mfma_bf(bw[k2 % DEP][c], af[k2 & 1][r], acc[r][c]);
      const int nk = k2 + DEP;
      if (nk < 18) bw[k2 % DEP][c] = ld16(rs, lane * 16, o4 + (c * 144 + nk) * 1024);
    }
    __builtin_amdgcn_sched_barrier(0);
  }
  const int rpo = s >> 2, slot0 = g == 0 ? 0 : (g == 1 ? NC0 : NC0 + NC1);
  LAS unsigned char* own = lds + ML_A + (rpo ^ 1) * 32 * ROWB + slot0 * 1024;
  {
    const int po = WS_PART + (s * 32 + grp) * 72 * 1024;
#pragma unroll
    for (int c = 0; c < NC; ++c)
#pragma unroll
      for (int rp = 0; rp < 2; ++rp) {
        unsigned x0 = cvtpk(acc[2 * rp][c][0], acc[2 * rp][c][1]), x1 = cvtpk(acc[2 * rp][c][2], acc[2 * rp][c][3]);
        unsigned y0 = cvtpk(acc[2 * rp + 1][c][0], acc[2 * rp + 1][c][1]), y1 = cvtpk(acc[2 * rp + 1][c][2], acc[2 * rp + 1][c][3]);
        auto s0 = __builtin_amdgcn_permlane16_swap(x0, y0, false, false);
        auto s1 = __builtin_amdgcn_permlane16_swap(x1, y1, false, false);
        const v4u o = {s0[0], s1[0], s0[1], s1[1]};
        if (rp == rpo && wi == (s & 3)) *(LAS v4u*)(own + c * 1024 + lane * 16) = o;
        else st16(rs, o, lane * 16, po + (rp * 36 + oc0 + c) * 1024);
      }
  }
  asm volatile("s_waitcnt lgkmcnt(0)" ::: "memory");
  if (drain_arrive_last(mcnt + 4 + g, 4, lane)) { if (lane == 0) __hip_atomic_store((gu64*)(ws + WS_F5) + (grp * 3 + g) * 8 + s, tag, RLX); }
  if (wi < NC) {
    if (wi == 0) { poll_tag((gu64*)(ws + WS_F5) + (grp * 3 + g) * 8 + (lane & 7), tag); lds_arrive(mcnt + 7 + g, lane); }
    else lds_wait(mcnt + 7 + g, 1);
    lds_wait(mcnt + 4 + g, 4);
    const int oc = (g == 0 ? 0 : (g == 1 ? 4 * NC0 : 4 * (NC0 + NC1))) + (s & 3) * NC + wi, u = rpo * 36 + oc;
    const int row = rpo * 32 + (q & 1) * 16 + tok;
    const int off = ((grp * 64 + row) * D + oc * 16 + 8 * (q >> 1)) * 2;
    v4u p[8];
#pragma unroll
    for (int k = 0; k < 7; ++k) p[k] = ld16(rs, lane * 16, WS_PART + ((((s + 1 + k) & 7) * 32 + grp) * 72 + u) * 1024);
    const v4u hv = ld16(rs, WS_HR + off);
    p[7] = *(LAS v4u*)(own + wi * 1024 + lane * 16);
    const v4u xv = *(LAS v4u*)(lds + ML_A + row * ROWB + (((2 * oc + (q >> 1)) ^ ((row >> 1) & 7)) << 4));
    v4u o;
#pragma unroll
    for (int j = 0; j < 4; ++j) {
      f32x2 sum = f32x2{bflo(xv[j]), bfhi(xv[j])} + f32x2{bflo(hv[j]), bfhi(hv[j])};
#pragma unroll
      for (int k = 0; k < 8; ++k) sum += f32x2{bflo(p[k][j]), bfhi(p[k][j])};
      o[j] = cvtpk(sum[0], sum[1]);
    }
    *(GAS v4u*)((GAS unsigned char*)out + off) = o;
  }
}

__global__ __launch_bounds__(NT, 3) void k_all(const unsigned short* __restrict__ x, const unsigned short* __restrict__ Are, const unsigned short* __restrict__ Aim,
                                               const unsigned short* __restrict__ Bre, const unsigned short* __restrict__ Bim, const unsigned short* __restrict__ Cre,
                                               const unsigned short* __restrict__ Cim, const unsigned short* __restrict__ logdt, const unsigned short* __restrict__ dskip,
                                               const unsigned short* __restrict__ w1, const unsigned short* __restrict__ w2, const unsigned short* __restrict__ w3,
                                               const unsigned short* __restrict__ w4, unsigned char* __restrict__ ws, unsigned short* __restrict__ out) {
  extern __shared__ __attribute__((aligned(16))) unsigned char smem[];
  LAS unsigned char* lds = (LAS unsigned char*)smem;
  const int tid = threadIdx.x, lane = tid & 63, wv = __builtin_amdgcn_readfirstlane(tid >> 6);
  const int b = blockIdx.x, xcd = b & 7, jj = b >> 3;
  const rsrc_t rs = __builtin_amdgcn_make_buffer_rsrc((void*)ws, 0, WS_END, 0x00020000);
  const bool is_conv = jj < 18, is_gemm = jj < 24;
  const int c0 = 4 * (18 * xcd + jj);
  if (tid < 16) ((LAS unsigned*)(lds + CV_MISC))[tid] = 0;
  asm volatile("s_waitcnt lgkmcnt(0)" ::: "memory");
  asm volatile("" ::"s"(x), "s"(Are), "s"(Aim), "s"(Bre), "s"(Bim), "s"(Cre), "s"(Cim), "s"(logdt), "s"(ws));
  const u64 epoch = __hip_atomic_load((gu64*)(ws + WS_CTRL), RLX);
#define GET_TAG() ((((u64)(unsigned)__builtin_amdgcn_readfirstlane((unsigned)(epoch >> 32)) << 32) | (unsigned)__builtin_amdgcn_readfirstlane((unsigned)epoch)) + 1)
  u64 tag;
  LAS unsigned* cnt = (LAS unsigned*)(lds + CV_MISC);
  LAS float* smx = (LAS float*)(lds + CV_MISC + 64);
  LAS float* kscs = (LAS float*)(lds + CV_MISC + 96);

  if (wv < 4) {
    if (wv < 3) {
      const int t8 = lane >> 3, sub = lane & 7;
      const GAS v4u* row = (const GAS v4u*)(x + (size_t)(b * 8 + t8) * D) + 24 * wv + sub;
      v4u a[3];
#pragma unroll
      for (int i = 0; i < 3; ++i) a[i] = row[8 * i];
      __builtin_amdgcn_sched_barrier(0);
      __builtin_amdgcn_s_barrier();
      tag = GET_TAG();
      float s = 0.f, ss = 0.f;
      const unsigned ones = 0x3F803F80u;
#pragma unroll
      for (int i = 0; i < 3; ++i) {
#pragma unroll
        for (int j = 0; j < 4; ++j) { s = dot2bf(a[i][j], ones, s); ss = dot2bf(a[i][j], a[i][j], ss); }
      }
#pragma unroll
      for (int o = 0; o < 1; ++o) { s = sum8(s); ss = sum8(ss); }
      LAS f32x2* part = (LAS f32x2*)(lds + CV_MISC + 128);
      if (sub == 0) part[wv * 8 + t8] = f32x2{s, ss};
#pragma unroll
      for (int i = 0; i < 3; ++i) *(LAS v4u*)(lds + PR_X + t8 * PR_ROW + (24 * wv + 8 * i + sub) * 16) = a[i];
      lds_arrive_wait(cnt + 4, 3, lane);
      {
        const f32x2 p0 = part[t8], p1 = part[8 + t8], p2 = part[16 + t8];
        float m, sd;
        ln_scalars(p0.x + p1.x + p2.x, p0.y + p1.y + p2.y, m, sd);
        const float r = __builtin_amdgcn_rcpf(sd), nmr = -m * r;
#pragma unroll
        for (int i = 0; i < 3; ++i) {
          v4u up;
#pragma unroll
          for (int j = 0; j < 4; ++j) {
            const f32x2 xv = {bflo(a[i][j]), bfhi(a[i][j])};
            const f32x2 uv = __builtin_elementwise_fma(xv, f32x2{r, r}, f32x2{nmr, nmr});
            up[j] = cvtpkh(uv[0], uv[1]);
          }
          *(LAS v4u*)(lds + PR_U + t8 * PR_ROW + (24 * wv + 8 * i + sub) * 16) = up;
        }
      }
      {
        const int g = lane >> 4, q = (lane & 15) >> 2, p = lane & 3;
        LAS unsigned char* pa = lds + ((p & 1) ? PR_X : PR_U) + q * PR_ROW + (192 * wv + 8 * g + 4 * (p >> 1)) * 2;
#pragma unroll
        for (int k = 0; k < 6; ++k) {
          v2u lo = tr_read(pa + k * 64), hi = tr_read(pa + k * 64 + 4 * PR_ROW);
          st16(rs, v4u{lo.x, lo.y, hi.x, hi.y}, lane * 16, WS_UX + b * 18432 + (6 * wv + k) * 1024);
        }
      }
      DRAIN();
      lds_arrive_wait(cnt + 4, 6, lane);
      if (tid < 8) __hip_atomic_store((gu64*)(ws + WS_F1) + tid * 256 + b, tag, RLX);
    } else {
      __builtin_amdgcn_s_barrier();
      tag = GET_TAG();
    }
    if (is_conv) {
      poll_tag((gu64*)(ws + WS_F1 + (b & 7) * 2048) + 64 * wv + lane, tag);
      if (wv == 3) lds_arrive_wait(cnt + 4, 6, 64);
#pragma unroll
      for (int i = 0; i < 8; ++i) {
        const int oct = 64 * wv + 8 * i + (lane >> 3);
        dma16(rs, lds + CV_U + (32 + 64 * wv + 8 * i) * 128, WS_UX + oct * 18432 + (c0 >> 2) * 128 + (((lane & 7) ^ ((oct >> 1) & 7)) << 4));
      }
      *(LAS v4u*)(lds + CV_U + tid * 16) = v4u{0, 0, 0, 0};
    }
  } else if (is_conv) {
    const int cl = (wv - 4) >> 1, half = wv & 1, ch = c0 + cl, n = half * 64 + lane;
    const int gi = ch * NS + n;
    const int t2 = half * 64 + lane;
    const unsigned short rld = logdt[ch], rar = Are[gi], rai = Aim[gi], rbr = Bre[gi], rbi = Bim[gi], rcr = Cre[gi], rci = Cim[gi];
    v4u kid[3];
    unsigned kid2[3];
#pragma unroll
    for (int i = 0; i < 3; ++i) {
      const int p = t2 + 128 * i < 259 ? t2 + 128 * i : 258;
      kid[i] = *(const GAS v4u*)(kIdxTab.v + p * 8);
      kid2[i] = *(const GAS unsigned*)(kIdxTab.v + p * 8 + 8);
    }
    __builtin_amdgcn_sched_barrier(0);
    __builtin_amdgcn_s_barrier();
    tag = GET_TAG();
    {
      const float dt = bfr(__builtin_amdgcn_exp2f(bf2f(rld) * 1.44269504089f));
      const float zr = dt * bf2f(rar), zi = dt * bf2f(rai);
      const float dBr = dt * bf2f(rbr), dBi = dt * bf2f(rbi);
      cplx W = cmul(cplx{bf2f(rcr), bf2f(rci)}, cplx{dBr, dBi});
      float smax = fmaxf(fabsf(W.r), fabsf(W.i));
      smax = wave_max(smax);
      const float inv = smax > 1e-30f ? __builtin_amdgcn_rcpf(smax) : 0.f;
      W.r *= inv; W.i *= inv;
      const float a2 = zr * 1.44269504089f, th = zi * 0.15915494309f;
      W.i = -W.i;
      const cplx e1 = cexp_r(a2, -th, 1.f), e8 = cexp_r(a2, th, 8.f), e128 = cexp_r(a2, -th, 128.f);
      const cplx e256 = cexp_r(a2, -th, 256.f), e512 = cexp_r(a2, -th, 512.f), e1024 = cexp_r(a2, -th, 1024.f);
      LAS unsigned char* chan = lds + cl * CV_CH;
      LAS unsigned char* tab = chan + half * CV_TAB;
      const int nsw = lane >> 2, nlo = (lane & 3) * 4;
#define PUT(row, val) *(LAS unsigned*)(tab + (row) * 256 + ((nsw ^ (((row) & 3) << 2)) << 4) + nlo) = (val)
#define PUTA(row, z) PUT(row, cvtpkh((z).r, (z).i))
#define PUTB(row, z) PUT(41 + (row), cvtpkh((z).r, (z).i))
      {
        cplx c[16];
        c[0] = cplx{1.f, 0.f}; c[1] = e8; c[2] = cmul(e8, e8); c[3] = cmul(c[2], e8); c[4] = cmul(c[2], c[2]);
        c[5] = cmul(c[4], c[1]); c[6] = cmul(c[4], c[2]); c[7] = cmul(c[4], c[3]); c[8] = cmul(c[4], c[4]);
#pragma unroll
        for (int j = 9; j < 16; ++j) c[j] = cmul(c[8], c[j - 8]);
#pragma unroll
        for (int j = 0; j < 16; ++j) PUTB(j, c[j]);
      }
      {
        const cplx e2 = cmul(e1, e1), e4 = cmul(e2, e2);
        cplx p[8];
        p[0] = W; p[1] = cmul(W, e1); p[2] = cmul(W, e2); p[3] = cmul(p[1], e2);
#pragma unroll
        for (int c = 4; c < 8; ++c) p[c] = cmul(p[c - 4], e4);
#pragma unroll
        for (int c = 0; c < 8; ++c) {
          const cplx qv = cmul(p[c], e128);
          PUTA(c, p[c]);
          PUTA(8 + c, qv);
          if ((c & 1) == 0) {
            PUTA(16 + (c >> 1), cmul(p[c], e256));
            PUTA(20 + (c >> 1), cmul(qv, e256));
          }
        }
        const cplx e384 = cmul(e256, e128);
        const cplx s0 = cmul(p[0], e512), s1 = cmul(p[4], e512);
        PUTA(24, s0); PUTA(25, s1);
        PUTA(26, cmul(s0, e128)); PUTA(27, cmul(s1, e128));
        PUTA(28, cmul(s0, e256)); PUTA(29, cmul(s1, e256));
        PUTA(30, cmul(s0, e384)); PUTA(31, cmul(s1, e384));
        const cplx t0 = cmul(W, e1024), t4 = cmul(t0, e512);
        PUTA(32, t0); PUTA(33, cmul(t0, e128)); PUTA(34, cmul(t0, e256)); PUTA(35, cmul(t0, e384));
        PUTA(36, t4); PUTA(37, cmul(t4, e128)); PUTA(38, cmul(t4, e256)); PUTA(39, cmul(t4, e384));
        PUTA(40, cmul(t0, e1024));
      }
#undef PUT
#undef PUTA
#undef PUTB
      if (lane == 0) smx[wv - 4] = smax;
      __builtin_amdgcn_s_setprio(2);
      lds_arrive_wait(cnt + cl, 2, lane);
      const float s0_ = smx[2 * cl], s1_ = smx[2 * cl + 1];
      const float sm = fmaxf(s0_, s1_);
      const float ksc = sm * (182.f / 1024.f);
      const float kinv = sm > 1e-30f ? __builtin_amdgcn_rcpf(ksc) : 0.f;
      LAS unsigned short* kc = (LAS unsigned short*)(lds + CV_KCT + cl * 1600);
      {
        const int r = lane & 15, h = lane >> 4;
        const float f0 = s0_ * kinv, f1 = s1_ * kinv;
#pragma unroll
        for (int tI = 0; tI < 2; ++tI) {
          if (tI == 1 && half == 1) break;
          const int t = half ? 1 : 2 * tI;
          f32x4 d0 = {0, 0, 0, 0}, d1 = {0, 0, 0, 0};
#pragma unroll
          for (int ks = 0; ks < 4; ++ks) {
            const int ob = (41 + r) * 256 + (((4 * ks + h) ^ (((41 + r) & 3) << 2)) << 4), oa = (16 * t + r) * 256 + (((4 * ks + h) ^ ((r & 3) << 2)) << 4);
            d0 = mfma_h(*(LAS v4u*)(chan + oa), *(LAS v4u*)(chan + ob), d0);
            d1 = mfma_h(*(LAS v4u*)(chan + CV_TAB + oa), *(LAS v4u*)(chan + CV_TAB + ob), d1);
          }
#pragma unroll
          for (int e = 0; e < 4; ++e) {
            const float v = __builtin_fmaf(d0[e], f0, d1[e] * f1);
            kc[(16 * t + 4 * h + e) * 16 + r] = (unsigned short)(cvtpkh(v, v) & 0xFFFFu);
          }
        }
        if (half == 1 && lane == 0) { kc[768] = 0; kscs[cl] = ksc; }
      }
      __builtin_amdgcn_s_setprio(0);
      lds_arrive_wait(cnt + cl, 4, lane);
#pragma unroll
      for (int i = 0; i < 3; ++i) {
        const int z = t2 + 128 * i;
        unsigned d[5];
#pragma unroll
        for (int j = 0; j < 4; ++j) d[j] = (unsigned)kc[kid[i][j] & 0xFFFFu] | ((unsigned)kc[kid[i][j] >> 16] << 16);
        d[4] = (unsigned)kc[kid2[i] & 0xFFFFu];
        if (z < 259) {
          LAS unsigned char* base = chan + z * 16;
          *(LAS v4u*)(base) = v4u{d[0], d[1], d[2], d[3]};
          *(LAS v4u*)(base + CV_KC) = v4u{__builtin_amdgcn_alignbit(d[1], d[0], 16), __builtin_amdgcn_alignbit(d[2], d[1], 16), __builtin_amdgcn_alignbit(d[3], d[2], 16), __builtin_amdgcn_alignbit(d[4], d[3], 16)};
        }
      }
    }
  } else {
    __builtin_amdgcn_s_barrier();
    tag = GET_TAG();
  }
  if (!is_conv) {
    const int idx = (jj - 18) * 8 + xcd;
    for (int i = 0; i < 2; ++i) __builtin_amdgcn_s_sleep(32);
    pretile_unit(lds, rs, ws, idx, tag, w1, w2, w3, w4);
    if (idx >= 48 && idx < 98) pretile_unit(lds, rs, ws, 112 + idx - 48, tag, w1, w2, w3, w4);
  }
  if (is_conv) {
    const float dsk = bf2f(dskip[c0 + (wv & 3)]);
    __syncthreads();
    const int cl = wv & 3, kh = (wv >> 2) & 1;
    f32x4 acc[8], base[4];
    float ksc = 0.f;
    if (wv < 8) {
#pragma unroll
      for (int t = 0; t < 8; ++t) acc[t] = f32x4{0, 0, 0, 0};
      const int bb = lane & 15, h = lane >> 4;
      const int cpy = (bb & 1) ^ 1;
      LAS unsigned char* abase = lds + cl * CV_CH + cpy * CV_KC + (2047 - 16 - bb + 8 * h - cpy) * 2 - 64 * 4 * kh;
      LAS unsigned char* bbase = lds + CV_U + (32 + 2 * (bb - 1) + h) * 128;
      v4u A[8], B[8];
#define LDA(i, rho) do { LAS unsigned* pa_ = (LAS unsigned*)(abase - 64 * ((rho) + 8 * (i))); A[i] = v4u{pa_[0], pa_[1], pa_[2], pa_[3]}; } while (0)
#define LDB(m, rho) B[m] = *(LAS v4u*)(bbase - 512 * (4 * kh + (rho)) + ((cl ^ ((bb - 1 + (h >> 1) - 2 * (4 * kh + (rho))) & 7)) << 4) + 4096 * (m))
#define LDBX(dst, m, rho) dst = *(LAS v4u*)(bbase - 512 * (4 * kh + (rho)) + ((cl ^ ((bb - 1 + (h >> 1) - 2 * (4 * kh + (rho))) & 7)) << 4) + 4096 * (m))
      v4u Bl[2][3];
#pragma unroll
      for (int i = 0; i < 8; ++i) { if (i < 3) LDBX(Bl[0][i], i, 0); else LDB(i, 0); LDA(i, 0); }
#pragma unroll
      for (int rho = 0; rho < 4; ++rho) {
#pragma unroll
        for (int i = 0; i < 8; ++i) {
#pragma unroll
          for (int m = 7 - i; m >= 0; --m) acc[i + m] = mfma_h(A[i], m < 3 ? Bl[rho & 1][m] : B[m], acc[i + m]);
          if (rho < 3) {
            LDA(i, rho + 1);
            if (i < 5) LDB(7 - i, rho + 1);
            if (i < 3) LDBX(Bl[(rho + 1) & 1][i], i, rho + 1);
          }
          __builtin_amdgcn_sched_barrier(0);
        }
      }
#undef LDBX
#undef LDA
#undef LDB
      LAS unsigned char* xo = lds + cl * CV_CH + CV_XCH + kh * 4096 + lane * 16;
#pragma unroll
      for (int t = 0; t < 4; ++t) {
        f32x4 lo = acc[t], hi = acc[4 + t];
        asm volatile("" : "+v"(lo), "+v"(hi));
        *(LAS f32x4*)(xo + t * 1024) = kh ? lo : hi;
        acc[t] = kh ? hi : lo;
      }
      const int a = lane & 15, q = lane >> 4;
#pragma unroll
      for (int t = 0; t < 4; ++t) {
        LAS unsigned char* po = lds + CV_U + (32 + 32 * (4 * kh + t) + 2 * a + (q >> 1)) * 128 + (q & 1) * 8;
        const v2u uu = *(LAS v2u*)(po + ((cl ^ (a & 7)) << 4));
        const v2u xx = *(LAS v2u*)(po + (((4 + cl) ^ (a & 7)) << 4));
#pragma unroll
        for (int e = 0; e < 2; ++e) {
          const unsigned uw = e ? uu.y : uu.x, xw = e ? xx.y : xx.x;
          const h16x2 u2 = __builtin_bit_cast(h16x2, uw);
          const f32x2 bv = __builtin_elementwise_fma(f32x2{(float)u2[0], (float)u2[1]}, f32x2{dsk, dsk}, f32x2{bflo(xw), bfhi(xw)});
          base[t][2 * e] = bv[0]; base[t][2 * e + 1] = bv[1];
        }
      }
      ksc = kscs[cl];
#pragma unroll
      for (int t = 0; t < 4; ++t) base[t] += acc[t] * ksc;
    }
    __syncthreads();
    if (wv < 8) {
      const int ch = c0 + cl;
      const int a = lane & 15, q = lane >> 4;
      LAS unsigned char* xi = lds + cl * CV_CH + CV_XCH + (kh ^ 1) * 4096 + lane * 16;
      f32x4 o[4];
#pragma unroll
      for (int t = 0; t < 4; ++t) o[t] = *(LAS f32x4*)(xi + t * 1024);
      unsigned pk[4][2];
#pragma unroll
      for (int t = 0; t < 4; ++t) {
        const f32x4 hv = base[t] + o[t] * ksc;
        pk[t][0] = cvtpk(hv[0], hv[1]);
        pk[t][1] = cvtpk(hv[2], hv[3]);
      }
#pragma unroll
      for (int t = 0; t < 4; t += 2) {
        auto s0 = __builtin_amdgcn_permlane16_swap(pk[t][0], pk[t + 1][0], false, false);
        auto s1 = __builtin_amdgcn_permlane16_swap(pk[t][1], pk[t + 1][1], false, false);
        const int tok = 256 * (4 * kh + t + (q & 1)) + 16 * a + 8 * (q >> 1);
        st16(rs, v4u{s0[0], s1[0], s0[1], s1[1]}, WS_HT + (ch * L + tok) * 2);
      }
      if (drain_arrive_last(cnt + 8, 8, lane)) { if (lane < 8) __hip_atomic_store((gu64*)(ws + WS_F2) + lane * 256 + (18 * xcd + jj), tag, RLX); }
    }
  }

  const int tt = 4 * xcd + jj / 6, sl = jj % 6;
  if (is_gemm) {
    const int cl = wv % 6, kh = wv / 6, ct = 6 * sl + cl;
    const int tok = lane & 15, q = lane >> 4;
    if (wv == 0) {
      const int u0 = (96 * sl) >> 6;
      poll_tag((gu64*)(ws + WS_FPT + (b & 3) * 2048) + (lane & 1) + u0 + ((lane & 2) ? 9 : 0), tag);
    }
    __syncthreads();
    v4u bw[9];
    if (wv != 0) {
#pragma unroll
      for (int ks = 0; ks < 9; ++ks) bw[ks] = ld16(rs, lane * 16, WS_W1T + (ct * 18 + 9 * kh + ks) * 1024);
    }
    int aoff[6];
#pragma unroll
    for (int i = 0; i < 6; ++i) {
      const int p = tid + NT * i, c = p >> 3, pp = p & 7;
      const int f = ((c >> 1) & 1) | (((c >> 3) & 1) << 1);
      aoff[i] = WS_HT + (c * L + tt * 64 + ((((pp >> 1) ^ f) << 1) | (pp & 1)) * 8) * 2;
      asm volatile("" : "+v"(aoff[i]));
    }
    if (wv == 0) {
      const int fo = WS_F2 + (b & 7) * 2048 + (lane < 36 ? lane : 0) * 32;
      const unsigned tl = (unsigned)tag, th = (unsigned)(tag >> 32);
      for (unsigned spins = 0; spins < SPIN_CAP; ++spins) {
        asm volatile("" ::: "memory");
        const v4u f0 = ld16(rs, fo), f1 = ld16(rs, fo + 16);
        if (__all(f0.x == tl && f0.y == th && f0.z == tl && f0.w == th && f1.x == tl && f1.y == th && f1.z == tl && f1.w == th)) break;
        __builtin_amdgcn_s_sleep(PSLEEP);
      }
      ORDER();
#pragma unroll
      for (int ks = 0; ks < 9; ++ks) bw[ks] = ld16(rs, lane * 16, WS_W1T + (ct * 18 + 9 * kh + ks) * 1024);
    }
    const f32x4 cs = __builtin_bit_cast(f32x4, ld16(rs, WS_CS1 + (ct * 16 + 4 * q) * 4));
    __syncthreads();
#pragma unroll
    for (int i = 0; i < 6; ++i) dma16(rs, lds + G_A + (wv * 64 + NT * i) * 16, aoff[i]);
    __syncthreads();
    v4u bw2[9];
    v4u hpiece;
    f32x4 acc[4];
#pragma unroll
    for (int r = 0; r < 4; ++r) acc[r] = f32x4{0, 0, 0, 0};
    {
      const int pq = (lane & 15) >> 2, pp = lane & 3;
      const int f = ((pq >> 1) & 1) | ((q & 1) << 1);
      const unsigned ones = 0x3F803F80u;
      LAS unsigned char* pa0 = lds + G_A + (8 * q + pq) * 128 + pp * 8;
      LAS unsigned char* par[4];
#pragma unroll
      for (int r = 0; r < 4; ++r) par[r] = pa0 + ((r ^ f) << 5);
      v4u af[2][4];
#pragma unroll
      for (int r = 0; r < 4; ++r) { v2u lo = tr_read(par[r] + 9 * kh * 4096), hi = tr_read(par[r] + 9 * kh * 4096 + 512); af[0][r] = v4u{lo.x, lo.y, hi.x, hi.y}; }
#pragma unroll
      for (int ks = 0; ks < 9; ++ks) {
        if (ks < 8) {
#pragma unroll
          for (int r = 0; r < 4; ++r) {
            v2u lo = tr_read(par[r] + (9 * kh + ks + 1) * 4096), hi = tr_read(par[r] + (9 * kh + ks + 1) * 4096 + 512);
            af[(ks + 1) & 1][r] = v4u{lo.x, lo.y, hi.x, hi.y};
          }
        }
        __builtin_amdgcn_sched_barrier(0);
#pragma unroll
        for (int r = 0; r < 4; ++r) acc[r] = mfma_bf(bw[ks], af[ks & 1][r], acc[r]);
        __builtin_amdgcn_sched_barrier(0);
      }
    }
#pragma unroll
    for (int ks = 0; ks < 9; ++ks) bw2[ks] = ld16(rs, lane * 16, WS_W2T + (ct * 18 + 9 * kh + ks) * 1024);
    hpiece = ld16(rs, WS_HT + ((ct * 16 + (lane >> 2)) * L + tt * 64 + kh * 32 + (lane & 3) * 8) * 2);
    __builtin_amdgcn_sched_barrier(0);
    {
      LAS unsigned char* xo = lds + G_X + wv * 2048 + lane * 16;
#pragma unroll
      for (int r = 0; r < 2; ++r) {
        f32x4 lo = acc[r], hi = acc[2 + r];
        asm volatile("" : "+v"(lo), "+v"(hi));
        *(LAS f32x4*)(xo + r * 1024) = kh ? lo : hi;
        acc[r] = kh ? hi : lo;
      }
    }
    if (wv < 4) {
      const int pq = (lane & 15) >> 2, pp = lane & 3;
      const int f = ((pq >> 1) & 1) | ((q & 1) << 1);
      LAS unsigned char* ps0 = lds + G_A + (8 * q + pq) * 128 + pp * 8 + ((wv ^ f) << 5);
      f32x4 dsum = {0, 0, 0, 0}, dsq = {0, 0, 0, 0};
      const v4u onesv = {0x3F803F80u, 0x3F803F80u, 0x3F803F80u, 0x3F803F80u};
#pragma unroll
      for (int hf = 0; hf < 2; ++hf) {
        v4u as[9];
#pragma unroll
        for (int i = 0; i < 9; ++i) { v2u lo = tr_read(ps0 + (9 * hf + i) * 4096), hi = tr_read(ps0 + (9 * hf + i) * 4096 + 512); as[i] = v4u{lo.x, lo.y, hi.x, hi.y}; }
#pragma unroll
        for (int i = 0; i < 9; ++i) { dsum = mfma_bf(onesv, as[i], dsum); dsq = mfma_bf(as[i], as[i], dsq); }
      }
      const int e = tok & 3;
      const float sq = e == 0 ? dsq[0] : (e == 1 ? dsq[1] : (e == 2 ? dsq[2] : dsq[3]));
      const float s_ = dsum[0], ss = sq;
      float m1, s1;
      ln_scalars(s_, ss, m1, s1);
      const float mean = s_ * (1.0f / D);
      float var = __builtin_fmaf(-mean, mean, ss * (1.0f / D));
      var = var < 0.f ? 0.f : var;
      const float r1 = __builtin_amdgcn_rcpf(s1);
      const float m2 = bfr((mean - m1) * r1);
      const float s2 = bfr(__builtin_amdgcn_sqrtf(bfr(bfr(var * r1 * r1) + 1e-5f)));
      if (q == (tok >> 2)) *(LAS f32x4*)(lds + G_ST + 2048 + (wv * 16 + tok) * 16) = f32x4{__builtin_fmaf(m2, s1, m1), r1 * __builtin_amdgcn_rcpf(s2), m1, r1};
    }
    __syncthreads();
    f32x2 st1[2];
    {
      LAS unsigned char* xi = lds + G_X + (kh ? wv - 6 : wv + 6) * 2048 + lane * 16;
      f32x4 o[2], sc[2];
#pragma unroll
      for (int r = 0; r < 2; ++r) { o[r] = *(LAS f32x4*)(xi + r * 1024); sc[r] = *(LAS f32x4*)(lds + G_ST + 2048 + ((2 * kh + r) * 16 + tok) * 16); }
      __builtin_amdgcn_sched_barrier(0);
      unsigned pk[2][2];
#pragma unroll
      for (int r = 0; r < 2; ++r) {
        st1[r] = f32x2{sc[r][2], sc[r][3]};
        const f32x4 v = (acc[r] + o[r] - cs * sc[r][0]) * sc[r][1];
        const f32x2 ga = gelu2(f32x2{v[0], v[1]}), gb = gelu2(f32x2{v[2], v[3]});
        pk[r][0] = cvtpk(ga[0], ga[1]);
        pk[r][1] = cvtpk(gb[0], gb[1]);
      }
#pragma unroll
      for (int r = 0; r < 2; ++r) *(LAS v2u*)(lds + G_O + ((2 * kh + r) * 16 + tok) * G_OROW + cl * 32 + q * 8) = v2u{pk[r][0], pk[r][1]};
    }
    const int orow = tid / 12, ocol = tid % 12;
    __syncthreads();
    st16(rs, *(LAS v4u*)(lds + G_O + orow * G_OROW + ocol * 16), WS_H1 + ((tt * 64 + orow) * D + sl * 96 + ocol * 8) * 2);
    if (drain_arrive_last(cnt + 9, 12, lane)) { if (lane == 0) __hip_atomic_store((gu64*)(ws + WS_F3) + tt * 8 + sl, tag, RLX); }

    {
      const int c = lane >> 2, pc = lane & 3;
      *(LAS v4u*)(lds + G_H + wv * 1024 + c * 64 + ((((pc >> 1) ^ ((c >> 3) & 1)) << 1 | (pc & 1)) << 4)) = hpiece;
    }
#pragma unroll
    for (int i = 0; i < 6; ++i) {
      const int p = tid + NT * i, r = p / 72, c = (p % 72) ^ ((r >> 1) & 7);
      const int cg = c / 18;
      const int sh = cg == 0 ? -1 : (cg == 1 ? 1 : (cg == 2 ? -2 : 2));
      const int t = tt * 64 + r + sh;
      aoff[i] = (t >= 0 && t < L) ? WS_H1 + (t * D + c * 8) * 2 : WS_END;
      asm volatile("" : "+v"(aoff[i]));
    }
    if (wv == 0) {
      int t = tt - 1 + (lane < 18 ? lane / 6 : 1);
      t = t < 0 ? 0 : (t > 31 ? 31 : t);
      poll_tag((gu64*)(ws + WS_F3) + t * 8 + (lane < 18 ? lane % 6 : 0), tag);
    }
    __syncthreads();
#pragma unroll
    for (int i = 0; i < 6; ++i) dma16(rs, lds + G_A + (wv * 64 + NT * i) * 16, aoff[i]);
    __syncthreads();
#pragma unroll
    for (int r = 0; r < 4; ++r) acc[r] = f32x4{0, 0, 0, 0};
    {
      const int sw = (tok >> 1) & 7;
      LAS unsigned char* pa = lds + G_A + tok * ROWB;
      v4u af[2][4];
#pragma unroll
      for (int r = 0; r < 4; ++r) af[0][r] = *(LAS v4u*)(pa + r * 16 * ROWB + (((4 * 9 * kh + q) ^ sw) << 4));
#pragma unroll
      for (int ks = 0; ks < 9; ++ks) {
        if (ks < 8) {
#pragma unroll
          for (int r = 0; r < 4; ++r) af[(ks + 1) & 1][r] = *(LAS v4u*)(pa + r * 16 * ROWB + (((4 * (9 * kh + ks + 1) + q) ^ sw) << 4));
        }
        __builtin_amdgcn_sched_barrier(0);
#pragma unroll
        for (int r = 0; r < 4; ++r) acc[r] = mfma_bf(bw2[ks], af[ks & 1][r], acc[r]);
        __builtin_amdgcn_sched_barrier(0);
      }
    }
    v2u hh[2];
    {
      LAS unsigned char* xo = lds + G_X + wv * 2048 + lane * 16;
#pragma unroll
      for (int r = 0; r < 2; ++r) {
        f32x4 lo = acc[r], hi = acc[2 + r];
        asm volatile("" : "+v"(lo), "+v"(hi));
        *(LAS f32x4*)(xo + r * 1024) = kh ? lo : hi;
        acc[r] = kh ? hi : lo;
      }
#pragma unroll
      for (int r = 0; r < 2; ++r) {
        const int c = 4 * q + ((lane & 15) >> 2);
        hh[r] = tr_read(lds + G_H + wv * 1024 + c * 64 + ((r ^ ((c >> 3) & 1)) << 5) + (lane & 3) * 8);
        const float m1 = st1[r][0], r1 = st1[r][1];
        const f32x4 hv = {bflo(hh[r].x), bfhi(hh[r].x), bflo(hh[r].y), bfhi(hh[r].y)};
        acc[r] += (hv - m1) * r1;
        *(LAS v2u*)(lds + G_O + 64 * G_OROW + ((2 * kh + r) * 16 + tok) * G_OROW + cl * 32 + q * 8) = hh[r];
      }
    }
    __syncthreads();
    {
      LAS unsigned char* xi = lds + G_X + (kh ? wv - 6 : wv + 6) * 2048 + lane * 16;
      f32x4 o[2];
#pragma unroll
      for (int r = 0; r < 2; ++r) o[r] = *(LAS f32x4*)(xi + r * 1024);
      unsigned pk[2][2];
#pragma unroll
      for (int r = 0; r < 2; ++r) {
        const f32x4 v = acc[r] + o[r];
        pk[r][0] = cvtpk(v[0], v[1]);
        pk[r][1] = cvtpk(v[2], v[3]);
      }
#pragma unroll
      for (int r = 0; r < 2; ++r) *(LAS v2u*)(lds + G_O + ((2 * kh + r) * 16 + tok) * G_OROW + cl * 32 + q * 8) = v2u{pk[r][0], pk[r][1]};
    }
    __syncthreads();
    {
      const int off = ((tt * 64 + orow) * D + sl * 96 + ocol * 8) * 2;
      st16(rs, *(LAS v4u*)(lds + G_O + orow * G_OROW + ocol * 16), WS_X1 + off);
      st16(rs, *(LAS v4u*)(lds + G_O + 64 * G_OROW + orow * G_OROW + ocol * 16), WS_HR + off);
    }
    if (drain_arrive_last(cnt + 10, 12, lane)) { if (lane == 0) __hip_atomic_store((gu64*)(ws + WS_F4) + tt * 8 + sl, tag, RLX); }
  }

  const int grp = 2 * (jj >> 1) + (xcd >> 2), s = 2 * (xcd & 3) + (jj & 1);
  {
    constexpr int DEPTH = 3;
    if (wv == 0) {
      poll_tag((gu64*)(ws + WS_FPT + (b & 3) * 2048) + (lane < 9 ? 18 + 9 * s + lane : (lane < 18 ? 90 + 9 * s + lane - 9 : 18 + 9 * s)), tag);
    }
    __syncthreads();
    const int o3 = WS_W3T + (36 * s + 3 * wv) * 18 * 1024;
    LAS unsigned* mcnt = (LAS unsigned*)(lds + ML_ST + 512);
    v4u bw[DEPTH][3];
    const v4u csl = ld16(rs, WS_CS3 + 36 * 16 * 4 * s + (tid < 144 ? tid : 0) * 16);
    if (wv != 0) {
#pragma unroll
      for (int d = 0; d < DEPTH; ++d)
#pragma unroll
        for (int c = 0; c < 3; ++c) bw[d][c] = ld16(rs, lane * 16, o3 + (c * 18 + d) * 1024);
    }
    if (tid < 144) *(LAS v4u*)(lds + ML_CS + tid * 16) = csl;
    if (tid >= 704 && tid < 714) mcnt[tid - 704] = 0;
    int xoff[6];
#pragma unroll
    for (int i = 0; i < 6; ++i) {
      const int p = tid + NT * i, r = p / 72, c = (p % 72) ^ ((r >> 1) & 7);
      xoff[i] = WS_X1 + grp * 64 * ROWB + r * ROWB + c * 16;
      asm volatile("" : "+v"(xoff[i]));
    }
    if (wv == 0) {
      poll_tag((gu64*)(ws + WS_F4) + grp * 8 + (lane < 6 ? lane : 0), tag);
#pragma unroll
      for (int d = 0; d < DEPTH; ++d)
#pragma unroll
        for (int c = 0; c < 3; ++c) bw[d][c] = ld16(rs, lane * 16, o3 + (c * 18 + d) * 1024);
    }
    __syncthreads();
#pragma unroll
    for (int i = 0; i < 6; ++i) dma16(rs, lds + ML_A + (wv * 64 + NT * i) * 16, xoff[i]);
    __syncthreads();
    const int tok = lane & 15, q = lane >> 4;
    const int sw = (tok >> 1) & 7;
    const int wg = wv >> 2, wi = wv & 3;
    LAS unsigned char* abase = lds + ML_A + tok * ROWB;
    if (wv < 4) {
      f32x4 dsum = {0, 0, 0, 0}, dsq = {0, 0, 0, 0};
      const v4u onesv = {0x3F803F80u, 0x3F803F80u, 0x3F803F80u, 0x3F803F80u};
      LAS unsigned char* pst = abase + wv * 16 * ROWB;
      v4u as[18];
#pragma unroll
      for (int ks = 0; ks < 18; ++ks) as[ks] = *(LAS v4u*)(pst + (((4 * ks + q) ^ sw) << 4));
#pragma unroll
      for (int ks = 0; ks < 18; ++ks) { dsum = mfma_bf(onesv, as[ks], dsum); dsq = mfma_bf(as[ks], as[ks], dsq); }
      const int e = tok & 3;
      const float sq = e == 0 ? dsq[0] : (e == 1 ? dsq[1] : (e == 2 ? dsq[2] : dsq[3]));
      float m, sd;
      ln_scalars(dsum[0], sq, m, sd);
      if (q == (tok >> 2)) *(LAS f32x2*)(lds + ML_ST + (wv * 16 + tok) * 8) = f32x2{m, __builtin_amdgcn_rcpf(sd)};
      lds_arrive(mcnt, lane);
    }
    LAS unsigned char* H = lds + ML_H;
    {
      f32x4 acc[4][3];
#pragma unroll
      for (int r = 0; r < 4; ++r)
#pragma unroll
        for (int c = 0; c < 3; ++c) acc[r][c] = f32x4{0, 0, 0, 0};
      __builtin_amdgcn_s_setprio(2);
      v4u af[2][4];
#pragma unroll
      for (int r = 0; r < 4; ++r) af[0][r] = *(LAS v4u*)(abase + r * 16 * ROWB + ((q ^ sw) << 4));
#pragma unroll
      for (int ks = 0; ks < 18; ++ks) {
        if (ks < 17) {
#pragma unroll
          for (int r = 0; r < 4; ++r) af[(ks + 1) & 1][r] = *(LAS v4u*)(abase + r * 16 * ROWB + (((4 * (ks + 1) + q) ^ sw) << 4));
        }
        __builtin_amdgcn_sched_barrier(0);
#pragma unroll
        for (int c = 0; c < 3; ++c) {
#pragma unroll
          for (int r = 0; r < 4; ++r) acc[r][c] = mfma_bf(bw[ks % DEPTH][c], af[ks & 1][r], acc[r][c]);
          const int nk = ks + DEPTH;
          if (nk < 18) bw[ks % DEPTH][c] = ld16(rs, lane * 16, o3 + (c * 18 + nk) * 1024);
        }
        __builtin_amdgcn_sched_barrier(0);
      }
      __builtin_amdgcn_s_setprio(3);
      lds_wait(mcnt, 4);
      f32x2 st[4];
#pragma unroll
      for (int r = 0; r < 4; ++r) st[r] = *(LAS f32x2*)(lds + ML_ST + (r * 16 + tok) * 8);
#pragma unroll
      for (int c = 0; c < 3; ++c) {
        const f32x4 cs = *(LAS f32x4*)(lds + ML_CS + ((3 * wv + c) * 16 + 4 * q) * 4);
#pragma unroll
        for (int r = 0; r < 4; ++r) {
          const f32x2 nm = {-st[r].x, -st[r].x}, rr = {st[r].y, st[r].y};
          const f32x2 ga = gelu2(__builtin_elementwise_fma(nm, f32x2{cs[0], cs[1]}, f32x2{acc[r][c][0], acc[r][c][1]}) * rr);
          const f32x2 gb = gelu2(__builtin_elementwise_fma(nm, f32x2{cs[2], cs[3]}, f32x2{acc[r][c][2], acc[r][c][3]}) * rr);
          v2u o = {cvtpk(ga[0], ga[1]), cvtpk(gb[0], gb[1])};
          const int chunk = 2 * (3 * wv + c) + (q >> 1);
          *(LAS v2u*)(H + (r * 16 + tok) * ROWB + ((chunk ^ sw) << 4) + (q & 1) * 8) = o;
        }
      }
      lds_arrive(mcnt + 1 + wg, lane);
      if (wg == 0) __builtin_amdgcn_s_setprio(0); else if (wg == 1) __builtin_amdgcn_s_setprio(1); else __builtin_amdgcn_s_setprio(2);
    }
    if (wg == 0) mlp_second<NC0>(lds, rs, ws, out, tag, grp, s, NC0 * wi, wi, 0, mcnt, lane, tok, q, sw);
    else if (wg == 1) mlp_second<NC1>(lds, rs, ws, out, tag, grp, s, 4 * NC0 + NC1 * wi, wi, 1, mcnt, lane, tok, q, sw);
    else mlp_second<NC2>(lds, rs, ws, out, tag, grp, s, 4 * (NC0 + NC1) + NC2 * wi, wi, 2, mcnt, lane, tok, q, sw);
  }
  if (b == 0 && tid == 0) __hip_atomic_store((gu64*)(ws + WS_CTRL), tag, RLX);
}

extern "C" void kernel_launch(void* const* d_in, const int* in_sizes, int n_in, void* d_out, int out_size, void* d_ws, size_t ws_size,
                              hipStream_t stream) {
  (void)hipFuncSetAttribute((const void*)k_all, hipFuncAttributeMaxDynamicSharedMemorySize, LDS_BYTES);
  const unsigned short* const* in = (const unsigned short* const*)d_in;
  k_all<<<256, NT, LDS_BYTES, stream>>>(in[0], in[1], in[2], in[3], in[4], in[5], in[6], in[7], in[8], in[10], in[11], in[12], in[13], (unsigned char*)d_ws,
                                        (unsigned short*)d_out);
}
```
